```python
import torch

def _softmax(x, axis=-1):
    return torch.softmax(x, dim=axis)
def _gelu(x):
    return torch.nn.functional.gelu(x, approximate="tanh")
def _silu(x):
    return torch.nn.functional.silu(x)
def _sigmoid(x):
    return torch.sigmoid(x)
def _layernorm(x, axis=-1, eps=1e-5):
    m = x.mean(dim=axis, keepdim=True)
    v = x.var(dim=axis, keepdim=True, unbiased=False)
    return (x - m) / torch.sqrt(v + eps)
def _scatter_add(tgt, idx, src, axis=0):
    return tgt.clone().index_add_(axis, idx, src)
def _segment_sum(src, seg, n):
    out = torch.zeros((n,) + src.shape[1:], dtype=src.dtype)
    return out.index_add_(0, seg, src)
def _pad(x, pw):
    flat = tuple(v for pair in reversed(pw) for v in pair)
    return torch.nn.functional.pad(x, flat)

kernel_name = 'standalone__linear_kernel_hedgehog__prompt'

def setup_inputs():

    _w = (lambda a: (lambda t: t.to(torch.bfloat16) if t.is_floating_point() else t)(torch.as_tensor(a)))
    B, S, D, H = 1, 2048, 1024, 32
    Dh = D // H
    return {
        "x": _w((lambda *s: torch.randn(*s, dtype=torch.bfloat16) * 0.4)(B, S, D)),
        "wqkv": _w((lambda *s: torch.randn(*s, dtype=torch.bfloat16) * 0.4)(D, 3 * D)),
        "w_phi": _w((lambda *s: torch.randn(*s, dtype=torch.bfloat16) * 0.4)(H, Dh, Dh) * (Dh ** -0.5)),
        "wo": _w((lambda *s: torch.randn(*s, dtype=torch.bfloat16) * 0.05)(D, D)),
    }

def reference(x, wqkv, w_phi, wo):
    B, S, D = x.shape
    H = 32; Dh = D // H
    qkv = (lambda x, *d: x.permute(*d))((x @ wqkv).reshape(B, S, 3, H, Dh), 2, 0, 3, 1, 4)
    q, k, v = qkv[0], qkv[1], qkv[2]
    pq = torch.einsum("bhsd,hdf->bhsf", q, w_phi)
    pk = torch.einsum("bhsd,hdf->bhsf", k, w_phi)
    phi_q = torch.concatenate([_softmax(pq, axis=-1), _softmax(-pq, axis=-1)], axis=-1)
    phi_k = torch.concatenate([_softmax(pk, axis=-1), _softmax(-pk, axis=-1)], axis=-1)
    kv = torch.einsum("bhsf,bhsd->bhfd", phi_k, v)
    z  = torch.einsum("bhsf->bhf", phi_k)
    num = torch.einsum("bhqf,bhfd->bhqd", phi_q, kv)
    den = torch.einsum("bhqf,bhf->bhq", phi_q, z)[..., None] + 1e-6
    o = num / den
    return (lambda x, *d: x.permute(*d))(o, 0, 2, 1, 3).reshape(B, S, D) @ wo
```

```cpp
#include <hip/hip_runtime.h>
#include <hip/hip_bf16.h>
typedef unsigned short u16;
__device__ __forceinline__ float bf2f(u16 v){ return __uint_as_float(((unsigned)v)<<16); }
__device__ __forceinline__ u16 f2bf(float f){ unsigned u=__float_as_uint(f); u += 0x7fffu + ((u>>16)&1u); return (u16)(u>>16); }
__device__ __forceinline__ float rbf(float f){ return bf2f(f2bf(f)); }
constexpr int S=2048, D=1024, H=32, DH=32;
__global__ void gemm_naive(const u16* A, const u16* B, u16* C, int M, int N, int K){
  __shared__ float As[32][33]; __shared__ float Bs[32][33];
  int tx=threadIdx.x&31, ty=threadIdx.x>>5;
  int bm=blockIdx.y*32, bn=blockIdx.x*32;
  float acc[4]={0,0,0,0};
  for(int k0=0;k0<K;k0+=32){
    for(int i=0;i<4;i++){ As[ty+8*i][tx]=bf2f(A[(size_t)(bm+ty+8*i)*K+k0+tx]); Bs[ty+8*i][tx]=bf2f(B[(size_t)(k0+ty+8*i)*N+bn+tx]); }
    __syncthreads();
    for(int k=0;k<32;k++){ float b=Bs[k][tx]; for(int i=0;i<4;i++) acc[i]+=As[ty+8*i][k]*b; }
    __syncthreads();
  }
  for(int i=0;i<4;i++) C[(size_t)(bm+ty+8*i)*N+bn+tx]=f2bf(acc[i]);
}
__global__ void phi_kernel(const u16* qkv, const u16* wphi, u16* phi  ){
  int idx=blockIdx.x*blockDim.x+threadIdx.x;
  int f=idx&31; int r=idx>>5; int h=r%H; int s=(r/H)%S; int w=r/(H*S);
  const u16* src=qkv+(size_t)s*3*D+w*D+h*DH;
  float p=0; for(int d=0;d<32;d++) p+=bf2f(src[d])*bf2f(wphi[h*1024+d*32+f]);
  p=rbf(p);
  float mx=p, mn=p;
  for(int o=1;o<32;o<<=1){ mx=fmaxf(mx,__shfl_xor(mx,o)); mn=fminf(mn,__shfl_xor(mn,o)); }
  float e1=__expf(p-mx), e2=__expf(mn-p);
  float s1=e1,s2=e2;
  for(int o=1;o<32;o<<=1){ s1+=__shfl_xor(s1,o); s2+=__shfl_xor(s2,o); }
  u16* dst=phi+(((size_t)w*H+h)*S+s)*64;
  dst[f]=f2bf(e1/s1); dst[32+f]=f2bf(e2/s2);
}
__global__ void kv_kernel(const u16* qkv, const u16* phi, float* kv  ){
  int h=blockIdx.x/64, f=blockIdx.x%64; int d=threadIdx.x;
  const u16* pk=phi+((size_t)(H+h)*S)*64;
  float acc=0;
  if(d<33){ for(int s=0;s<S;s++){ float p=bf2f(pk[(size_t)s*64+f]); float v= d<32? bf2f(qkv[(size_t)s*3*D+2*D+h*DH+d]) : 1.f; acc+=p*v; }
    kv[(h*64+f)*33+d]=rbf(acc); }
}
__global__ void o_kernel(const u16* phi, const float* kv, u16* o){
  int idx=blockIdx.x*blockDim.x+threadIdx.x;
  int d=idx&31, h=(idx>>5)&31, s=idx>>10;
  const u16* pq=phi+((size_t)h*S+s)*64;
  float num=0, den=0;
  for(int f=0;f<64;f++){ float p=bf2f(pq[f]); num+=p*kv[(h*64+f)*33+d]; den+=p*kv[(h*64+f)*33+32]; }
  num=rbf(num); den=rbf(rbf(den)+1e-6f);
  o[idx]=f2bf(num/den);
}
extern "C" void kernel_launch(void* const* d_in, const int* in_sizes, int n_in,
                              void* d_out, int out_size, void* d_ws, size_t ws_size,
                              hipStream_t stream) {
  const u16* x=(const u16*)d_in[0]; const u16* wqkv=(const u16*)d_in[1]; const u16* wphi=(const u16*)d_in[2]; const u16* wo=(const u16*)d_in[3];
  char* ws=(char*)d_ws;
  u16* qkv=(u16*)ws; u16* phi=(u16*)(ws+(16<<20)); float* kv=(float*)(ws+(40<<20)); u16* o=(u16*)(ws+(48<<20));
  gemm_naive<<<dim3(3*D/32,S/32),256,0,stream>>>(x,wqkv,qkv,S,3*D,D);
  phi_kernel<<<2*S*H*32/256,256,0,stream>>>(qkv,wphi,phi);
  kv_kernel<<<H*64,64,0,stream>>>(qkv,phi,kv);
  o_kernel<<<S*D/256,256,0,stream>>>(phi,kv,o);
  gemm_naive<<<dim3(D/32,S/32),256,0,stream>>>(o,wo,(u16*)d_out,S,D,D);
}
```

```cpp
#include <hip/hip_runtime.h>
#include <hip/hip_bf16.h>

typedef unsigned short u16;
typedef unsigned long long u64;
typedef __attribute__((ext_vector_type(8))) __bf16 bf16x8;
typedef __attribute__((ext_vector_type(4))) __bf16 bf16x4;
typedef __attribute__((ext_vector_type(2))) __bf16 bf16x2;
typedef __attribute__((ext_vector_type(16))) float f32x16;
typedef __attribute__((ext_vector_type(4))) float f32x4;
typedef __attribute__((ext_vector_type(2))) float f32x2;
typedef __attribute__((ext_vector_type(4))) unsigned u32x4;
typedef __attribute__((ext_vector_type(2))) unsigned u32x2;
#define LAS __attribute__((address_space(3)))
#define DI __device__ __forceinline__
#define MFMA(a, b, c) __builtin_amdgcn_mfma_f32_32x32x16_bf16((a), (b), (c), 0, 0, 0)
#define SC1 16

#define ROT 1
#define OMAP 1

constexpr int SLOT = 40960;
constexpr int L_WO = 8192;
constexpr int L_SA = 139264;
constexpr int L_XZ = L_SA + 16384;
constexpr int L_Z = L_XZ + 1024;
constexpr int L_CNT = L_Z + 256;
constexpr int L_PHI = L_SA + 18432;
constexpr int L_KV = 0;
constexpr int LDS_BYTES = 163840;
constexpr unsigned W_O = 0;
constexpr unsigned W_P = 4u << 20;
constexpr unsigned W_PZ = 8u << 20;
constexpr unsigned W_R = W_PZ + (128u << 10);
constexpr unsigned W_F = W_R + (256u << 10);
constexpr unsigned W_E = W_F + (64u << 10);
constexpr unsigned W_TS = W_E + 4096;
constexpr unsigned W_BYTES = 10u << 20;
constexpr int SPIN_CAP = 1 << 18;

DI void bar() { asm volatile("s_waitcnt lgkmcnt(0)\n\ts_barrier" ::: "memory"); }
template <int N> DI void waitv() { asm volatile("s_waitcnt vmcnt(%0)" ::"i"(N) : "memory"); }
DI unsigned pk2(float a, float b) { f32x2 v = {a, b}; return __builtin_bit_cast(unsigned, __builtin_convertvector(v, bf16x2)); }
DI bf16x8 pack8(const f32x16& x, int s) {
  u32x4 p = {pk2(x[8 * s], x[8 * s + 1]), pk2(x[8 * s + 2], x[8 * s + 3]), pk2(x[8 * s + 4], x[8 * s + 5]), pk2(x[8 * s + 6], x[8 * s + 7])};
  return __builtin_bit_cast(bf16x8, p);
}
DI float xh_max(float a) { auto r = __builtin_amdgcn_permlane32_swap(__float_as_uint(a), __float_as_uint(a), false, false); return fmaxf(__uint_as_float(r[0]), __uint_as_float(r[1])); }
DI float xh_min(float a) { auto r = __builtin_amdgcn_permlane32_swap(__float_as_uint(a), __float_as_uint(a), false, false); return fminf(__uint_as_float(r[0]), __uint_as_float(r[1])); }
DI float xh_add(float a) { auto r = __builtin_amdgcn_permlane32_swap(__float_as_uint(a), __float_as_uint(a), false, false); return __uint_as_float(r[0]) + __uint_as_float(r[1]); }
DI float x16_add(float a) { auto r = __builtin_amdgcn_permlane16_swap(__float_as_uint(a), __float_as_uint(a), false, false); return __uint_as_float(r[0]) + __uint_as_float(r[1]); }
DI bf16x8 lds_b128(unsigned a) { return *(const LAS bf16x8*)a; }
DI bf16x8 lds_tr2(unsigned a0, unsigned a1) {
  bf16x4 lo = __builtin_amdgcn_ds_read_tr16_b64_v4bf16((LAS bf16x4*)a0);
  bf16x4 hi = __builtin_amdgcn_ds_read_tr16_b64_v4bf16((LAS bf16x4*)a1);
  return __builtin_shufflevector(lo, hi, 0, 1, 2, 3, 4, 5, 6, 7);
}
DI void dual_softmax(const f32x16& p, bf16x8 (&pos)[2], bf16x8 (&neg)[2]) {
  float mx = p[0], mn = p[0];
#pragma unroll
  for (int i = 1; i < 16; i++) { mx = fmaxf(mx, p[i]); mn = fminf(mn, p[i]); }
  mx = xh_max(mx); mn = xh_min(mn);
  const float c = 1.44269504088896341f;
  const float bx = -mx * c, bn = mn * c;
  f32x16 ep, en; float sp = 0.f, sn = 0.f;
#pragma unroll
  for (int i = 0; i < 16; i++) {
    ep[i] = __builtin_amdgcn_exp2f(fmaf(p[i], c, bx)); en[i] = __builtin_amdgcn_exp2f(fmaf(p[i], -c, bn));
    sp += ep[i]; sn += en[i];
  }
  const float rp = __builtin_amdgcn_rcpf(xh_add(sp)), rn = __builtin_amdgcn_rcpf(xh_add(sn));
#pragma unroll
  for (int i = 0; i < 16; i++) { ep[i] *= rp; en[i] *= rn; }
  pos[0] = pack8(ep, 0); pos[1] = pack8(ep, 1); neg[0] = pack8(en, 0); neg[1] = pack8(en, 1);
}
DI u32x4 tag4(float a, float b, float c, float d, unsigned T) {
  u32x4 r = {(__float_as_uint(a) & ~255u) | (T & 255u), (__float_as_uint(b) & ~255u) | ((T >> 8) & 255u),
             (__float_as_uint(c) & ~255u) | ((T >> 16) & 255u), (__float_as_uint(d) & ~255u) | (T >> 24)};
  return r;
}
DI bool tag_ok(const u32x4& g, unsigned T) {
  unsigned lo = __builtin_amdgcn_perm(g[1], g[0], 0x0c0c0400u), hi = __builtin_amdgcn_perm(g[3], g[2], 0x04000c0cu);
  return (lo | hi) == T;
}
DI unsigned epoch_tag(const char* ws) {
  unsigned e;
  asm volatile("s_load_dword %0, %1, 0x0\n\ts_waitcnt lgkmcnt(0)" : "=s"(e) : "s"(ws + W_E) : "memory");
  return e + 0x01010101u;
}
DI float untag(unsigned v) { return __uint_as_float(v & ~255u); }

#define STAMP(i)

__global__ __launch_bounds__(512, 2) void hedgehog_fused(const u16* __restrict__ x, const u16* __restrict__ wqkv, const u16* __restrict__ wphi,
                                                         const u16* __restrict__ wo, u16* __restrict__ out, char* __restrict__ ws) {
  extern __shared__ __attribute__((aligned(16))) char smem[];
  const int tid = threadIdx.x, lane = tid & 63;
  const int w = __builtin_amdgcn_readfirstlane(tid >> 6);
  const int b = blockIdx.x, xcd = b & 7, idx = b >> 3;
  const int rl = idx & 7, cl = idx >> 3;
  const int rb = (xcd & 1) * 8 + rl, cb = (xcd >> 1) * 4 + cl;
  const int orb = (xcd >> 1) * 4 + (idx & 3), ocb = (xcd & 1) * 8 + (idx >> 2), orot = (idx >> 2) * 4;
  const unsigned sb = (unsigned)(size_t)(LAS char*)smem;
  const __amdgpu_buffer_rsrc_t rws = __builtin_amdgcn_make_buffer_rsrc(ws, 0, W_BYTES, 0x00020000);
  const int hh = lane >> 5, l31 = lane & 31;
  u64 tstart = 0; (void)tstart;
  STAMP(0);
  unsigned T;
  bf16x8 phq[2][2][2];

  if (w >= 4) {
    const int lw = w - 4;
    const __amdgpu_buffer_rsrc_t rx = __builtin_amdgcn_make_buffer_rsrc((void*)x, 0, 2048 * 1024 * 2, 0x00020000);
    const __amdgpu_buffer_rsrc_t rw = __builtin_amdgcn_make_buffer_rsrc((void*)wqkv, 0, 1024 * 3072 * 2, 0x00020000);
    const __amdgpu_buffer_rsrc_t ro = __builtin_amdgcn_make_buffer_rsrc((void*)wo, 0, 1024 * 1024 * 2, 0x00020000);
    const __amdgpu_buffer_rsrc_t rp = __builtin_amdgcn_make_buffer_rsrc((void*)wphi, 0, 32 * 1024 * 2, 0x00020000);
    const int r8 = lane >> 3, c8 = lane & 7;
    const unsigned vx0 = r8 * 2048 + ((c8 ^ (r8 >> 1)) << 4), vx1 = r8 * 2048 + ((c8 ^ (4 + (r8 >> 1))) << 4);
    const unsigned vw = r8 * 6144 + ((c8 ^ ((r8 & 2) << 1)) << 4);
    const unsigned vo = r8 * 2048 + ((c8 ^ ((r8 & 2) << 1)) << 4);
    const int s0 = ROT ? (4 * cl + 2 * rl) & 15 : 0;
    const unsigned sx = (rb * 128 + lw * 32) * 2048, sw = cb * 128 + lw * 16 * 6144;
    auto stage = [&](int t, int slot) {
      const int kt = (t + s0) & 15;
      LAS char* dst = (LAS char*)smem + slot * SLOT;
#pragma unroll
      for (int i = 0; i < 4; i++)
        __builtin_amdgcn_raw_ptr_buffer_load_lds(rx, dst + (lw * 4 + i) * 1024, 16, (i & 1) ? vx1 : vx0, sx + i * 16384 + kt * 128, 0, 0);
#pragma unroll
      for (int i = 0; i < 6; i++)
        __builtin_amdgcn_raw_ptr_buffer_load_lds(rw, dst + 16384 + (i >> 1) * 8192 + (lw * 2 + (i & 1)) * 1024, 16, vw,
                                                 sw + (i >> 1) * 2048 + (kt * 64 + (i & 1) * 8) * 6144, 0, 0);
    };
    auto wochunks = [&](int c0, int n) {
      for (int c = c0; c < c0 + n; c++)
#pragma unroll
        for (int i = 0; i < 2; i++)
          __builtin_amdgcn_raw_ptr_buffer_load_lds(ro, (LAS char*)smem + L_WO + c * 8192 + (lw * 2 + i) * 1024, 16, vo,
                                                   (c * 64 + (lw * 2 + i) * 8) * 2048 + ocb * 128, 0, 0);
    };
    stage(0, 0); stage(1, 1);
    T = epoch_tag(ws);
    waitv<10>(); bar();
    stage(2, 2);
    int slot = 0;
#pragma unroll 1
    for (int t = 1; t < 14; t++) {
      waitv<10>(); bar();
      stage(t + 2, slot);
      slot = slot == 2 ? 0 : slot + 1;
    }
    waitv<10>(); bar();
    __builtin_amdgcn_raw_ptr_buffer_load_lds(rp, (LAS char*)smem + L_PHI + lw * 1024, 16, lane * 16, cb * 4096 + lw * 1024, 0, 0);
    wochunks(14, 2); wochunks(4, 3);
    waitv<10>(); bar();
    wochunks(7, 7);
    if (tid == 256) *(LAS unsigned*)(sb + L_CNT) = 0;
    STAMP(1);
    bar();
    wochunks(0, 4);
    {
      u32x4 g[4], gz[2];
      const unsigned pa = W_P + (cb * 16 + (lane >> 4)) * 16384 + rb * 1024 + (lw * 16 + (lane & 15)) * 16;
      const unsigned za = W_PZ + (cb * 16 + (lane >> 3)) * 512 + lw * 128 + (lane & 7) * 16;
#pragma unroll 1
      for (int sp = 0; sp < SPIN_CAP; sp++) {
#pragma unroll
        for (int i = 0; i < 4; i++) g[i] = __builtin_amdgcn_raw_buffer_load_b128(rws, pa + i * 65536, 0, SC1);
        asm volatile("" ::: "memory");
        bool ok = tag_ok(g[0], T) & tag_ok(g[1], T) & tag_ok(g[2], T) & tag_ok(g[3], T);
        if (__all(ok)) break;
        __builtin_amdgcn_s_sleep(2);
      }
      float s[4];
#pragma unroll
      for (int j = 0; j < 4; j++) s[j] = xh_add(x16_add((untag(g[0][j]) + untag(g[1][j])) + (untag(g[2][j]) + untag(g[3][j]))));
      if (lane < 16) __builtin_amdgcn_raw_buffer_store_b128(tag4(s[0], s[1], s[2], s[3], T), rws, W_R + (cb * 16 + rb) * 1024 + (lw * 16 + lane) * 16, 0, SC1);
      STAMP(2);
#pragma unroll 1
      for (int sp = 0; sp < SPIN_CAP; sp++) {
#pragma unroll
        for (int i = 0; i < 2; i++) gz[i] = __builtin_amdgcn_raw_buffer_load_b128(rws, za + i * 4096, 0, SC1);
        asm volatile("" ::: "memory");
        bool ok = tag_ok(gz[0], T) & tag_ok(gz[1], T);
        if (__all(ok)) break;
        __builtin_amdgcn_s_sleep(2);
      }
#pragma unroll
      for (int j = 0; j < 4; j++) {
        float v = untag(gz[0][j]) + untag(gz[1][j]);
        v += __uint_as_float(__builtin_amdgcn_update_dpp(0u, __float_as_uint(v), 0x128, 0xf, 0xf, false));
        s[j] = xh_add(x16_add(v));
      }
      if (lane < 8) { u32x2 zz = {pk2(s[0], s[1]), pk2(s[2], s[3])};
        *(LAS u32x2*)(sb + L_Z + lw * 64 + (lane & 1) * 32 + (lane >> 2) * 16 + ((lane >> 1) & 1) * 8) = zz; }
      const unsigned ra = W_R + (cb * 16 + lw * 4) * 1024 + lane * 16;
#pragma unroll 1
      for (int sp = 0; sp < SPIN_CAP; sp++) {
#pragma unroll
        for (int i = 0; i < 4; i++) g[i] = __builtin_amdgcn_raw_buffer_load_b128(rws, ra + i * 1024, 0, SC1);
        asm volatile("" ::: "memory");
        bool ok = tag_ok(g[0], T) & tag_ok(g[1], T) & tag_ok(g[2], T) & tag_ok(g[3], T);
        if (__all(ok)) break;
        __builtin_amdgcn_s_sleep(2);
      }
#pragma unroll
      for (int st = 0; st < 2; st++) {
        u32x4 f = {pk2(untag(g[2 * st][0]), untag(g[2 * st][1])), pk2(untag(g[2 * st][2]), untag(g[2 * st][3])),
                   pk2(untag(g[2 * st + 1][0]), untag(g[2 * st + 1][1])), pk2(untag(g[2 * st + 1][2]), untag(g[2 * st + 1][3]))};
        *(LAS u32x4*)(sb + L_KV + (lw * 2 + st) * 1024 + lane * 16) = f;
      }
    }
    STAMP(3);
    waitv<0>();
    bar();
    if (w == 4) {
#pragma unroll 1
      for (int sp = 0; sp < SPIN_CAP; sp++) {
        unsigned f = T;
        if (lane < 16) f = __builtin_amdgcn_raw_buffer_load_b32(rws, W_F + (orb * 16 + lane) * 256, 0, SC1);
        asm volatile("" ::: "memory");
        if (__all(f == T)) break;
        __builtin_amdgcn_s_sleep(1);
      }
      if (b == 0 && lane == 0) __builtin_amdgcn_raw_buffer_store_b32(T, rws, W_E, 0, SC1);
    }
  } else {
    const int wr = w >> 1, wc = w & 1;
    f32x16 aq[2] = {}, ak[2] = {}, av[2] = {};
    unsigned xa[4];
#pragma unroll
    for (int ks = 0; ks < 4; ks++) xa[ks] = sb + (wr * 64 + l31) * 128 + (((2 * ks + hh) ^ ((l31 >> 1) & 7)) << 4);
    const int q4 = (lane & 15) >> 2;
    const unsigned wa = sb + 16384 + (8 * hh + q4) * 128 + ((wc ^ (q4 >> 1)) << 6) + ((lane >> 4) & 1) * 32 + (lane & 3) * 8;
    bf16x8 fx[2][2], fw[2][3];
    auto rd = [&](int buf, int ks, unsigned so) {
      fw[buf][0] = lds_tr2(wa + so + ks * 2048, wa + so + ks * 2048 + 512);
      fx[buf][0] = lds_b128(xa[ks] + so);
      fx[buf][1] = lds_b128(xa[ks] + so + 4096);
      fw[buf][1] = lds_tr2(wa + so + 8192 + ks * 2048, wa + so + 8192 + ks * 2048 + 512);
      fw[buf][2] = lds_tr2(wa + so + 16384 + ks * 2048, wa + so + 16384 + ks * 2048 + 512);
    };
    auto mm = [&](int buf) {
      aq[0] = MFMA(fw[buf][0], fx[buf][0], aq[0]); aq[1] = MFMA(fw[buf][0], fx[buf][1], aq[1]);
      ak[0] = MFMA(fw[buf][1], fx[buf][0], ak[0]); ak[1] = MFMA(fw[buf][1], fx[buf][1], ak[1]);
      av[0] = MFMA(fx[buf][0], fw[buf][2], av[0]); av[1] = MFMA(fx[buf][1], fw[buf][2], av[1]);
    };
    auto sched = [&]() {
#pragma unroll
      for (int i = 0; i < 4; i++) { __builtin_amdgcn_sched_group_barrier(0x8, 1, 0); __builtin_amdgcn_sched_group_barrier(0x100, 2, 0); }
      __builtin_amdgcn_sched_group_barrier(0x8, 2, 0);
      __builtin_amdgcn_sched_barrier(0);
    };
    asm volatile("" : "+v"(aq[0]), "+v"(aq[1]), "+v"(ak[0]), "+v"(ak[1]), "+v"(av[0]), "+v"(av[1]));
    T = epoch_tag(ws);
    bar();
    STAMP(1);
    unsigned so = 0;
    rd(0, 0, so);
    __builtin_amdgcn_sched_barrier(0);
    for (int t = 0; t < 15; t++) {
      rd(1, 1, so); mm(0); sched();
      rd(0, 2, so); mm(1); sched();
      rd(1, 3, so); mm(0); sched();
      bar();
      so = so == 2 * SLOT ? 0 : so + SLOT;
      rd(0, 0, so); mm(1); sched();
    }
    rd(1, 1, so); mm(0); sched();
    rd(0, 2, so); mm(1); sched();
    rd(1, 3, so); mm(0); sched();
    mm(1);
    STAMP(2);
    const f32x16 zero = {};
    bf16x8 wp[2];
    {
      const unsigned pa = sb + L_PHI + wc * 2048 + (4 * hh + q4) * 64 + ((lane >> 4) & 1) * 32 + (lane & 3) * 8;
      wp[0] = lds_tr2(pa, pa + 512); wp[1] = lds_tr2(pa + 1024, pa + 1536);
    }
    bf16x8 id[2];
#pragma unroll
    for (int st = 0; st < 2; st++) {
      u32x4 v;
#pragma unroll
      for (int j = 0; j < 4; j++) {
        const int f = 16 * st + 8 * (j >> 1) + 4 * hh + 2 * (j & 1);
        v[j] = (f == l31 ? 0x3f80u : 0u) | (f + 1 == l31 ? 0x3f800000u : 0u);
      }
      id[st] = __builtin_bit_cast(bf16x8, v);
    }
    f32x16 kv[2] = {}; float z[2] = {0.f, 0.f};
#pragma unroll
    for (int rt = 0; rt < 2; rt++) {
      f32x16 p = MFMA(wp[0], pack8(ak[rt], 0), zero); p = MFMA(wp[1], pack8(ak[rt], 1), p);
      bf16x8 ph[2][2];
      dual_softmax(p, ph[0], ph[1]);
      const bf16x8 v0 = pack8(av[rt], 0), v1 = pack8(av[rt], 1);
#pragma unroll
      for (int sg = 0; sg < 2; sg++) {
        f32x16 tr = MFMA(ph[sg][0], id[0], zero); tr = MFMA(ph[sg][1], id[1], tr);
        kv[sg] = MFMA(pack8(tr, 0), v0, kv[sg]); kv[sg] = MFMA(pack8(tr, 1), v1, kv[sg]);
#pragma unroll
        for (int i = 0; i < 16; i++) z[sg] += tr[i];
      }
    }
    {
      const f32x16 snd = wr ? kv[0] : kv[1];
      f32x16 mine = wr ? kv[1] : kv[0];
      float zm = wr ? z[1] : z[0];
#pragma unroll
      for (int q = 0; q < 4; q++) { f32x4 v = {snd[4 * q], snd[4 * q + 1], snd[4 * q + 2], snd[4 * q + 3]}; *(LAS f32x4*)(sb + L_SA + w * 4096 + q * 1024 + lane * 16) = v; }
      *(LAS float*)(sb + L_XZ + w * 256 + lane * 4) = wr ? z[0] : z[1];
      STAMP(3);
      bar();
      const int tl = wc * 2 + wr;
#pragma unroll
      for (int q = 0; q < 4; q++) {
        const f32x4 v = *(const LAS f32x4*)(sb + L_SA + (w ^ 2) * 4096 + q * 1024 + lane * 16);
        __builtin_amdgcn_raw_buffer_store_b128(tag4(mine[4 * q] + v[0], mine[4 * q + 1] + v[1], mine[4 * q + 2] + v[2], mine[4 * q + 3] + v[3], T), rws,
                                               W_P + (cb * 16 + rb) * 16384 + tl * 4096 + q * 1024 + lane * 16, 0, SC1);
      }
      zm = xh_add(zm + *(const LAS float*)(sb + L_XZ + (w ^ 2) * 256 + lane * 4));
      if (lane < 32) __builtin_amdgcn_raw_buffer_store_b32((__float_as_uint(zm) & ~255u) | ((T >> (8 * (lane & 3))) & 255u), rws,
                                                           W_PZ + (cb * 16 + rb) * 512 + tl * 128 + lane * 4, 0, SC1);
    }
    STAMP(4);
#pragma unroll
    for (int rt = 0; rt < 2; rt++) {
      f32x16 p = MFMA(wp[0], pack8(aq[rt], 0), zero); p = MFMA(wp[1], pack8(aq[rt], 1), p);
      dual_softmax(p, phq[rt][0], phq[rt][1]);
    }
#pragma unroll
    for (int rt = 0; rt < 2; rt++) asm volatile("" : "+v"(phq[rt][0][0]), "+v"(phq[rt][0][1]), "+v"(phq[rt][1][0]), "+v"(phq[rt][1][1]));
    STAMP(5);
    bar();
    STAMP(6);
    bf16x8 kf[2][2], zf[2][2];
#pragma unroll
    for (int sg = 0; sg < 2; sg++)
#pragma unroll
      for (int st = 0; st < 2; st++) {
        kf[sg][st] = lds_b128(sb + L_KV + ((wc * 2 + sg) * 2 + st) * 1024 + lane * 16);
        zf[sg][st] = lds_b128(sb + L_Z + (wc * 2 + sg) * 64 + hh * 32 + st * 16);
      }
#pragma unroll
    for (int rt = 0; rt < 2; rt++) {
      f32x16 num = MFMA(kf[0][0], phq[rt][0][0], zero);
      num = MFMA(kf[0][1], phq[rt][0][1], num); num = MFMA(kf[1][0], phq[rt][1][0], num); num = MFMA(kf[1][1], phq[rt][1][1], num);
      float d0 = 0.f, d1 = 0.f;
#pragma unroll
      for (int st = 0; st < 2; st++)
#pragma unroll
        for (int j = 0; j < 4; j++) {
          const bf16x2 a0 = {phq[rt][0][st][2 * j], phq[rt][0][st][2 * j + 1]}, b0 = {zf[0][st][2 * j], zf[0][st][2 * j + 1]};
          const bf16x2 a1 = {phq[rt][1][st][2 * j], phq[rt][1][st][2 * j + 1]}, b1 = {zf[1][st][2 * j], zf[1][st][2 * j + 1]};
          d0 = __builtin_amdgcn_fdot2_f32_bf16(a0, b0, d0, false); d1 = __builtin_amdgcn_fdot2_f32_bf16(a1, b1, d1, false);
        }
      const float r = __builtin_amdgcn_rcpf(xh_add(d0 + d1) + 1e-6f);
#pragma unroll
      for (int i = 0; i < 16; i++) num[i] *= r;
#pragma unroll
      for (int st = 0; st < 2; st++)
        __builtin_amdgcn_raw_buffer_store_b128(__builtin_bit_cast(u32x4, pack8(num, st)), rws,
                                               W_O + ((rb * 4 + wr * 2 + rt) * 64 + cb * 4 + wc * 2 + st) * 1024 + lane * 16, 0, SC1);
    }
    STAMP(7);
    waitv<0>();
    if (lane == 0) {
      const unsigned n = __hip_atomic_fetch_add((LAS unsigned*)(sb + L_CNT), 1u, __ATOMIC_RELAXED, __HIP_MEMORY_SCOPE_WORKGROUP);
      if (n == 3) __builtin_amdgcn_raw_buffer_store_b32(T, rws, W_F + (rb * 16 + cb) * 256, 0, SC1);
    }
    STAMP(8);
  }
  bar();
  STAMP(9);
  {
    const int rg = w & 3, kh = w >> 2;
    const int q4 = (lane & 15) >> 2;
    const unsigned wb = sb + L_WO + kh * 65536 + (4 * hh + q4) * 128 + ((lane >> 4) & 1) * 32 + (lane & 3) * 8;
    const unsigned wa0 = wb + ((q4 >> 1) << 6), wa1 = wb + ((1 ^ (q4 >> 1)) << 6);
    const unsigned ob = W_O + ((orb * 4 + rg) * 64 + kh * 32) * 1024;
    f32x16 c0 = {}, c1 = {};
    constexpr int PF = 4;
    u32x4 of[PF];
#pragma unroll
    for (int i = 0; i < PF; i++) of[i] = __builtin_amdgcn_raw_buffer_load_b128(rws, lane * 16, ob + ((i + orot) & 31) * 1024, SC1);
    for (int g = 0; g < 8; g++) {
#pragma unroll
      for (int i = 0; i < PF; i++) {
        const unsigned ko = ((g * PF + i + orot) & 31) * 2048;
        const bf16x8 a0 = lds_tr2(wa0 + ko, wa0 + ko + 1024), a1 = lds_tr2(wa1 + ko, wa1 + ko + 1024);
        const bf16x8 bo = __builtin_bit_cast(bf16x8, of[i]);
        c0 = MFMA(a0, bo, c0); c1 = MFMA(a1, bo, c1);
        if (g < 7) of[i] = __builtin_amdgcn_raw_buffer_load_b128(rws, lane * 16, ob + ((g * PF + PF + i + orot) & 31) * 1024, SC1);
      }
    }
    STAMP(10);
    const f32x16 snd = kh ? c0 : c1;
    f32x16 mine = kh ? c1 : c0;
    const unsigned xw = sb + (w < 2 ? w * 4096 : L_SA + (w - 2) * 4096), pw = w ^ 4, xr = sb + (pw < 2 ? pw * 4096 : L_SA + (pw - 2) * 4096);
#pragma unroll
    for (int q = 0; q < 4; q++) { f32x4 v = {snd[4 * q], snd[4 * q + 1], snd[4 * q + 2], snd[4 * q + 3]}; *(LAS f32x4*)(xw + q * 1024 + lane * 16) = v; }
    bar();
    unsigned pk[8];
#pragma unroll
    for (int q = 0; q < 4; q++) {
      const f32x4 v = *(const LAS f32x4*)(xr + q * 1024 + lane * 16);
      pk[2 * q] = pk2(mine[4 * q] + v[0], mine[4 * q + 1] + v[1]); pk[2 * q + 1] = pk2(mine[4 * q + 2] + v[2], mine[4 * q + 3] + v[3]);
    }
    char* orow = (char*)out + (size_t)(orb * 128 + rg * 32 + l31) * 2048 + ocb * 128 + kh * 64 + hh * 16;
#pragma unroll
    for (int q = 0; q < 4; q += 2) {
      auto r0 = __builtin_amdgcn_permlane32_swap(pk[2 * q], pk[2 * q + 2], false, false);
      auto r1 = __builtin_amdgcn_permlane32_swap(pk[2 * q + 1], pk[2 * q + 3], false, false);
      u32x4 v = {r0[0], r1[0], r0[1], r1[1]};
      *(u32x4*)(orow + q * 16) = v;
    }
    STAMP(11);
  }
}

extern "C" void kernel_launch(void* const* d_in, const int* in_sizes, int n_in, void* d_out, int out_size, void* d_ws, size_t ws_size,
                              hipStream_t stream) {
  (void)hipFuncSetAttribute((const void*)hedgehog_fused, hipFuncAttributeMaxDynamicSharedMemorySize, LDS_BYTES);
  hedgehog_fused<<<256, 512, LDS_BYTES, stream>>>((const u16*)d_in[0], (const u16*)d_in[1], (const u16*)d_in[2], (const u16*)d_in[3],
                                                   (u16*)d_out, (char*)d_ws);
}
```

```cpp
#define PADAT 11
#define PADUS 60
#define GSEL min
#include <hip/hip_runtime.h>
#include <hip/hip_bf16.h>

typedef unsigned short u16;
typedef unsigned long long u64;
typedef __attribute__((ext_vector_type(8))) __bf16 bf16x8;
typedef __attribute__((ext_vector_type(4))) __bf16 bf16x4;
typedef __attribute__((ext_vector_type(2))) __bf16 bf16x2;
typedef __attribute__((ext_vector_type(16))) float f32x16;
typedef __attribute__((ext_vector_type(4))) float f32x4;
typedef __attribute__((ext_vector_type(2))) float f32x2;
typedef __attribute__((ext_vector_type(4))) unsigned u32x4;
typedef __attribute__((ext_vector_type(2))) unsigned u32x2;
#define LAS __attribute__((address_space(3)))
#define DI __device__ __forceinline__
#define MFMA(a, b, c) __builtin_amdgcn_mfma_f32_32x32x16_bf16((a), (b), (c), 0, 0, 0)
#define SC1 16

#define ROT 1
#define OMAP 1

constexpr int SLOT = 40960;
constexpr int L_WO = 8192;
constexpr int L_SA = 139264;
constexpr int L_XZ = L_SA + 16384;
constexpr int L_Z = L_XZ + 1024;
constexpr int L_CNT = L_Z + 256;
constexpr int L_PHI = L_SA + 18432;
constexpr int L_KV = 0;
constexpr int LDS_BYTES = 163840;
constexpr unsigned W_O = 0;
constexpr unsigned W_P = 4u << 20;
constexpr unsigned W_PZ = 8u << 20;
constexpr unsigned W_R = W_PZ + (128u << 10);
constexpr unsigned W_F = W_R + (256u << 10);
constexpr unsigned W_E = W_F + (64u << 10);
constexpr unsigned W_TS = W_E + 4096;
constexpr unsigned W_BYTES = 10u << 20;
constexpr int SPIN_CAP = 1 << 18;

DI void bar() { asm volatile("s_waitcnt lgkmcnt(0)\n\ts_barrier" ::: "memory"); }
template <int N> DI void waitv() { asm volatile("s_waitcnt vmcnt(%0)" ::"i"(N) : "memory"); }
DI unsigned pk2(float a, float b) { f32x2 v = {a, b}; return __builtin_bit_cast(unsigned, __builtin_convertvector(v, bf16x2)); }
DI bf16x8 pack8(const f32x16& x, int s) {
  u32x4 p = {pk2(x[8 * s], x[8 * s + 1]), pk2(x[8 * s + 2], x[8 * s + 3]), pk2(x[8 * s + 4], x[8 * s + 5]), pk2(x[8 * s + 6], x[8 * s + 7])};
  return __builtin_bit_cast(bf16x8, p);
}
DI float xh_max(float a) { auto r = __builtin_amdgcn_permlane32_swap(__float_as_uint(a), __float_as_uint(a), false, false); return fmaxf(__uint_as_float(r[0]), __uint_as_float(r[1])); }
DI float xh_min(float a) { auto r = __builtin_amdgcn_permlane32_swap(__float_as_uint(a), __float_as_uint(a), false, false); return fminf(__uint_as_float(r[0]), __uint_as_float(r[1])); }
DI float xh_add(float a) { auto r = __builtin_amdgcn_permlane32_swap(__float_as_uint(a), __float_as_uint(a), false, false); return __uint_as_float(r[0]) + __uint_as_float(r[1]); }
DI float x16_add(float a) { auto r = __builtin_amdgcn_permlane16_swap(__float_as_uint(a), __float_as_uint(a), false, false); return __uint_as_float(r[0]) + __uint_as_float(r[1]); }
DI bf16x8 lds_b128(unsigned a) { return *(const LAS bf16x8*)a; }
DI bf16x8 lds_tr2(unsigned a0, unsigned a1) {
  bf16x4 lo = __builtin_amdgcn_ds_read_tr16_b64_v4bf16((LAS bf16x4*)a0);
  bf16x4 hi = __builtin_amdgcn_ds_read_tr16_b64_v4bf16((LAS bf16x4*)a1);
  return __builtin_shufflevector(lo, hi, 0, 1, 2, 3, 4, 5, 6, 7);
}
DI void dual_softmax(const f32x16& p, bf16x8 (&pos)[2], bf16x8 (&neg)[2]) {
  float mx = p[0], mn = p[0];
#pragma unroll
  for (int i = 1; i < 16; i++) { mx = fmaxf(mx, p[i]); mn = fminf(mn, p[i]); }
  mx = xh_max(mx); mn = xh_min(mn);
  const float c = 1.44269504088896341f;
  const float bx = -mx * c, bn = mn * c;
  f32x16 ep, en; float sp = 0.f, sn = 0.f;
#pragma unroll
  for (int i = 0; i < 16; i++) {
    ep[i] = __builtin_amdgcn_exp2f(fmaf(p[i], c, bx)); en[i] = __builtin_amdgcn_exp2f(fmaf(p[i], -c, bn));
    sp += ep[i]; sn += en[i];
  }
  const float rp = __builtin_amdgcn_rcpf(xh_add(sp)), rn = __builtin_amdgcn_rcpf(xh_add(sn));
#pragma unroll
  for (int i = 0; i < 16; i++) { ep[i] *= rp; en[i] *= rn; }
  pos[0] = pack8(ep, 0); pos[1] = pack8(ep, 1); neg[0] = pack8(en, 0); neg[1] = pack8(en, 1);
}
DI u32x4 tag4(float a, float b, float c, float d, unsigned T) {
  u32x4 r = {(__float_as_uint(a) & ~255u) | (T & 255u), (__float_as_uint(b) & ~255u) | ((T >> 8) & 255u),
             (__float_as_uint(c) & ~255u) | ((T >> 16) & 255u), (__float_as_uint(d) & ~255u) | (T >> 24)};
  return r;
}
DI bool tag_ok(const u32x4& g, unsigned T) {
  unsigned lo = __builtin_amdgcn_perm(g[1], g[0], 0x0c0c0400u), hi = __builtin_amdgcn_perm(g[3], g[2], 0x04000c0cu);
  return (lo | hi) == T;
}
DI unsigned epoch_tag(const char* ws) {
  unsigned e;
  asm volatile("s_load_dword %0, %1, 0x0\n\ts_waitcnt lgkmcnt(0)" : "=s"(e) : "s"(ws + W_E) : "memory");
  return e + 0x01010101u;
}
DI float untag(unsigned v) { return __uint_as_float(v & ~255u); }

#define STAMP(i) do { if ((i) == 0) { tstart = __builtin_amdgcn_s_memrealtime(); if (tid == 0) __builtin_amdgcn_raw_buffer_store_b32((unsigned)tstart, rws, W_TS + b * 4, 0, SC1); } if ((i) == PADAT && w < 4) { u32x4 q = __builtin_amdgcn_raw_buffer_load_b128(rws, W_TS + lane * 16, 0, SC1); unsigned me = (unsigned)tstart; int d = GSEL(GSEL((int)(q[0] - me), (int)(q[1] - me)), GSEL((int)(q[2] - me), (int)(q[3] - me))); for (int o = 1; o < 64; o <<= 1) d = GSEL(d, __shfl_xor(d, o)); tstart += __builtin_amdgcn_readfirstlane(d); } if ((i) == PADAT && w < 4) { while (__builtin_amdgcn_s_memrealtime() < tstart + PADUS * 100) __builtin_amdgcn_s_sleep(1); } } while (0)

__global__ __launch_bounds__(512, 2) void hedgehog_fused(const u16* __restrict__ x, const u16* __restrict__ wqkv, const u16* __restrict__ wphi,
                                                         const u16* __restrict__ wo, u16* __restrict__ out, char* __restrict__ ws) {
  extern __shared__ __attribute__((aligned(16))) char smem[];
  const int tid = threadIdx.x, lane = tid & 63;
  const int w = __builtin_amdgcn_readfirstlane(tid >> 6);
  const int b = blockIdx.x, xcd = b & 7, idx = b >> 3;
  const int rl = idx & 7, cl = idx >> 3;
  const int rb = (xcd & 1) * 8 + rl, cb = (xcd >> 1) * 4 + cl;
  const int orb = (xcd >> 1) * 4 + (idx & 3), ocb = (xcd & 1) * 8 + (idx >> 2), orot = (idx >> 2) * 4;
  const unsigned sb = (unsigned)(size_t)(LAS char*)smem;
  const __amdgpu_buffer_rsrc_t rws = __builtin_amdgcn_make_buffer_rsrc(ws, 0, W_BYTES, 0x00020000);
  const int hh = lane >> 5, l31 = lane & 31;
  u64 tstart = 0; (void)tstart;
  STAMP(0);
  unsigned T;
  bf16x8 phq[2][2][2];

  if (w >= 4) {
    const int lw = w - 4;
    const __amdgpu_buffer_rsrc_t rx = __builtin_amdgcn_make_buffer_rsrc((void*)x, 0, 2048 * 1024 * 2, 0x00020000);
    const __amdgpu_buffer_rsrc_t rw = __builtin_amdgcn_make_buffer_rsrc((void*)wqkv, 0, 1024 * 3072 * 2, 0x00020000);
    const __amdgpu_buffer_rsrc_t ro = __builtin_amdgcn_make_buffer_rsrc((void*)wo, 0, 1024 * 1024 * 2, 0x00020000);
    const __amdgpu_buffer_rsrc_t rp = __builtin_amdgcn_make_buffer_rsrc((void*)wphi, 0, 32 * 1024 * 2, 0x00020000);
    const int r8 = lane >> 3, c8 = lane & 7;
    const unsigned vx0 = r8 * 2048 + ((c8 ^ (r8 >> 1)) << 4), vx1 = r8 * 2048 + ((c8 ^ (4 + (r8 >> 1))) << 4);
    const unsigned vw = r8 * 6144 + ((c8 ^ ((r8 & 2) << 1)) << 4);
    const unsigned vo = r8 * 2048 + ((c8 ^ ((r8 & 2) << 1)) << 4);
    const int s0 = ROT ? (4 * cl + 2 * rl) & 15 : 0;
    const unsigned sx = (rb * 128 + lw * 32) * 2048, sw = cb * 128 + lw * 16 * 6144;
    auto stage = [&](int t, int slot) {
      const int kt = (t + s0) & 15;
      LAS char* dst = (LAS char*)smem + slot * SLOT;
#pragma unroll
      for (int i = 0; i < 4; i++)
        __builtin_amdgcn_raw_ptr_buffer_load_lds(rx, dst + (lw * 4 + i) * 1024, 16, (i & 1) ? vx1 : vx0, sx + i * 16384 + kt * 128, 0, 0);
#pragma unroll
      for (int i = 0; i < 6; i++)
        __builtin_amdgcn_raw_ptr_buffer_load_lds(rw, dst + 16384 + (i >> 1) * 8192 + (lw * 2 + (i & 1)) * 1024, 16, vw,
                                                 sw + (i >> 1) * 2048 + (kt * 64 + (i & 1) * 8) * 6144, 0, 0);
    };
    auto wochunks = [&](int c0, int n) {
      for (int c = c0; c < c0 + n; c++)
#pragma unroll
        for (int i = 0; i < 2; i++)
          __builtin_amdgcn_raw_ptr_buffer_load_lds(ro, (LAS char*)smem + L_WO + c * 8192 + (lw * 2 + i) * 1024, 16, vo,
                                                   (c * 64 + (lw * 2 + i) * 8) * 2048 + ocb * 128, 0, 0);
    };
    stage(0, 0); stage(1, 1);
    T = epoch_tag(ws);
    waitv<10>(); bar();
    stage(2, 2);
    int slot = 0;
#pragma unroll 1
    for (int t = 1; t < 14; t++) {
      waitv<10>(); bar();
      stage(t + 2, slot);
      slot = slot == 2 ? 0 : slot + 1;
    }
    waitv<10>(); bar();
    __builtin_amdgcn_raw_ptr_buffer_load_lds(rp, (LAS char*)smem + L_PHI + lw * 1024, 16, lane * 16, cb * 4096 + lw * 1024, 0, 0);
    wochunks(14, 2); wochunks(4, 3);
    waitv<10>(); bar();
    wochunks(7, 7);
    if (tid == 256) *(LAS unsigned*)(sb + L_CNT) = 0;
    STAMP(1);
    bar();
    wochunks(0, 4);
    {
      u32x4 g[4], gz[2];
      const unsigned pa = W_P + (cb * 16 + (lane >> 4)) * 16384 + rb * 1024 + (lw * 16 + (lane & 15)) * 16;
      const unsigned za = W_PZ + (cb * 16 + (lane >> 3)) * 512 + lw * 128 + (lane & 7) * 16;
#pragma unroll 1
      for (int sp = 0; sp < SPIN_CAP; sp++) {
#pragma unroll
        for (int i = 0; i < 4; i++) g[i] = __builtin_amdgcn_raw_buffer_load_b128(rws, pa + i * 65536, 0, SC1);
        asm volatile("" ::: "memory");
        bool ok = tag_ok(g[0], T) & tag_ok(g[1], T) & tag_ok(g[2], T) & tag_ok(g[3], T);
        if (__all(ok)) break;
        __builtin_amdgcn_s_sleep(2);
      }
      float s[4];
#pragma unroll
      for (int j = 0; j < 4; j++) s[j] = xh_add(x16_add((untag(g[0][j]) + untag(g[1][j])) + (untag(g[2][j]) + untag(g[3][j]))));
      if (lane < 16) __builtin_amdgcn_raw_buffer_store_b128(tag4(s[0], s[1], s[2], s[3], T), rws, W_R + (cb * 16 + rb) * 1024 + (lw * 16 + lane) * 16, 0, SC1);
      STAMP(2);
#pragma unroll 1
      for (int sp = 0; sp < SPIN_CAP; sp++) {
#pragma unroll
        for (int i = 0; i < 2; i++) gz[i] = __builtin_amdgcn_raw_buffer_load_b128(rws, za + i * 4096, 0, SC1);
        asm volatile("" ::: "memory");
        bool ok = tag_ok(gz[0], T) & tag_ok(gz[1], T);
        if (__all(ok)) break;
        __builtin_amdgcn_s_sleep(2);
      }
#pragma unroll
      for (int j = 0; j < 4; j++) {
        float v = untag(gz[0][j]) + untag(gz[1][j]);
        v += __uint_as_float(__builtin_amdgcn_update_dpp(0u, __float_as_uint(v), 0x128, 0xf, 0xf, false));
        s[j] = xh_add(x16_add(v));
      }
      if (lane < 8) { u32x2 zz = {pk2(s[0], s[1]), pk2(s[2], s[3])};
        *(LAS u32x2*)(sb + L_Z + lw * 64 + (lane & 1) * 32 + (lane >> 2) * 16 + ((lane >> 1) & 1) * 8) = zz; }
      const unsigned ra = W_R + (cb * 16 + lw * 4) * 1024 + lane * 16;
#pragma unroll 1
      for (int sp = 0; sp < SPIN_CAP; sp++) {
#pragma unroll
        for (int i = 0; i < 4; i++) g[i] = __builtin_amdgcn_raw_buffer_load_b128(rws, ra + i * 1024, 0, SC1);
        asm volatile("" ::: "memory");
        bool ok = tag_ok(g[0], T) & tag_ok(g[1], T) & tag_ok(g[2], T) & tag_ok(g[3], T);
        if (__all(ok)) break;
        __builtin_amdgcn_s_sleep(2);
      }
#pragma unroll
      for (int st = 0; st < 2; st++) {
        u32x4 f = {pk2(untag(g[2 * st][0]), untag(g[2 * st][1])), pk2(untag(g[2 * st][2]), untag(g[2 * st][3])),
                   pk2(untag(g[2 * st + 1][0]), untag(g[2 * st + 1][1])), pk2(untag(g[2 * st + 1][2]), untag(g[2 * st + 1][3]))};
        *(LAS u32x4*)(sb + L_KV + (lw * 2 + st) * 1024 + lane * 16) = f;
      }
    }
    STAMP(3);
    waitv<0>();
    bar();
    if (w == 4) {
#pragma unroll 1
      for (int sp = 0; sp < SPIN_CAP; sp++) {
        unsigned f = T;
        if (lane < 16) f = __builtin_amdgcn_raw_buffer_load_b32(rws, W_F + (orb * 16 + lane) * 256, 0, SC1);
        asm volatile("" ::: "memory");
        if (__all(f == T)) break;
        __builtin_amdgcn_s_sleep(1);
      }
      if (b == 0 && lane == 0) __builtin_amdgcn_raw_buffer_store_b32(T, rws, W_E, 0, SC1);
    }
  } else {
    const int wr = w >> 1, wc = w & 1;
    f32x16 aq[2] = {}, ak[2] = {}, av[2] = {};
    unsigned xa[4];
#pragma unroll
    for (int ks = 0; ks < 4; ks++) xa[ks] = sb + (wr * 64 + l31) * 128 + (((2 * ks + hh) ^ ((l31 >> 1) & 7)) << 4);
    const int q4 = (lane & 15) >> 2;
    const unsigned wa = sb + 16384 + (8 * hh + q4) * 128 + ((wc ^ (q4 >> 1)) << 6) + ((lane >> 4) & 1) * 32 + (lane & 3) * 8;
    bf16x8 fx[2][2], fw[2][3];
    auto rd = [&](int buf, int ks, unsigned so) {
      fw[buf][0] = lds_tr2(wa + so + ks * 2048, wa + so + ks * 2048 + 512);
      fx[buf][0] = lds_b128(xa[ks] + so);
      fx[buf][1] = lds_b128(xa[ks] + so + 4096);
      fw[buf][1] = lds_tr2(wa + so + 8192 + ks * 2048, wa + so + 8192 + ks * 2048 + 512);
      fw[buf][2] = lds_tr2(wa + so + 16384 + ks * 2048, wa + so + 16384 + ks * 2048 + 512);
    };
    auto mm = [&](int buf) {
      aq[0] = MFMA(fw[buf][0], fx[buf][0], aq[0]); aq[1] = MFMA(fw[buf][0], fx[buf][1], aq[1]);
      ak[0] = MFMA(fw[buf][1], fx[buf][0], ak[0]); ak[1] = MFMA(fw[buf][1], fx[buf][1], ak[1]);
      av[0] = MFMA(fx[buf][0], fw[buf][2], av[0]); av[1] = MFMA(fx[buf][1], fw[buf][2], av[1]);
    };
    auto sched = [&]() {
#pragma unroll
      for (int i = 0; i < 4; i++) { __builtin_amdgcn_sched_group_barrier(0x8, 1, 0); __builtin_amdgcn_sched_group_barrier(0x100, 2, 0); }
      __builtin_amdgcn_sched_group_barrier(0x8, 2, 0);
      __builtin_amdgcn_sched_barrier(0);
    };
    asm volatile("" : "+v"(aq[0]), "+v"(aq[1]), "+v"(ak[0]), "+v"(ak[1]), "+v"(av[0]), "+v"(av[1]));
    T = epoch_tag(ws);
    bar();
    STAMP(1);
    unsigned so = 0;
    rd(0, 0, so);
    __builtin_amdgcn_sched_barrier(0);
    for (int t = 0; t < 15; t++) {
      rd(1, 1, so); mm(0); sched();
      rd(0, 2, so); mm(1); sched();
      rd(1, 3, so); mm(0); sched();
      bar();
      so = so == 2 * SLOT ? 0 : so + SLOT;
      rd(0, 0, so); mm(1); sched();
    }
    rd(1, 1, so); mm(0); sched();
    rd(0, 2, so); mm(1); sched();
    rd(1, 3, so); mm(0); sched();
    mm(1);
    STAMP(2);
    const f32x16 zero = {};
    bf16x8 wp[2];
    {
      const unsigned pa = sb + L_PHI + wc * 2048 + (4 * hh + q4) * 64 + ((lane >> 4) & 1) * 32 + (lane & 3) * 8;
      wp[0] = lds_tr2(pa, pa + 512); wp[1] = lds_tr2(pa + 1024, pa + 1536);
    }
    bf16x8 id[2];
#pragma unroll
    for (int st = 0; st < 2; st++) {
      u32x4 v;
#pragma unroll
      for (int j = 0; j < 4; j++) {
        const int f = 16 * st + 8 * (j >> 1) + 4 * hh + 2 * (j & 1);
        v[j] = (f == l31 ? 0x3f80u : 0u) | (f + 1 == l31 ? 0x3f800000u : 0u);
      }
      id[st] = __builtin_bit_cast(bf16x8, v);
    }
    f32x16 kv[2] = {}; float z[2] = {0.f, 0.f};
#pragma unroll
    for (int rt = 0; rt < 2; rt++) {
      f32x16 p = MFMA(wp[0], pack8(ak[rt], 0), zero); p = MFMA(wp[1], pack8(ak[rt], 1), p);
      bf16x8 ph[2][2];
      dual_softmax(p, ph[0], ph[1]);
      const bf16x8 v0 = pack8(av[rt], 0), v1 = pack8(av[rt], 1);
#pragma unroll
      for (int sg = 0; sg < 2; sg++) {
        f32x16 tr = MFMA(ph[sg][0], id[0], zero); tr = MFMA(ph[sg][1], id[1], tr);
        kv[sg] = MFMA(pack8(tr, 0), v0, kv[sg]); kv[sg] = MFMA(pack8(tr, 1), v1, kv[sg]);
#pragma unroll
        for (int i = 0; i < 16; i++) z[sg] += tr[i];
      }
    }
    {
      const f32x16 snd = wr ? kv[0] : kv[1];
      f32x16 mine = wr ? kv[1] : kv[0];
      float zm = wr ? z[1] : z[0];
#pragma unroll
      for (int q = 0; q < 4; q++) { f32x4 v = {snd[4 * q], snd[4 * q + 1], snd[4 * q + 2], snd[4 * q + 3]}; *(LAS f32x4*)(sb + L_SA + w * 4096 + q * 1024 + lane * 16) = v; }
      *(LAS float*)(sb + L_XZ + w * 256 + lane * 4) = wr ? z[0] : z[1];
      STAMP(3);
      bar();
      const int tl = wc * 2 + wr;
#pragma unroll
      for (int q = 0; q < 4; q++) {
        const f32x4 v = *(const LAS f32x4*)(sb + L_SA + (w ^ 2) * 4096 + q * 1024 + lane * 16);
        __builtin_amdgcn_raw_buffer_store_b128(tag4(mine[4 * q] + v[0], mine[4 * q + 1] + v[1], mine[4 * q + 2] + v[2], mine[4 * q + 3] + v[3], T), rws,
                                               W_P + (cb * 16 + rb) * 16384 + tl * 4096 + q * 1024 + lane * 16, 0, SC1);
      }
      zm = xh_add(zm + *(const LAS float*)(sb + L_XZ + (w ^ 2) * 256 + lane * 4));
      if (lane < 32) __builtin_amdgcn_raw_buffer_store_b32((__float_as_uint(zm) & ~255u) | ((T >> (8 * (lane & 3))) & 255u), rws,
                                                           W_PZ + (cb * 16 + rb) * 512 + tl * 128 + lane * 4, 0, SC1);
    }
    STAMP(4);
#pragma unroll
    for (int rt = 0; rt < 2; rt++) {
      f32x16 p = MFMA(wp[0], pack8(aq[rt], 0), zero); p = MFMA(wp[1], pack8(aq[rt], 1), p);
      dual_softmax(p, phq[rt][0], phq[rt][1]);
    }
#pragma unroll
    for (int rt = 0; rt < 2; rt++) asm volatile("" : "+v"(phq[rt][0][0]), "+v"(phq[rt][0][1]), "+v"(phq[rt][1][0]), "+v"(phq[rt][1][1]));
    STAMP(5);
    bar();
    STAMP(6);
    bf16x8 kf[2][2], zf[2][2];
#pragma unroll
    for (int sg = 0; sg < 2; sg++)
#pragma unroll
      for (int st = 0; st < 2; st++) {
        kf[sg][st] = lds_b128(sb + L_KV + ((wc * 2 + sg) * 2 + st) * 1024 + lane * 16);
        zf[sg][st] = lds_b128(sb + L_Z + (wc * 2 + sg) * 64 + hh * 32 + st * 16);
      }
#pragma unroll
    for (int rt = 0; rt < 2; rt++) {
      f32x16 num = MFMA(kf[0][0], phq[rt][0][0], zero);
      num = MFMA(kf[0][1], phq[rt][0][1], num); num = MFMA(kf[1][0], phq[rt][1][0], num); num = MFMA(kf[1][1], phq[rt][1][1], num);
      float d0 = 0.f, d1 = 0.f;
#pragma unroll
      for (int st = 0; st < 2; st++)
#pragma unroll
        for (int j = 0; j < 4; j++) {
          const bf16x2 a0 = {phq[rt][0][st][2 * j], phq[rt][0][st][2 * j + 1]}, b0 = {zf[0][st][2 * j], zf[0][st][2 * j + 1]};
          const bf16x2 a1 = {phq[rt][1][st][2 * j], phq[rt][1][st][2 * j + 1]}, b1 = {zf[1][st][2 * j], zf[1][st][2 * j + 1]};
          d0 = __builtin_amdgcn_fdot2_f32_bf16(a0, b0, d0, false); d1 = __builtin_amdgcn_fdot2_f32_bf16(a1, b1, d1, false);
        }
      const float r = __builtin_amdgcn_rcpf(xh_add(d0 + d1) + 1e-6f);
#pragma unroll
      for (int i = 0; i < 16; i++) num[i] *= r;
#pragma unroll
      for (int st = 0; st < 2; st++)
        __builtin_amdgcn_raw_buffer_store_b128(__builtin_bit_cast(u32x4, pack8(num, st)), rws,
                                               W_O + ((rb * 4 + wr * 2 + rt) * 64 + cb * 4 + wc * 2 + st) * 1024 + lane * 16, 0, SC1);
    }
    STAMP(7);
    waitv<0>();
    if (lane == 0) {
      const unsigned n = __hip_atomic_fetch_add((LAS unsigned*)(sb + L_CNT), 1u, __ATOMIC_RELAXED, __HIP_MEMORY_SCOPE_WORKGROUP);
      if (n == 3) __builtin_amdgcn_raw_buffer_store_b32(T, rws, W_F + (rb * 16 + cb) * 256, 0, SC1);
    }
    STAMP(8);
  }
  bar();
  STAMP(9);
  {
    const int rg = w & 3, kh = w >> 2;
    const int q4 = (lane & 15) >> 2;
    const unsigned wb = sb + L_WO + kh * 65536 + (4 * hh + q4) * 128 + ((lane >> 4) & 1) * 32 + (lane & 3) * 8;
    const unsigned wa0 = wb + ((q4 >> 1) << 6), wa1 = wb + ((1 ^ (q4 >> 1)) << 6);
    const unsigned ob = W_O + ((orb * 4 + rg) * 64 + kh * 32) * 1024;
    f32x16 c0 = {}, c1 = {};
    constexpr int PF = 4;
    u32x4 of[PF];
#pragma unroll
    for (int i = 0; i < PF; i++) of[i] = __builtin_amdgcn_raw_buffer_load_b128(rws, lane * 16, ob + ((i + orot) & 31) * 1024, SC1);
    for (int g = 0; g < 8; g++) {
#pragma unroll
      for (int i = 0; i < PF; i++) {
        const unsigned ko = ((g * PF + i + orot) & 31) * 2048;
        const bf16x8 a0 = lds_tr2(wa0 + ko, wa0 + ko + 1024), a1 = lds_tr2(wa1 + ko, wa1 + ko + 1024);
        const bf16x8 bo = __builtin_bit_cast(bf16x8, of[i]);
        c0 = MFMA(a0, bo, c0); c1 = MFMA(a1, bo, c1);
        if (g < 7) of[i] = __builtin_amdgcn_raw_buffer_load_b128(rws, lane * 16, ob + ((g * PF + PF + i + orot) & 31) * 1024, SC1);
      }
    }
    STAMP(10);
    const f32x16 snd = kh ? c0 : c1;
    f32x16 mine = kh ? c1 : c0;
    const unsigned xw = sb + (w < 2 ? w * 4096 : L_SA + (w - 2) * 4096), pw = w ^ 4, xr = sb + (pw < 2 ? pw * 4096 : L_SA + (pw - 2) * 4096);
#pragma unroll
    for (int q = 0; q < 4; q++) { f32x4 v = {snd[4 * q], snd[4 * q + 1], snd[4 * q + 2], snd[4 * q + 3]}; *(LAS f32x4*)(xw + q * 1024 + lane * 16) = v; }
    bar();
    unsigned pk[8];
#pragma unroll
    for (int q = 0; q < 4; q++) {
      const f32x4 v = *(const LAS f32x4*)(xr + q * 1024 + lane * 16);
      pk[2 * q] = pk2(mine[4 * q] + v[0], mine[4 * q + 1] + v[1]); pk[2 * q + 1] = pk2(mine[4 * q + 2] + v[2], mine[4 * q + 3] + v[3]);
    }
    char* orow = (char*)out + (size_t)(orb * 128 + rg * 32 + l31) * 2048 + ocb * 128 + kh * 64 + hh * 16;
#pragma unroll
    for (int q = 0; q < 4; q += 2) {
      auto r0 = __builtin_amdgcn_permlane32_swap(pk[2 * q], pk[2 * q + 2], false, false);
      auto r1 = __builtin_amdgcn_permlane32_swap(pk[2 * q + 1], pk[2 * q + 3], false, false);
      u32x4 v = {r0[0], r1[0], r0[1], r1[1]};
      *(u32x4*)(orow + q * 16) = v;
    }
    STAMP(11);
  }
}

extern "C" void kernel_launch(void* const* d_in, const int* in_sizes, int n_in, void* d_out, int out_size, void* d_ws, size_t ws_size,
                              hipStream_t stream) {
  (void)hipFuncSetAttribute((const void*)hedgehog_fused, hipFuncAttributeMaxDynamicSharedMemorySize, LDS_BYTES);
  hedgehog_fused<<<256, 512, LDS_BYTES, stream>>>((const u16*)d_in[0], (const u16*)d_in[1], (const u16*)d_in[2], (const u16*)d_in[3],
                                                   (u16*)d_out, (char*)d_ws);
}
```

```cpp
#include <hip/hip_runtime.h>
#include <hip/hip_bf16.h>

typedef unsigned short u16;
typedef unsigned long long u64;
typedef __attribute__((ext_vector_type(8))) __bf16 bf16x8;
typedef __attribute__((ext_vector_type(4))) __bf16 bf16x4;
typedef __attribute__((ext_vector_type(2))) __bf16 bf16x2;
typedef __attribute__((ext_vector_type(16))) float f32x16;
typedef __attribute__((ext_vector_type(4))) float f32x4;
typedef __attribute__((ext_vector_type(2))) float f32x2;
typedef __attribute__((ext_vector_type(4))) unsigned u32x4;
typedef __attribute__((ext_vector_type(2))) unsigned u32x2;
#define LAS __attribute__((address_space(3)))
#define LP(T, a) ((LAS T*)(size_t)(a))
#define DI __device__ __forceinline__
#define MFMA32(a, b, c) __builtin_amdgcn_mfma_f32_32x32x16_bf16((a), (b), (c), 0, 0, 0)
#define MFMA16(a, b, c) __builtin_amdgcn_mfma_f32_16x16x32_bf16((a), (b), (c), 0, 0, 0)
#define SC1 16

#define ROT 0
#define SPF 0
#define OMAP 1
#define CODE_KB 20
#define OPF 3
#define PR 1
#define VPF 3
#define IPF 1
#define IPN 2
#define WPF 1
#define WROT 1
#define Y2 1
#define SL1 12
#define MASKED 0
#define SL2 0
#define SL3 0
#define HPRIO 0
#define WPT 9
#define IPD 0
#define CODE_BACK 4
#define PSL 2
#define TPF 1
#define GPRIO 0
#define SLW 2
#define OSC 16

constexpr int SLOT = 40960;
constexpr int L_WO = 0;
constexpr int L_V = 122880;
constexpr int L_KV = 139264;
constexpr int L_PHI = 147456;
constexpr int L_Z = 151552;
constexpr int L_CNT = 151808;
constexpr int L_DUMMY = 152064;
constexpr int L_FX = 131072;
constexpr int LDS_BYTES = 163840;
constexpr unsigned W_O = 0;
constexpr unsigned W_P = 4u << 20;
constexpr unsigned W_PZ = 8u << 20;
constexpr unsigned W_R = W_PZ + (128u << 10);
constexpr unsigned W_F = W_R + (256u << 10);
constexpr unsigned W_E = W_F + (64u << 10);
constexpr unsigned W_TS = 9u  __H2__ ;
constexpr unsigned W_BYTES = 10u << 20;
constexpr int SPIN_CAP = 1 << 18;

DI void bar() { asm volatile("s_waitcnt lgkmcnt(0)\n\ts_barrier" ::: "memory"); }
DI void bar0() { asm volatile("s_barrier" ::: "memory"); }
template <int N> DI void waitv() { asm volatile("s_waitcnt vmcnt(%0)" ::"i"(N) : "memory"); }
DI unsigned pk2(float a, float b) { f32x2 v = {a, b}; return __builtin_bit_cast(unsigned, __builtin_convertvector(v, bf16x2)); }
DI unsigned pk2v(f32x2 v) { return __builtin_bit_cast(unsigned, __builtin_convertvector(v, bf16x2)); }
DI bf16x8 pack8(const f32x16& x, int s) {
  u32x4 p = {pk2(x[8 * s], x[8 * s + 1]), pk2(x[8 * s + 2], x[8 * s + 3]), pk2(x[8 * s + 4], x[8 * s + 5]), pk2(x[8 * s + 6], x[8 * s + 7])};
  return __builtin_bit_cast(bf16x8, p);
}
DI bf16x8 pack16to32(const f32x4& c0, const f32x4& c1) {
  auto a = __builtin_amdgcn_permlane16_swap(pk2(c0[0], c0[1]), pk2(c1[0], c1[1]), false, false);
  auto b = __builtin_amdgcn_permlane16_swap(pk2(c0[2], c0[3]), pk2(c1[2], c1[3]), false, false);
  u32x4 p = {a[0], b[0], a[1], b[1]};
  return __builtin_bit_cast(bf16x8, p);
}
DI float max3(float a, float b, float c) { float r; asm("v_max3_f32 %0, %1, %2, %3" : "=v"(r) : "v"(a), "v"(b), "v"(c)); return r; }
DI float min3(float a, float b, float c) { float r; asm("v_min3_f32 %0, %1, %2, %3" : "=v"(r) : "v"(a), "v"(b), "v"(c)); return r; }
DI float xh_add(float a) { auto r = __builtin_amdgcn_permlane32_swap(__float_as_uint(a), __float_as_uint(a), false, false); return __uint_as_float(r[0]) + __uint_as_float(r[1]); }
DI float x16_add(float a) { auto r = __builtin_amdgcn_permlane16_swap(__float_as_uint(a), __float_as_uint(a), false, false); return __uint_as_float(r[0]) + __uint_as_float(r[1]); }
DI float dot2(unsigned a, unsigned b, float c) { return __builtin_amdgcn_fdot2_f32_bf16(__builtin_bit_cast(bf16x2, a), __builtin_bit_cast(bf16x2, b), c, false); }
DI bf16x8 lds_b128(unsigned a) { return *LP(const bf16x8, a); }
DI bf16x8 lds_tr2(unsigned a0, unsigned a1) {
  bf16x4 lo = __builtin_amdgcn_ds_read_tr16_b64_v4bf16(LP(bf16x4, a0));
  bf16x4 hi = __builtin_amdgcn_ds_read_tr16_b64_v4bf16(LP(bf16x4, a1));
  return __builtin_shufflevector(lo, hi, 0, 1, 2, 3, 4, 5, 6, 7);
}
DI void dual_softmax(const f32x16& p, unsigned (&pos)[8], unsigned (&neg)[8]) {
  float mx = fmaxf(p[0], p[1]), mn = fminf(p[0], p[1]);
#pragma unroll
  for (int i = 2; i < 16; i += 2) { mx = fmaxf(fmaxf(mx, p[i]), p[i + 1]); mn = fminf(fminf(mn, p[i]), p[i + 1]); }
  {
    auto r = __builtin_amdgcn_permlane32_swap(__float_as_uint(mx), __float_as_uint(mx), false, false);
    auto s = __builtin_amdgcn_permlane32_swap(__float_as_uint(mn), __float_as_uint(mn), false, false);
    mx = fmaxf(__uint_as_float(r[0]), __uint_as_float(r[1])); mn = fminf(__uint_as_float(s[0]), __uint_as_float(s[1]));
  }
  const float c = 1.44269504088896341f;
  const f32x2 cp = {c, c}, cn = {-c, -c}, bx = {-mx * c, -mx * c}, bn = {mn * c, mn * c};
  f32x2 ep[8], en[8];
#pragma unroll
  for (int i = 0; i < 8; i++) {
    const f32x2 v = {p[2 * i], p[2 * i + 1]};
    const f32x2 a = __builtin_elementwise_fma(v, cp, bx), b = __builtin_elementwise_fma(v, cn, bn);
    ep[i] = f32x2{__builtin_amdgcn_exp2f(a[0]), __builtin_amdgcn_exp2f(a[1])};
    en[i] = f32x2{__builtin_amdgcn_exp2f(b[0]), __builtin_amdgcn_exp2f(b[1])};
  }
  const f32x2 s2p = ((ep[0] + ep[1]) + (ep[2] + ep[3])) + ((ep[4] + ep[5]) + (ep[6] + ep[7]));
  const f32x2 s2n = ((en[0] + en[1]) + (en[2] + en[3])) + ((en[4] + en[5]) + (en[6] + en[7]));
  const float rp = __builtin_amdgcn_rcpf(xh_add(s2p[0] + s2p[1])), rn = __builtin_amdgcn_rcpf(xh_add(s2n[0] + s2n[1]));
  const f32x2 r2p = {rp, rp}, r2n = {rn, rn};
#pragma unroll
  for (int i = 0; i < 8; i++) { pos[i] = pk2v(ep[i] * r2p); neg[i] = pk2v(en[i] * r2n); }
}
DI u32x4 tag4(const f32x4& v, unsigned T) {
  u32x4 r = {__builtin_amdgcn_perm(__float_as_uint(v[0]), T, 0x07060500u), __builtin_amdgcn_perm(__float_as_uint(v[1]), T, 0x07060501u),
             __builtin_amdgcn_perm(__float_as_uint(v[2]), T, 0x07060502u), __builtin_amdgcn_perm(__float_as_uint(v[3]), T, 0x07060503u)};
  return r;
}
DI bool tag_ok(const u32x4& g, unsigned T) {
  unsigned lo = __builtin_amdgcn_perm(g[1], g[0], 0x0c0c0400u), hi = __builtin_amdgcn_perm(g[3], g[2], 0x04000c0cu);
  return (lo | hi) == T;
}
template <int N> DI void gather(u32x4 (&g)[N], __amdgpu_buffer_rsrc_t r, unsigned a, unsigned stride, unsigned T) {
  unsigned off[N];
#pragma unroll
  for (int i = 0; i < N; i++) off[i] = a + i * stride;
#pragma unroll
  for (int i = 0; i < N; i++) g[i] = __builtin_amdgcn_raw_buffer_load_b128(r, off[i], 0, SC1);
#pragma unroll 1
  for (int sp = 0; sp < SPIN_CAP; sp++) {
    bool ok[N], all = true;
#pragma unroll
    for (int i = 0; i < N; i++) { ok[i] = tag_ok(g[i], T); all &= ok[i]; }
    if (__all(all)) break;
    __builtin_amdgcn_s_sleep(PSL);
    asm volatile("" ::: "memory");
    u32x4 n[N];
#pragma unroll
    for (int i = 0; i < N; i++) n[i] = __builtin_amdgcn_raw_buffer_load_b128(r, MASKED && ok[i] ? 0x80000000u : off[i], 0, SC1);
#pragma unroll
    for (int i = 0; i < N; i++) g[i] = MASKED && ok[i] ? g[i] : n[i];
  }
}
DI float untag(unsigned v) { return __uint_as_float(v & ~255u); }
DI unsigned epoch_tag(const char* ws) {
  unsigned e;
  asm volatile("s_load_dword %0, %1, 0x0\n\ts_waitcnt lgkmcnt(0)" : "=s"(e) : "s"(ws + W_E) : "memory");
  return e + 0x01010101u;
}

#define STAMP(i)
#define STAMPV(i)
#define STAMP_DUMP()

__global__ __launch_bounds__(512, 2) void hedgehog_fused(const u16* __restrict__ x, const u16* __restrict__ wqkv, const u16* __restrict__ wphi,
                                                         const u16* __restrict__ wo, u16* __restrict__ out, char* __restrict__ ws) {
  extern __shared__ __attribute__((aligned(16))) char smem[];
  const int tid = threadIdx.x, lane = tid & 63;
  const int w = __builtin_amdgcn_readfirstlane(tid >> 6);
  const int b = blockIdx.x, xcd = b & 7, idx = b >> 3;
  const int rl = idx & 7, cl = idx >> 3;
#define CBX 0
  const int rb = (xcd & 1) * 8 + rl, cb = CBX == 0 ? (xcd >> 1) * 4 + cl : CBX == 1 ? ((xcd >> 1) ^ 1) * 4 + cl : CBX == 2 ? (xcd >> 1) + cl * 4 : (xcd >> 2) * 8 + ((xcd >> 1) & 1) * 2 + (cl & 1) + (cl >> 1) * 4;
  const int orb = (xcd >> 1) * 4 + (idx & 3), ocb = (xcd & 1) * 8 + (idx >> 2), orot = (idx >> 2) * 2;
  const unsigned sb = (unsigned)(size_t)(LAS char*)smem;
  const __amdgpu_buffer_rsrc_t rws = __builtin_amdgcn_make_buffer_rsrc(ws, 0, W_BYTES, 0x00020000);
  const int hh = lane >> 5, l31 = lane & 31, l15 = lane & 15, g4 = lane >> 4, q4 = l15 >> 2, p4 = lane & 3;
  unsigned st_r = 0, st_c = 0; (void)st_r; (void)st_c;
  asm volatile("" :: "s"(x), "s"(wqkv), "s"(wphi), "s"(wo), "s"(out), "s"(ws));
  STAMP(0);
  const __amdgpu_buffer_rsrc_t rx = __builtin_amdgcn_make_buffer_rsrc((void*)x, 0, 2048 * 1024 * 2, 0x00020000);
  const __amdgpu_buffer_rsrc_t rw = __builtin_amdgcn_make_buffer_rsrc((void*)wqkv, 0, 1024 * 3072 * 2, 0x00020000);
  const int lw = w & 3;
  const int turn = (rl ^ xcd) & 7;
  auto pull = [&](int s) {
    const int u = cl * 4 + lw;
    const unsigned vx = lane < 16 ? lane * 2048 : 0x80000000u;
    __builtin_amdgcn_raw_ptr_buffer_load_lds(rx, (LAS char*)smem + L_DUMMY, 4, vx, ((xcd & 1) * 1024 + ((xcd >> 1) * 16 + u) * 16) * 2048 + s * 128, 0, 0);
    const int l3 = lane / 3;
    const unsigned vw = lane < 18 ? ((xcd & 1) * 96 + u * 6 + l3) * 2048 + (lane - l3 * 3) * 128 : lane < 30 ? (u * 12 + lane - 18) * 2048 + 384 : 0x80000000u;
    __builtin_amdgcn_raw_ptr_buffer_load_lds(rw, (LAS char*)smem + L_DUMMY, 4, vw, (xcd >> 1) * 512 + s * 64 * 6144, 0, 0);
  };

  const int r8 = lane >> 3, c8 = lane & 7;
  const unsigned vx0 = r8 * 2048 + ((c8 ^ (r8 >> 1)) << 4), vx1 = r8 * 2048 + ((c8 ^ (4 + (r8 >> 1))) << 4);
  const unsigned vw0 = r8 * 6144 + ((c8 ^ (r8 & 2)) << 4), vw1 = r8 * 6144 + ((c8 ^ (4 + (r8 & 2))) << 4);
  const unsigned sx = (rb * 128 + lw * 32) * 2048, sw = cb * 128 + lw * 16 * 6144;
  auto stage = [&](int t, int slot) {
    LAS char* dst = (LAS char*)smem + slot * SLOT;
#define LX(i) __builtin_amdgcn_raw_ptr_buffer_load_lds(rx, dst + (lw * 4 + i) * 1024, 16, (i & 1) ? vx1 : vx0, sx + i * 16384 + t * 128, 0, 0)
#define LW(i) __builtin_amdgcn_raw_ptr_buffer_load_lds(rw, dst + 16384 + (i >> 1) * 8192 + (lw * 2 + (i & 1)) * 1024, 16, (i & 1) ? vw1 : vw0, \
                                                     sw + (i >> 1) * 2048 + (t * 64 + (i & 1) * 8) * 6144, 0, 0)
    LW(2); LX(0); LW(3); LX(1); LW(4); LX(2); LW(5); LX(3); LW(0); LW(1);
  };
  u64 pc; asm volatile("s_getpc_b64 %0" : "=s"(pc));
  if (w < 4) {
    STAMP(12);
    stage(0, 0);
    STAMP(13);
  }
  int role = w; asm volatile("" : "+s"(role));

  if (role < 4) {
    const __amdgpu_buffer_rsrc_t ro = __builtin_amdgcn_make_buffer_rsrc((void*)wo, 0, 1024 * 1024 * 2, 0x00020000);
    const bool early = VPF && turn >= IPN && turn <= VPF;
    if (early) pull(turn);
    stage(1, 1);
    STAMP(14);
    if (early) waitv<12>(); else waitv<10>();
    STAMP(15);
    bar0();
    stage(2, 2);
    {
      const int u = idx * 4 + lw;
      const char* c = (const char*)(pc & ~127ull) + (u < CODE_KB * 4 ? u : 0) * 256;
      const char* p0 = ws + u * 65536;
      const char* p1 = u < 64 ? (const char*)out + u * 65536 : u < 96 ? (const char*)wo + (u - 64) * 65536 : ws + W_PZ + (u & 7) * 65536;
      asm volatile("s_load_dwordx2 s[100:101], %0, 0x3c\n\ts_load_dwordx2 s[100:101], %0, 0xbc\n\ts_load_dword s100, %1, 0x0\n\ts_load_dword s100, %2, 0x0"
                   :: "s"(c), "s"(p0), "s"(p1) : "s100", "s101");
    }
    int slot = 0; bool pfd = false;
#pragma unroll 1
    for (int t = 1; t < 14; t++) {
      if (pfd) waitv<12>(); else waitv<10>();
      pfd = false;
      bar0();
      STAMPV(t + 16);
      if (t + VPF < 16 && ((t + VPF) & 7) == turn) { pull(t + VPF); pfd = true; }
      if (t == WPT + turn && turn < 2) {
        const unsigned u = ((turn * 8 + xcd) * 4 + cl) * 4 + lw;
        __builtin_amdgcn_raw_ptr_buffer_load_lds(ro, (LAS char*)smem + L_DUMMY, 4, l31 * 128, u * 8192, 0, 0);
        __builtin_amdgcn_raw_ptr_buffer_load_lds(ro, (LAS char*)smem + L_DUMMY, 4, l31 * 128, u * 8192 + 4096, 0, 0);
        pfd = true;
      }
      stage(t + 2, slot);
      slot = slot == 2 ? 0 : slot + 1;
    }
    const __amdgpu_buffer_rsrc_t rp = __builtin_amdgcn_make_buffer_rsrc((void*)wphi, 0, 32 * 1024 * 2, 0x00020000);
    __builtin_amdgcn_raw_ptr_buffer_load_lds(rp, (LAS char*)smem + L_PHI + lw * 1024, 16, lane * 16, cb * 4096 + lw * 1024, 0, 0);
    waitv<11>(); bar0();
    u32x4 wr_[32];
    const int wrot = WROT ? (OMAP == 0 ? rl * 4 : OMAP == 1 ? (idx & 3) * 8 : (idx & 1) * 16) : 0;
    const unsigned vo = r8 * 2048 + c8 * 16, so_ = lw * 256 * 2048 + ocb * 128;
#pragma unroll
    for (int i = 0; i < 8; i++) wr_[i] = __builtin_amdgcn_raw_buffer_load_b128(ro, vo, so_ + ((i + wrot) & 31) * 16384, 0);
    waitv<8>(); bar0();
#pragma unroll
    for (int i = 8; i < 32; i++) wr_[i] = __builtin_amdgcn_raw_buffer_load_b128(ro, vo, so_ + ((i + wrot) & 31) * 16384, 0);
    const unsigned T = epoch_tag(ws);
    if (tid == 0) *LP(unsigned, sb + L_CNT) = 0;
    STAMP(1);
    bar();
    {
      u32x4 g[4], gz[2];
      const unsigned pa = W_P + (cb * 16 + g4) * 16384 + rb * 1024 + (lw * 16 + l15) * 16;
      const unsigned za = W_PZ + (cb * 16 + r8) * 512 + lw * 128 + c8 * 16;
      bar0();
      STAMP(6);
      __builtin_amdgcn_s_setprio(HPRIO);
      __builtin_amdgcn_s_sleep(SL1);
      gather<4>(g, rws, pa, 65536, T);
      STAMP(7);
      f32x4 s;
#pragma unroll
      for (int j = 0; j < 4; j++) s[j] = xh_add(x16_add((untag(g[0][j]) + untag(g[1][j])) + (untag(g[2][j]) + untag(g[3][j]))));
      if (lane < 16) __builtin_amdgcn_raw_buffer_store_b128(tag4(s, T), rws, W_R + (cb * 16 + rb) * 1024 + (lw * 16 + lane) * 16, 0, SC1);
      STAMP(2);
      gather<2>(gz, rws, za, 4096, T);
#pragma unroll
      for (int j = 0; j < 4; j++) {
        float v = untag(gz[0][j]) + untag(gz[1][j]);
        v += __uint_as_float(__builtin_amdgcn_update_dpp(0u, __float_as_uint(v), 0x128, 0xf, 0xf, false));
        s[j] = xh_add(x16_add(v));
      }
      if (lane < 8) { u32x2 zz = {pk2(s[0], s[1]), pk2(s[2], s[3])};
        *LP(u32x2, sb + L_Z + lw * 64 + (lane & 1) * 32 + (lane >> 2) * 16 + ((lane >> 1) & 1) * 8) = zz; }
      STAMP(3);
      __builtin_amdgcn_s_sleep(SL2);
      gather<4>(g, rws, W_R + (cb * 16 + lw * 4) * 1024 + lane * 16, 1024, T);
#pragma unroll
      for (int st = 0; st < 2; st++) {
        u32x4 f = {pk2(untag(g[2 * st][0]), untag(g[2 * st][1])), pk2(untag(g[2 * st][2]), untag(g[2 * st][3])),
                   pk2(untag(g[2 * st + 1][0]), untag(g[2 * st + 1][1])), pk2(untag(g[2 * st + 1][2]), untag(g[2 * st + 1][3]))};
        *LP(u32x4, sb + L_KV + (lw * 2 + st) * 1024 + lane * 16) = f;
      }
    }
    STAMP(4);
    __builtin_amdgcn_s_setprio(0);
    bar();
    __builtin_amdgcn_s_sleep(SLW);
    {
      const unsigned da = sb + L_WO + lw * 32768 + r8 * 128 + ((c8 ^ (r8 & 6)) << 4);
#pragma unroll
      for (int i = 0; i < 32; i++) *LP(u32x4, da + ((i + wrot) & 31) * 1024) = wr_[i];
    }
    STAMP(5);
    if (w == 0) {
      __builtin_amdgcn_s_sleep(SL3);
      unsigned fa = lane < 16 ? W_F + (orb * 16 + lane) * 256 : 0x80000000u;
#pragma unroll 1
      for (int sp = 0; sp < SPIN_CAP; sp++) {
        const unsigned f = __builtin_amdgcn_raw_buffer_load_b32(rws, fa, 0, SC1);
        asm volatile("" ::: "memory");
        if (f == T) fa = 0x80000000u;
        if (__all(fa == 0x80000000u)) break;
        __builtin_amdgcn_s_sleep(PSL);
      }
      if (b == 0 && lane == 0) __builtin_amdgcn_raw_buffer_store_b32(T, rws, W_E, 0, SC1);
    }
  } else {
    const int wr = lw >> 1, wc = lw & 1;
    __builtin_amdgcn_s_waitcnt(0x0f70);
    if (lw < 2) {
      const unsigned vo = lw ? (b * 12 + (lane & 15)) * 128 + g4 * 64 * 6144 : (b * 8 + (lane & 7)) * 2048 + g4 * 128;
      const u16* base = lw ? wqkv : x;
      if (g4 < IPN && l15 < (lw ? 12 : 8)) {
        unsigned keep;
        asm volatile("s_mov_b32 %0, m0\n\ts_mov_b32 m0, %3\n\ts_nop 0\n\tglobal_load_lds_dword %1, %2\n\ts_mov_b32 m0, %0"
                     : "=&s"(keep) : "v"(vo), "s"(base), "s"(sb + L_DUMMY) : "memory");
      }
    }
    f32x4 acc[6][4] = {};
    unsigned xa[2];
#pragma unroll
    for (int ks = 0; ks < 2; ks++) xa[ks] = sb + (wr * 64 + l15) * 128 + (((4 * ks + g4) ^ (l15 >> 1)) << 4);
    unsigned wa[2];
#pragma unroll
    for (int nt = 0; nt < 2; nt++) wa[nt] = sb + 16384 + (8 * g4 + q4) * 128 + (((wc * 2 + nt) ^ ((q4 >> 1) | ((g4 & 1) << 1))) << 5) + p4 * 8;
    bf16x8 fx[2][4], fw[2][6];
    unsigned xs[2], wsl[2];
    auto slot_at = [&](unsigned so) {
      xs[0] = xa[0] + so; xs[1] = xa[1] + so; wsl[0] = wa[0] + so; wsl[1] = wa[1] + so;
      asm volatile("" : "+v"(xs[0]), "+v"(xs[1]), "+v"(wsl[0]), "+v"(wsl[1]));
    };
    auto rdw = [&](int buf, int j, int ks) {
      const unsigned a = wsl[j & 1] + (j < 4 ? 8192 + (j >> 1) * 8192 : 0) + ks * 4096;
      fw[buf][j] = lds_tr2(a, a + 512);
    };
    auto rd = [&](int buf, int ks, unsigned so, int j0, int j1) {
      if (ks == 0) slot_at(so);
      rdw(buf, j0, ks);
#pragma unroll
      for (int m = 0; m < 4; m++) fx[buf][m] = lds_b128(xs[ks] + m * 2048);
#pragma unroll
      for (int j = j0 + 1; j < j1; j++) rdw(buf, j, ks);
    };
    auto mm = [&](int buf, int j0, int j1) {
#pragma unroll
      for (int j = j0; j < j1; j++)
#pragma unroll
        for (int m0 = 0; m0 < 4; m0++) {
          const int m = (j & 1) ? 3 - m0 : m0;
          acc[j][m] = (j == 2 || j == 3) ? MFMA16(fx[buf][m], fw[buf][j], acc[j][m]) : MFMA16(fw[buf][j], fx[buf][m], acc[j][m]);
        }
    };
#define SCHED(nds, nmf) do { _Pragma("unroll") for (int i_ = 0; i_ < (nds); i_++) { __builtin_amdgcn_sched_group_barrier(0x8, 1, 0); __builtin_amdgcn_sched_group_barrier(0x100, 1, 0); } \
      if ((nmf) > (nds)) __builtin_amdgcn_sched_group_barrier(0x8, (nmf) - (nds), 0); __builtin_amdgcn_sched_barrier(0); } while (0)
#pragma unroll
    for (int j = 0; j < 6; j++) asm volatile("" : "+v"(acc[j][0]), "+v"(acc[j][1]), "+v"(acc[j][2]), "+v"(acc[j][3]));
    const unsigned T = epoch_tag(ws);
    bar();
    STAMP(1);
    unsigned so = 0;
    rd(0, 0, so, 0, 6);
    __builtin_amdgcn_sched_barrier(0);
#pragma unroll 1
    for (int t = 0; t < 12; t++) {
      rd(1, 1, so, 0, 6); mm(0, 0, 6); SCHED(16, 24);
      bar();
      so = so == 2 * SLOT ? 0 : so + SLOT;
      rd(0, 0, so, 0, 6); mm(1, 0, 6); SCHED(16, 24);
    }
    rd(1, 1, 0, 0, 6); mm(0, 0, 6); SCHED(16, 24);
    bar();
    rd(0, 0, SLOT, 0, 4); mm(1, 0, 6); SCHED(12, 24);
    rd(1, 1, SLOT, 0, 4); mm(0, 0, 4); SCHED(12, 16);
    bar();
    rd(0, 0, 2 * SLOT, 0, 4); mm(1, 0, 4); SCHED(12, 16);
    rd(1, 1, 2 * SLOT, 0, 4); mm(0, 0, 4); SCHED(12, 16);
    bar();
    rd(0, 0, 0, 0, 4); mm(1, 0, 4); SCHED(12, 16);
    rd(1, 1, 0, 0, 4); mm(0, 0, 4); SCHED(12, 16);
    bf16x8 wp[2];
    {
      const unsigned pa = sb + L_PHI + wc * 2048 + (8 * hh + q4) * 64 + (g4 & 1) * 32 + p4 * 8;
      wp[0] = lds_tr2(pa, pa + 256); wp[1] = lds_tr2(pa + 1024, pa + 1280);
    }
    mm(1, 0, 4);
    STAMP(2);
    const f32x16 zero = {};
    const unsigned img = sb + (1 + wc) * SLOT + 24576;
    u32x4 vf[2][2];
#pragma unroll
    for (int rt = 0; rt < 2; rt++) {
      f32x16 p = MFMA32(wp[0], pack16to32(acc[0][2 * rt], acc[0][2 * rt + 1]), zero);
      p = MFMA32(wp[1], pack16to32(acc[1][2 * rt], acc[1][2 * rt + 1]), p);
#pragma unroll
      for (int et = 0; et < 2; et++) {
        const f32x4 a = acc[2 + et][2 * rt], c = acc[2 + et][2 * rt + 1];
        vf[rt][et] = u32x4{pk2(a[0], a[1]), pk2(a[2], a[3]), pk2(c[0], c[1]), pk2(c[2], c[3])};
        *LP(u32x4, sb + L_V + lw * 4096 + (rt * 2 + et) * 1024 + lane * 16) = vf[rt][et];
      }
      unsigned ph[2][8];
      dual_softmax(p, ph[0], ph[1]);
      const int sw = ((l31 >> 1) & 1) | ((l31 >> 2) & 2) | (l31 & 4);
      const unsigned ra = img + (wr * 64 + rt * 32 + l31) * 64;
#pragma unroll
      for (int q = 0; q < 4; q++) {
        const unsigned a = ra + (((2 * q + hh) ^ sw) << 3);
        *LP(u32x2, a) = u32x2{ph[0][2 * q], ph[0][2 * q + 1]};
        *LP(u32x2, a + 8192) = u32x2{ph[1][2 * q], ph[1][2 * q + 1]};
      }
    }
    STAMP(3);
    bar();
    {
      u32x4 vo[2][2];
#pragma unroll
      for (int i = 0; i < 4; i++) vo[i >> 1][i & 1] = *LP(const u32x4, sb + L_V + (lw ^ 2) * 4096 + i * 1024 + lane * 16);
      const int srow = 4 * g4 + q4;
      const int sw = ((srow >> 1) & 1) | ((srow >> 2) & 2) | (srow & 4);
      bf16x8 pf[4][2];
#pragma unroll
      for (int ks = 0; ks < 4; ks++)
#pragma unroll
        for (int ft = 0; ft < 2; ft++) {
          const unsigned a = img + wr * 8192 + (((wr ^ (ks >> 1)) * 64 + (ks & 1) * 32 + srow) * 64) + (((4 * ft + p4) ^ sw) << 3);
          pf[ks][ft] = lds_tr2(a, a + 1024);
        }
      f32x4 kv[2][2] = {}, z[2] = {};
#pragma unroll
      for (int ks = 0; ks < 4; ks++)
#pragma unroll
        for (int ft = 0; ft < 2; ft++)
#pragma unroll
          for (int et = 0; et < 2; et++)
            kv[ft][et] = MFMA16(pf[ks][ft], __builtin_bit_cast(bf16x8, ks < 2 ? vf[ks & 1][et] : vo[ks & 1][et]), kv[ft][et]);
      const u32x4 one4 = {0x3f803f80u, 0x3f803f80u, 0x3f803f80u, 0x3f803f80u};
#pragma unroll
      for (int ks = 0; ks < 4; ks++)
#pragma unroll
        for (int ft = 0; ft < 2; ft++) z[ft] = MFMA16(pf[ks][ft], __builtin_bit_cast(bf16x8, one4), z[ft]);
      const int tl = wc * 2 + wr;
      const unsigned pa = W_P + (cb * 16 + rb) * 16384 + tl * 4096 + (g4 >> 1) * 1024 + ((g4 & 1) * 32 + l15) * 16;
#pragma unroll
      for (int ft = 0; ft < 2; ft++)
#pragma unroll
        for (int et = 0; et < 2; et++) __builtin_amdgcn_raw_buffer_store_b128(tag4(kv[ft][et], T), rws, pa + ft * 2048 + et * 256, 0, SC1);
      const f32x4 zs = (lane & 1) ? z[1] : z[0];
      if (l15 < 2) __builtin_amdgcn_raw_buffer_store_b128(tag4(zs, T), rws, W_PZ + (cb * 16 + rb) * 512 + tl * 128 + (l15 * 4 + g4) * 16, 0, SC1);
    }
    bar0();
    STAMP(4);
    __builtin_amdgcn_sched_barrier(0);
    rd(0, 0, SLOT, 4, 6);
    __builtin_amdgcn_sched_barrier(0);
    rd(1, 1, SLOT, 4, 6); mm(0, 4, 6); SCHED(8, 8);
    rd(0, 0, 2 * SLOT, 4, 6); mm(1, 4, 6); SCHED(8, 8);
    rd(1, 1, 2 * SLOT, 4, 6); mm(0, 4, 6); SCHED(8, 8);
    rd(0, 0, 0, 4, 6); mm(1, 4, 6); SCHED(8, 8);
    rd(1, 1, 0, 4, 6); mm(0, 4, 6); SCHED(8, 8);
    mm(1, 4, 6);
    u32x4 phq[2][2][2];
#pragma unroll
    for (int rt = 0; rt < 2; rt++) {
      f32x16 p = MFMA32(wp[0], pack16to32(acc[4][2 * rt], acc[4][2 * rt + 1]), zero);
      p = MFMA32(wp[1], pack16to32(acc[5][2 * rt], acc[5][2 * rt + 1]), p);
      unsigned ph[2][8];
      dual_softmax(p, ph[0], ph[1]);
#pragma unroll
      for (int sg = 0; sg < 2; sg++)
#pragma unroll
        for (int st = 0; st < 2; st++) phq[rt][sg][st] = u32x4{ph[sg][4 * st], ph[sg][4 * st + 1], ph[sg][4 * st + 2], ph[sg][4 * st + 3]};
      asm volatile("" : "+v"(phq[rt][0][0]), "+v"(phq[rt][0][1]), "+v"(phq[rt][1][0]), "+v"(phq[rt][1][1]));
    }
    STAMP(5);
    __builtin_amdgcn_s_setprio(2);
    bar();
    STAMP(6);
    bf16x8 kf[2][2]; u32x4 zf[2][2];
#pragma unroll
    for (int sg = 0; sg < 2; sg++)
#pragma unroll
      for (int st = 0; st < 2; st++) {
        kf[sg][st] = lds_b128(sb + L_KV + ((wc * 2 + sg) * 2 + st) * 1024 + lane * 16);
        zf[sg][st] = *LP(const u32x4, sb + L_Z + (wc * 2 + sg) * 64 + hh * 32 + st * 16);
      }
#pragma unroll
    for (int rt = 0; rt < 2; rt++) {
      f32x16 num = MFMA32(kf[0][0], __builtin_bit_cast(bf16x8, phq[rt][0][0]), zero);
      num = MFMA32(kf[0][1], __builtin_bit_cast(bf16x8, phq[rt][0][1]), num);
      num = MFMA32(kf[1][0], __builtin_bit_cast(bf16x8, phq[rt][1][0]), num);
      num = MFMA32(kf[1][1], __builtin_bit_cast(bf16x8, phq[rt][1][1]), num);
      float d0 = 0.f, d1 = 0.f;
#pragma unroll
      for (int st = 0; st < 2; st++)
#pragma unroll
        for (int j = 0; j < 4; j++) {
          d0 = dot2(phq[rt][0][st][j], zf[0][st][j], d0); d1 = dot2(phq[rt][1][st][j], zf[1][st][j], d1);
        }
      const float r = __builtin_amdgcn_rcpf(xh_add(d0 + d1) + 1e-6f);
      num *= r;
#pragma unroll
      for (int st = 0; st < 2; st++)
        __builtin_amdgcn_raw_buffer_store_b128(__builtin_bit_cast(u32x4, pack8(num, st)), rws,
                                               W_O + ((rb * 8 + wr * 4 + rt * 2 + (g4 & 1)) * 32 + cb * 2 + wc) * 1024 + (2 * st + hh) * 256 + l15 * 16, 0, SC1);
    }
    STAMP(7);
    waitv<0>();
    if (lane == 0) {
      const unsigned n = __hip_atomic_fetch_add(LP(unsigned, sb + L_CNT), 1u, __ATOMIC_RELAXED, __HIP_MEMORY_SCOPE_WORKGROUP);
      if (n == 3) __builtin_amdgcn_raw_buffer_store_b32(T, rws, W_F + (rb * 16 + cb) * 256, 0, SC1);
    }
    __builtin_amdgcn_s_setprio(GPRIO);
    STAMP(8);
  }
  bar();
  STAMP(9);
  {
    const int rg = w & 3, kh = w >> 2;
    const int kr = 16 * (g4 >> 1) + 4 * (g4 & 1) + q4;
    unsigned wa[4];
#pragma unroll
    for (int nt = 0; nt < 4; nt++) wa[nt] = sb + L_WO + kh * 65536 + kr * 128 + ((nt ^ ((kr >> 1) & 3)) << 5) + p4 * 8;
    const unsigned ob = W_O + ((orb * 8 + rg * 2) * 32 + kh * 16) * 1024;
    f32x4 c[2][4] = {};
    constexpr int PF = OPF;
    u32x4 of[PF][2];
    bf16x8 a[2][4];
    const unsigned v0 = lane * 16 + kh * 32768, v1 = lane * 16 + (kh ^ 1) * 32768;
    auto ldo = [&](int i) {
      const unsigned so = ob + ((i + orot) & 15) * 1024;
      of[i % PF][0] = __builtin_amdgcn_raw_buffer_load_b128(rws, v0, so, SC1);
      of[i % PF][1] = __builtin_amdgcn_raw_buffer_load_b128(rws, v1, so, SC1);
    };
    auto ldw = [&](int i) {
      const unsigned ko = ((i + orot) & 15) * 4096;
#pragma unroll
      for (int nt = 0; nt < 4; nt++) a[i & 1][nt] = lds_tr2(wa[nt] + ko, wa[nt] + ko + 1024);
    };
#pragma unroll
    for (int i = 0; i < PF - 1; i++) ldo(i);
    ldw(0);
    __builtin_amdgcn_sched_barrier(0);
#pragma unroll
    for (int i = 0; i < 16; i++) {
      if (i + PF - 1 < 16) ldo(i + PF - 1);
      __builtin_amdgcn_sched_barrier(0);
      if (i < 15) ldw(i + 1);
      const bf16x8 b0 = __builtin_bit_cast(bf16x8, of[i % PF][0]), b1 = __builtin_bit_cast(bf16x8, of[i % PF][1]);
#pragma unroll
      for (int nt = 0; nt < 4; nt++) { c[0][nt] = MFMA16(a[i & 1][nt], b0, c[0][nt]); c[1][nt] = MFMA16(a[i & 1][nt], b1, c[1][nt]); }
#pragma unroll
      for (int q = 0; q < 8; q++) { __builtin_amdgcn_sched_group_barrier(0x8, 1, 0); __builtin_amdgcn_sched_group_barrier(0x100, 1, 0); }
      __builtin_amdgcn_sched_barrier(0);
    }
    STAMP(10);
#pragma unroll
    for (int nt = 0; nt < 4; nt++) *LP(f32x4, sb + L_FX + w * 4096 + nt * 1024 + lane * 16) = c[1][nt];
    bar();
    unsigned pk[8];
#pragma unroll
    for (int nt = 0; nt < 4; nt++) {
      const f32x4 v = *LP(const f32x4, sb + L_FX + (w ^ 4) * 4096 + nt * 1024 + lane * 16) + c[0][nt];
      pk[2 * nt] = pk2(v[0], v[1]); pk[2 * nt + 1] = pk2(v[2], v[3]);
    }
    const __amdgpu_buffer_rsrc_t rout = __builtin_amdgcn_make_buffer_rsrc(out, 0, 2048 * 1024 * 2, 0x00020000);
    const unsigned oa = (orb * 128 + rg * 32 + kh * 16 + l15) * 2048 + ocb * 128 + (g4 & 1) * 32 + (g4 >> 1) * 16;
#pragma unroll
    for (int nt = 0; nt < 4; nt += 2) {
      auto r0 = __builtin_amdgcn_permlane16_swap(pk[2 * nt], pk[2 * nt + 2], false, false);
      auto r1 = __builtin_amdgcn_permlane16_swap(pk[2 * nt + 1], pk[2 * nt + 3], false, false);
      u32x4 v = {r0[0], r1[0], r0[1], r1[1]};
      __builtin_amdgcn_raw_buffer_store_b128(v, rout, oa + nt * 32, 0, OSC);
    }
    STAMP(11);
    STAMP_DUMP();
  }
}

extern "C" void kernel_launch(void* const* d_in, const int* in_sizes, int n_in, void* d_out, int out_size, void* d_ws, size_t ws_size,
                              hipStream_t stream) {
  (void)hipFuncSetAttribute((const void*)hedgehog_fused, hipFuncAttributeMaxDynamicSharedMemorySize, LDS_BYTES);
  hedgehog_fused<<<256, 512, LDS_BYTES, stream>>>((const u16*)d_in[0], (const u16*)d_in[1], (const u16*)d_in[2], (const u16*)d_in[3],
                                                   (u16*)d_out, (char*)d_ws);
}
```

```cpp
#define XS 0
#include <hip/hip_runtime.h>
#include <hip/hip_bf16.h>

typedef unsigned short u16;
typedef unsigned long long u64;
typedef __attribute__((ext_vector_type(8))) __bf16 bf16x8;
typedef __attribute__((ext_vector_type(4))) __bf16 bf16x4;
typedef __attribute__((ext_vector_type(2))) __bf16 bf16x2;
typedef __attribute__((ext_vector_type(16))) float f32x16;
typedef __attribute__((ext_vector_type(4))) float f32x4;
typedef __attribute__((ext_vector_type(2))) float f32x2;
typedef __attribute__((ext_vector_type(4))) unsigned u32x4;
typedef __attribute__((ext_vector_type(2))) unsigned u32x2;
#define LAS __attribute__((address_space(3)))
#define LP(T, a) ((LAS T*)(size_t)(a))
#define DI __device__ __forceinline__
#define MFMA32(a, b, c) __builtin_amdgcn_mfma_f32_32x32x16_bf16((a), (b), (c), 0, 0, 0)
#define MFMA16(a, b, c) __builtin_amdgcn_mfma_f32_16x16x32_bf16((a), (b), (c), 0, 0, 0)
#define SC1 16

#define ROT 0
#define SPF 0
#define OMAP 1
#define CODE_KB 20
#define OPF 3
#define PR 1
#define VPF 3
#define IPF 1
#define IPN 2
#define WPF 1
#define WROT 1
#define Y2 1
#define SL1 12
#define MASKED 0
#define SL2 0
#define SL3 0
#define HPRIO 0
#define WPT 9
#define IPD 0
#define CODE_BACK 4
#define PSL 2
#define TPF 1
#define GPRIO 0
#define SLW 2
#define OSC 16

constexpr int SLOT = 40960;
constexpr int L_WO = 0;
constexpr int L_V = 122880;
constexpr int L_KV = 139264;
constexpr int L_PHI = 147456;
constexpr int L_Z = 151552;
constexpr int L_CNT = 151808;
constexpr int L_DUMMY = 152064;
constexpr int L_FX = 131072;
constexpr int LDS_BYTES = 163840;
constexpr unsigned W_O = 0;
constexpr unsigned W_P = 4u << 20;
constexpr unsigned W_PZ = 8u << 20;
constexpr unsigned W_R = W_PZ + (128u << 10);
constexpr unsigned W_F = W_R + (256u << 10);
constexpr unsigned W_E = W_F + (64u << 10);
constexpr unsigned W_TS = 9u  __H2__ ;
constexpr unsigned W_BYTES = 10u << 20;
constexpr int SPIN_CAP = 1 << 18;

DI void bar() { asm volatile("s_waitcnt lgkmcnt(0)\n\ts_barrier" ::: "memory"); }
DI void bar0() { asm volatile("s_barrier" ::: "memory"); }
template <int N> DI void waitv() { asm volatile("s_waitcnt vmcnt(%0)" ::"i"(N) : "memory"); }
DI unsigned pk2(float a, float b) { f32x2 v = {a, b}; return __builtin_bit_cast(unsigned, __builtin_convertvector(v, bf16x2)); }
DI unsigned pk2v(f32x2 v) { return __builtin_bit_cast(unsigned, __builtin_convertvector(v, bf16x2)); }
DI bf16x8 pack8(const f32x16& x, int s) {
  u32x4 p = {pk2(x[8 * s], x[8 * s + 1]), pk2(x[8 * s + 2], x[8 * s + 3]), pk2(x[8 * s + 4], x[8 * s + 5]), pk2(x[8 * s + 6], x[8 * s + 7])};
  return __builtin_bit_cast(bf16x8, p);
}
DI bf16x8 pack16to32(const f32x4& c0, const f32x4& c1) {
  auto a = __builtin_amdgcn_permlane16_swap(pk2(c0[0], c0[1]), pk2(c1[0], c1[1]), false, false);
  auto b = __builtin_amdgcn_permlane16_swap(pk2(c0[2], c0[3]), pk2(c1[2], c1[3]), false, false);
  u32x4 p = {a[0], b[0], a[1], b[1]};
  return __builtin_bit_cast(bf16x8, p);
}
DI float max3(float a, float b, float c) { float r; asm("v_max3_f32 %0, %1, %2, %3" : "=v"(r) : "v"(a), "v"(b), "v"(c)); return r; }
DI float min3(float a, float b, float c) { float r; asm("v_min3_f32 %0, %1, %2, %3" : "=v"(r) : "v"(a), "v"(b), "v"(c)); return r; }
DI float xh_add(float a) { auto r = __builtin_amdgcn_permlane32_swap(__float_as_uint(a), __float_as_uint(a), false, false); return __uint_as_float(r[0]) + __uint_as_float(r[1]); }
DI float x16_add(float a) { auto r = __builtin_amdgcn_permlane16_swap(__float_as_uint(a), __float_as_uint(a), false, false); return __uint_as_float(r[0]) + __uint_as_float(r[1]); }
DI float dot2(unsigned a, unsigned b, float c) { return __builtin_amdgcn_fdot2_f32_bf16(__builtin_bit_cast(bf16x2, a), __builtin_bit_cast(bf16x2, b), c, false); }
DI bf16x8 lds_b128(unsigned a) { return *LP(const bf16x8, a); }
DI bf16x8 lds_tr2(unsigned a0, unsigned a1) {
  bf16x4 lo = __builtin_amdgcn_ds_read_tr16_b64_v4bf16(LP(bf16x4, a0));
  bf16x4 hi = __builtin_amdgcn_ds_read_tr16_b64_v4bf16(LP(bf16x4, a1));
  return __builtin_shufflevector(lo, hi, 0, 1, 2, 3, 4, 5, 6, 7);
}
DI void dual_softmax(const f32x16& p, unsigned (&pos)[8], unsigned (&neg)[8]) {
  float mx = fmaxf(p[0], p[1]), mn = fminf(p[0], p[1]);
#pragma unroll
  for (int i = 2; i < 16; i += 2) { mx = fmaxf(fmaxf(mx, p[i]), p[i + 1]); mn = fminf(fminf(mn, p[i]), p[i + 1]); }
  {
    auto r = __builtin_amdgcn_permlane32_swap(__float_as_uint(mx), __float_as_uint(mx), false, false);
    auto s = __builtin_amdgcn_permlane32_swap(__float_as_uint(mn), __float_as_uint(mn), false, false);
    mx = fmaxf(__uint_as_float(r[0]), __uint_as_float(r[1])); mn = fminf(__uint_as_float(s[0]), __uint_as_float(s[1]));
  }
  const float c = 1.44269504088896341f;
  const f32x2 cp = {c, c}, cn = {-c, -c}, bx = {-mx * c, -mx * c}, bn = {mn * c, mn * c};
  f32x2 ep[8], en[8];
#pragma unroll
  for (int i = 0; i < 8; i++) {
    const f32x2 v = {p[2 * i], p[2 * i + 1]};
    const f32x2 a = __builtin_elementwise_fma(v, cp, bx), b = __builtin_elementwise_fma(v, cn, bn);
    ep[i] = f32x2{__builtin_amdgcn_exp2f(a[0]), __builtin_amdgcn_exp2f(a[1])};
    en[i] = f32x2{__builtin_amdgcn_exp2f(b[0]), __builtin_amdgcn_exp2f(b[1])};
  }
  const f32x2 s2p = ((ep[0] + ep[1]) + (ep[2] + ep[3])) + ((ep[4] + ep[5]) + (ep[6] + ep[7]));
  const f32x2 s2n = ((en[0] + en[1]) + (en[2] + en[3])) + ((en[4] + en[5]) + (en[6] + en[7]));
  const float rp = __builtin_amdgcn_rcpf(xh_add(s2p[0] + s2p[1])), rn = __builtin_amdgcn_rcpf(xh_add(s2n[0] + s2n[1]));
  const f32x2 r2p = {rp, rp}, r2n = {rn, rn};
#pragma unroll
  for (int i = 0; i < 8; i++) { pos[i] = pk2v(ep[i] * r2p); neg[i] = pk2v(en[i] * r2n); }
}
DI u32x4 tag4(const f32x4& v, unsigned T) {
  u32x4 r = {__builtin_amdgcn_perm(__float_as_uint(v[0]), T, 0x07060500u), __builtin_amdgcn_perm(__float_as_uint(v[1]), T, 0x07060501u),
             __builtin_amdgcn_perm(__float_as_uint(v[2]), T, 0x07060502u), __builtin_amdgcn_perm(__float_as_uint(v[3]), T, 0x07060503u)};
  return r;
}
DI bool tag_ok(const u32x4& g, unsigned T) {
  unsigned lo = __builtin_amdgcn_perm(g[1], g[0], 0x0c0c0400u), hi = __builtin_amdgcn_perm(g[3], g[2], 0x04000c0cu);
  return (lo | hi) == T;
}
template <int N> DI void gather(u32x4 (&g)[N], __amdgpu_buffer_rsrc_t r, unsigned a, unsigned stride, unsigned T) {
  unsigned off[N];
#pragma unroll
  for (int i = 0; i < N; i++) off[i] = a + i * stride;
#pragma unroll
  for (int i = 0; i < N; i++) g[i] = __builtin_amdgcn_raw_buffer_load_b128(r, off[i], 0, SC1);
#pragma unroll 1
  for (int sp = 0; sp < SPIN_CAP; sp++) {
    bool ok[N], all = true;
#pragma unroll
    for (int i = 0; i < N; i++) { ok[i] = tag_ok(g[i], T); all &= ok[i]; }
    if (__all(all)) break;
    __builtin_amdgcn_s_sleep(PSL);
    asm volatile("" ::: "memory");
    u32x4 n[N];
#pragma unroll
    for (int i = 0; i < N; i++) n[i] = __builtin_amdgcn_raw_buffer_load_b128(r, MASKED && ok[i] ? 0x80000000u : off[i], 0, SC1);
#pragma unroll
    for (int i = 0; i < N; i++) g[i] = MASKED && ok[i] ? g[i] : n[i];
  }
}
DI float untag(unsigned v) { return __uint_as_float(v & ~255u); }
DI unsigned epoch_tag(const char* ws) {
  unsigned e;
  asm volatile("s_load_dword %0, %1, 0x0\n\ts_waitcnt lgkmcnt(0)" : "=s"(e) : "s"(ws + W_E) : "memory");
  return e + 0x01010101u;
}

#define STAMP(i) do { if ((i) == XS && (w >= 4)) { asm volatile("s_memrealtime %0\n\ts_waitcnt lgkmcnt(0)" : "=s"(tx) :: "memory"); } } while (0)
#define STAMPV(i) STAMP(i)
#define STAMP_DUMP() do { while (__builtin_amdgcn_s_memrealtime() < tstart + 4000 + 4 * (tx - tstart)) __builtin_amdgcn_s_sleep(1); } while (0)

__global__ __launch_bounds__(512, 2) void hedgehog_fused(const u16* __restrict__ x, const u16* __restrict__ wqkv, const u16* __restrict__ wphi,
                                                         const u16* __restrict__ wo, u16* __restrict__ out, char* __restrict__ ws) {
  extern __shared__ __attribute__((aligned(16))) char smem[];
  u64 tstart = __builtin_amdgcn_s_memrealtime(); u64 tx = tstart;
  const int tid = threadIdx.x, lane = tid & 63;
  const int w = __builtin_amdgcn_readfirstlane(tid >> 6);
  const int b = blockIdx.x, xcd = b & 7, idx = b >> 3;
  const int rl = idx & 7, cl = idx >> 3;
#define CBX 0
  const int rb = (xcd & 1) * 8 + rl, cb = CBX == 0 ? (xcd >> 1) * 4 + cl : CBX == 1 ? ((xcd >> 1) ^ 1) * 4 + cl : CBX == 2 ? (xcd >> 1) + cl * 4 : (xcd >> 2) * 8 + ((xcd >> 1) & 1) * 2 + (cl & 1) + (cl >> 1) * 4;
  const int orb = (xcd >> 1) * 4 + (idx & 3), ocb = (xcd & 1) * 8 + (idx >> 2), orot = (idx >> 2) * 2;
  const unsigned sb = (unsigned)(size_t)(LAS char*)smem;
  const __amdgpu_buffer_rsrc_t rws = __builtin_amdgcn_make_buffer_rsrc(ws, 0, W_BYTES, 0x00020000);
  const int hh = lane >> 5, l31 = lane & 31, l15 = lane & 15, g4 = lane >> 4, q4 = l15 >> 2, p4 = lane & 3;
  unsigned st_r = 0, st_c = 0; (void)st_r; (void)st_c;
  asm volatile("" :: "s"(x), "s"(wqkv), "s"(wphi), "s"(wo), "s"(out), "s"(ws));
  STAMP(0);
  const __amdgpu_buffer_rsrc_t rx = __builtin_amdgcn_make_buffer_rsrc((void*)x, 0, 2048 * 1024 * 2, 0x00020000);
  const __amdgpu_buffer_rsrc_t rw = __builtin_amdgcn_make_buffer_rsrc((void*)wqkv, 0, 1024 * 3072 * 2, 0x00020000);
  const int lw = w & 3;
  const int turn = (rl ^ xcd) & 7;
  auto pull = [&](int s) {
    const int u = cl * 4 + lw;
    const unsigned vx = lane < 16 ? lane * 2048 : 0x80000000u;
    __builtin_amdgcn_raw_ptr_buffer_load_lds(rx, (LAS char*)smem + L_DUMMY, 4, vx, ((xcd & 1) * 1024 + ((xcd >> 1) * 16 + u) * 16) * 2048 + s * 128, 0, 0);
    const int l3 = lane / 3;
    const unsigned vw = lane < 18 ? ((xcd & 1) * 96 + u * 6 + l3) * 2048 + (lane - l3 * 3) * 128 : lane < 30 ? (u * 12 + lane - 18) * 2048 + 384 : 0x80000000u;
    __builtin_amdgcn_raw_ptr_buffer_load_lds(rw, (LAS char*)smem + L_DUMMY, 4, vw, (xcd >> 1) * 512 + s * 64 * 6144, 0, 0);
  };

  const int r8 = lane >> 3, c8 = lane & 7;
  const unsigned vx0 = r8 * 2048 + ((c8 ^ (r8 >> 1)) << 4), vx1 = r8 * 2048 + ((c8 ^ (4 + (r8 >> 1))) << 4);
  const unsigned vw0 = r8 * 6144 + ((c8 ^ (r8 & 2)) << 4), vw1 = r8 * 6144 + ((c8 ^ (4 + (r8 & 2))) << 4);
  const unsigned sx = (rb * 128 + lw * 32) * 2048, sw = cb * 128 + lw * 16 * 6144;
  auto stage = [&](int t, int slot) {
    LAS char* dst = (LAS char*)smem + slot * SLOT;
#define LX(i) __builtin_amdgcn_raw_ptr_buffer_load_lds(rx, dst + (lw * 4 + i) * 1024, 16, (i & 1) ? vx1 : vx0, sx + i * 16384 + t * 128, 0, 0)
#define LW(i) __builtin_amdgcn_raw_ptr_buffer_load_lds(rw, dst + 16384 + (i >> 1) * 8192 + (lw * 2 + (i & 1)) * 1024, 16, (i & 1) ? vw1 : vw0, \
                                                     sw + (i >> 1) * 2048 + (t * 64 + (i & 1) * 8) * 6144, 0, 0)
    LW(2); LX(0); LW(3); LX(1); LW(4); LX(2); LW(5); LX(3); LW(0); LW(1);
  };
  u64 pc; asm volatile("s_getpc_b64 %0" : "=s"(pc));
  if (w < 4) {
    STAMP(12);
    stage(0, 0);
    STAMP(13);
  }
  int role = w; asm volatile("" : "+s"(role));

  if (role < 4) {
    const __amdgpu_buffer_rsrc_t ro = __builtin_amdgcn_make_buffer_rsrc((void*)wo, 0, 1024 * 1024 * 2, 0x00020000);
    const bool early = VPF && turn >= IPN && turn <= VPF;
    if (early) pull(turn);
    stage(1, 1);
    STAMP(14);
    if (early) waitv<12>(); else waitv<10>();
    STAMP(15);
    bar0();
    stage(2, 2);
    {
      const int u = idx * 4 + lw;
      const char* c = (const char*)(pc & ~127ull) + (u < CODE_KB * 4 ? u : 0) * 256;
      const char* p0 = ws + u * 65536;
      const char* p1 = u < 64 ? (const char*)out + u * 65536 : u < 96 ? (const char*)wo + (u - 64) * 65536 : ws + W_PZ + (u & 7) * 65536;
      asm volatile("s_load_dwordx2 s[100:101], %0, 0x3c\n\ts_load_dwordx2 s[100:101], %0, 0xbc\n\ts_load_dword s100, %1, 0x0\n\ts_load_dword s100, %2, 0x0"
                   :: "s"(c), "s"(p0), "s"(p1) : "s100", "s101");
    }
    int slot = 0; bool pfd = false;
#pragma unroll 1
    for (int t = 1; t < 14; t++) {
      if (pfd) waitv<12>(); else waitv<10>();
      pfd = false;
      bar0();
      STAMPV(t + 16);
      if (t + VPF < 16 && ((t + VPF) & 7) == turn) { pull(t + VPF); pfd = true; }
      if (t == WPT + turn && turn < 2) {
        const unsigned u = ((turn * 8 + xcd) * 4 + cl) * 4 + lw;
        __builtin_amdgcn_raw_ptr_buffer_load_lds(ro, (LAS char*)smem + L_DUMMY, 4, l31 * 128, u * 8192, 0, 0);
        __builtin_amdgcn_raw_ptr_buffer_load_lds(ro, (LAS char*)smem + L_DUMMY, 4, l31 * 128, u * 8192 + 4096, 0, 0);
        pfd = true;
      }
      stage(t + 2, slot);
      slot = slot == 2 ? 0 : slot + 1;
    }
    const __amdgpu_buffer_rsrc_t rp = __builtin_amdgcn_make_buffer_rsrc((void*)wphi, 0, 32 * 1024 * 2, 0x00020000);
    __builtin_amdgcn_raw_ptr_buffer_load_lds(rp, (LAS char*)smem + L_PHI + lw * 1024, 16, lane * 16, cb * 4096 + lw * 1024, 0, 0);
    waitv<11>(); bar0();
    u32x4 wr_[32];
    const int wrot = WROT ? (OMAP == 0 ? rl * 4 : OMAP == 1 ? (idx & 3) * 8 : (idx & 1) * 16) : 0;
    const unsigned vo = r8 * 2048 + c8 * 16, so_ = lw * 256 * 2048 + ocb * 128;
#pragma unroll
    for (int i = 0; i < 8; i++) wr_[i] = __builtin_amdgcn_raw_buffer_load_b128(ro, vo, so_ + ((i + wrot) & 31) * 16384, 0);
    waitv<8>(); bar0();
#pragma unroll
    for (int i = 8; i < 32; i++) wr_[i] = __builtin_amdgcn_raw_buffer_load_b128(ro, vo, so_ + ((i + wrot) & 31) * 16384, 0);
    const unsigned T = epoch_tag(ws);
    if (tid == 0) *LP(unsigned, sb + L_CNT) = 0;
    STAMP(1);
    bar();
    {
      u32x4 g[4], gz[2];
      const unsigned pa = W_P + (cb * 16 + g4) * 16384 + rb * 1024 + (lw * 16 + l15) * 16;
      const unsigned za = W_PZ + (cb * 16 + r8) * 512 + lw * 128 + c8 * 16;
      bar0();
      STAMP(6);
      __builtin_amdgcn_s_setprio(HPRIO);
      __builtin_amdgcn_s_sleep(SL1);
      gather<4>(g, rws, pa, 65536, T);
      STAMP(7);
      f32x4 s;
#pragma unroll
      for (int j = 0; j < 4; j++) s[j] = xh_add(x16_add((untag(g[0][j]) + untag(g[1][j])) + (untag(g[2][j]) + untag(g[3][j]))));
      if (lane < 16) __builtin_amdgcn_raw_buffer_store_b128(tag4(s, T), rws, W_R + (cb * 16 + rb) * 1024 + (lw * 16 + lane) * 16, 0, SC1);
      STAMP(2);
      gather<2>(gz, rws, za, 4096, T);
#pragma unroll
      for (int j = 0; j < 4; j++) {
        float v = untag(gz[0][j]) + untag(gz[1][j]);
        v += __uint_as_float(__builtin_amdgcn_update_dpp(0u, __float_as_uint(v), 0x128, 0xf, 0xf, false));
        s[j] = xh_add(x16_add(v));
      }
      if (lane < 8) { u32x2 zz = {pk2(s[0], s[1]), pk2(s[2], s[3])};
        *LP(u32x2, sb + L_Z + lw * 64 + (lane & 1) * 32 + (lane >> 2) * 16 + ((lane >> 1) & 1) * 8) = zz; }
      STAMP(3);
      __builtin_amdgcn_s_sleep(SL2);
      gather<4>(g, rws, W_R + (cb * 16 + lw * 4) * 1024 + lane * 16, 1024, T);
#pragma unroll
      for (int st = 0; st < 2; st++) {
        u32x4 f = {pk2(untag(g[2 * st][0]), untag(g[2 * st][1])), pk2(untag(g[2 * st][2]), untag(g[2 * st][3])),
                   pk2(untag(g[2 * st + 1][0]), untag(g[2 * st + 1][1])), pk2(untag(g[2 * st + 1][2]), untag(g[2 * st + 1][3]))};
        *LP(u32x4, sb + L_KV + (lw * 2 + st) * 1024 + lane * 16) = f;
      }
    }
    STAMP(4);
    __builtin_amdgcn_s_setprio(0);
    bar();
    __builtin_amdgcn_s_sleep(SLW);
    {
      const unsigned da = sb + L_WO + lw * 32768 + r8 * 128 + ((c8 ^ (r8 & 6)) << 4);
#pragma unroll
      for (int i = 0; i < 32; i++) *LP(u32x4, da + ((i + wrot) & 31) * 1024) = wr_[i];
    }
    STAMP(5);
    if (w == 0) {
      __builtin_amdgcn_s_sleep(SL3);
      unsigned fa = lane < 16 ? W_F + (orb * 16 + lane) * 256 : 0x80000000u;
#pragma unroll 1
      for (int sp = 0; sp < SPIN_CAP; sp++) {
        const unsigned f = __builtin_amdgcn_raw_buffer_load_b32(rws, fa, 0, SC1);
        asm volatile("" ::: "memory");
        if (f == T) fa = 0x80000000u;
        if (__all(fa == 0x80000000u)) break;
        __builtin_amdgcn_s_sleep(PSL);
      }
      if (b == 0 && lane == 0) __builtin_amdgcn_raw_buffer_store_b32(T, rws, W_E, 0, SC1);
    }
  } else {
    const int wr = lw >> 1, wc = lw & 1;
    __builtin_amdgcn_s_waitcnt(0x0f70);
    if (lw < 2) {
      const unsigned vo = lw ? (b * 12 + (lane & 15)) * 128 + g4 * 64 * 6144 : (b * 8 + (lane & 7)) * 2048 + g4 * 128;
      const u16* base = lw ? wqkv : x;
      if (g4 < IPN && l15 < (lw ? 12 : 8)) {
        unsigned keep;
        asm volatile("s_mov_b32 %0, m0\n\ts_mov_b32 m0, %3\n\ts_nop 0\n\tglobal_load_lds_dword %1, %2\n\ts_mov_b32 m0, %0"
                     : "=&s"(keep) : "v"(vo), "s"(base), "s"(sb + L_DUMMY) : "memory");
      }
    }
    f32x4 acc[6][4] = {};
    unsigned xa[2];
#pragma unroll
    for (int ks = 0; ks < 2; ks++) xa[ks] = sb + (wr * 64 + l15) * 128 + (((4 * ks + g4) ^ (l15 >> 1)) << 4);
    unsigned wa[2];
#pragma unroll
    for (int nt = 0; nt < 2; nt++) wa[nt] = sb + 16384 + (8 * g4 + q4) * 128 + (((wc * 2 + nt) ^ ((q4 >> 1) | ((g4 & 1) << 1))) << 5) + p4 * 8;
    bf16x8 fx[2][4], fw[2][6];
    unsigned xs[2], wsl[2];
    auto slot_at = [&](unsigned so) {
      xs[0] = xa[0] + so; xs[1] = xa[1] + so; wsl[0] = wa[0] + so; wsl[1] = wa[1] + so;
      asm volatile("" : "+v"(xs[0]), "+v"(xs[1]), "+v"(wsl[0]), "+v"(wsl[1]));
    };
    auto rdw = [&](int buf, int j, int ks) {
      const unsigned a = wsl[j & 1] + (j < 4 ? 8192 + (j >> 1) * 8192 : 0) + ks * 4096;
      fw[buf][j] = lds_tr2(a, a + 512);
    };
    auto rd = [&](int buf, int ks, unsigned so, int j0, int j1) {
      if (ks == 0) slot_at(so);
      rdw(buf, j0, ks);
#pragma unroll
      for (int m = 0; m < 4; m++) fx[buf][m] = lds_b128(xs[ks] + m * 2048);
#pragma unroll
      for (int j = j0 + 1; j < j1; j++) rdw(buf, j, ks);
    };
    auto mm = [&](int buf, int j0, int j1) {
#pragma unroll
      for (int j = j0; j < j1; j++)
#pragma unroll
        for (int m0 = 0; m0 < 4; m0++) {
          const int m = (j & 1) ? 3 - m0 : m0;
          acc[j][m] = (j == 2 || j == 3) ? MFMA16(fx[buf][m], fw[buf][j], acc[j][m]) : MFMA16(fw[buf][j], fx[buf][m], acc[j][m]);
        }
    };
#define SCHED(nds, nmf) do { _Pragma("unroll") for (int i_ = 0; i_ < (nds); i_++) { __builtin_amdgcn_sched_group_barrier(0x8, 1, 0); __builtin_amdgcn_sched_group_barrier(0x100, 1, 0); } \
      if ((nmf) > (nds)) __builtin_amdgcn_sched_group_barrier(0x8, (nmf) - (nds), 0); __builtin_amdgcn_sched_barrier(0); } while (0)
#pragma unroll
    for (int j = 0; j < 6; j++) asm volatile("" : "+v"(acc[j][0]), "+v"(acc[j][1]), "+v"(acc[j][2]), "+v"(acc[j][3]));
    const unsigned T = epoch_tag(ws);
    bar();
    STAMP(1);
    unsigned so = 0;
    rd(0, 0, so, 0, 6);
    __builtin_amdgcn_sched_barrier(0);
#pragma unroll 1
    for (int t = 0; t < 12; t++) {
      rd(1, 1, so, 0, 6); mm(0, 0, 6); SCHED(16, 24);
      bar();
      so = so == 2 * SLOT ? 0 : so + SLOT;
      rd(0, 0, so, 0, 6); mm(1, 0, 6); SCHED(16, 24);
    }
    rd(1, 1, 0, 0, 6); mm(0, 0, 6); SCHED(16, 24);
    bar();
    rd(0, 0, SLOT, 0, 4); mm(1, 0, 6); SCHED(12, 24);
    rd(1, 1, SLOT, 0, 4); mm(0, 0, 4); SCHED(12, 16);
    bar();
    rd(0, 0, 2 * SLOT, 0, 4); mm(1, 0, 4); SCHED(12, 16);
    rd(1, 1, 2 * SLOT, 0, 4); mm(0, 0, 4); SCHED(12, 16);
    bar();
    rd(0, 0, 0, 0, 4); mm(1, 0, 4); SCHED(12, 16);
    rd(1, 1, 0, 0, 4); mm(0, 0, 4); SCHED(12, 16);
    bf16x8 wp[2];
    {
      const unsigned pa = sb + L_PHI + wc * 2048 + (8 * hh + q4) * 64 + (g4 & 1) * 32 + p4 * 8;
      wp[0] = lds_tr2(pa, pa + 256); wp[1] = lds_tr2(pa + 1024, pa + 1280);
    }
    mm(1, 0, 4);
    STAMP(2);
    const f32x16 zero = {};
    const unsigned img = sb + (1 + wc) * SLOT + 24576;
    u32x4 vf[2][2];
#pragma unroll
    for (int rt = 0; rt < 2; rt++) {
      f32x16 p = MFMA32(wp[0], pack16to32(acc[0][2 * rt], acc[0][2 * rt + 1]), zero);
      p = MFMA32(wp[1], pack16to32(acc[1][2 * rt], acc[1][2 * rt + 1]), p);
#pragma unroll
      for (int et = 0; et < 2; et++) {
        const f32x4 a = acc[2 + et][2 * rt], c = acc[2 + et][2 * rt + 1];
        vf[rt][et] = u32x4{pk2(a[0], a[1]), pk2(a[2], a[3]), pk2(c[0], c[1]), pk2(c[2], c[3])};
        *LP(u32x4, sb + L_V + lw * 4096 + (rt * 2 + et) * 1024 + lane * 16) = vf[rt][et];
      }
      unsigned ph[2][8];
      dual_softmax(p, ph[0], ph[1]);
      const int sw = ((l31 >> 1) & 1) | ((l31 >> 2) & 2) | (l31 & 4);
      const unsigned ra = img + (wr * 64 + rt * 32 + l31) * 64;
#pragma unroll
      for (int q = 0; q < 4; q++) {
        const unsigned a = ra + (((2 * q + hh) ^ sw) << 3);
        *LP(u32x2, a) = u32x2{ph[0][2 * q], ph[0][2 * q + 1]};
        *LP(u32x2, a + 8192) = u32x2{ph[1][2 * q], ph[1][2 * q + 1]};
      }
    }
    STAMP(3);
    bar();
    {
      u32x4 vo[2][2];
#pragma unroll
      for (int i = 0; i < 4; i++) vo[i >> 1][i & 1] = *LP(const u32x4, sb + L_V + (lw ^ 2) * 4096 + i * 1024 + lane * 16);
      const int srow = 4 * g4 + q4;
      const int sw = ((srow >> 1) & 1) | ((srow >> 2) & 2) | (srow & 4);
      bf16x8 pf[4][2];
#pragma unroll
      for (int ks = 0; ks < 4; ks++)
#pragma unroll
        for (int ft = 0; ft < 2; ft++) {
          const unsigned a = img + wr * 8192 + (((wr ^ (ks >> 1)) * 64 + (ks & 1) * 32 + srow) * 64) + (((4 * ft + p4) ^ sw) << 3);
          pf[ks][ft] = lds_tr2(a, a + 1024);
        }
      f32x4 kv[2][2] = {}, z[2] = {};
#pragma unroll
      for (int ks = 0; ks < 4; ks++)
#pragma unroll
        for (int ft = 0; ft < 2; ft++)
#pragma unroll
          for (int et = 0; et < 2; et++)
            kv[ft][et] = MFMA16(pf[ks][ft], __builtin_bit_cast(bf16x8, ks < 2 ? vf[ks & 1][et] : vo[ks & 1][et]), kv[ft][et]);
      const u32x4 one4 = {0x3f803f80u, 0x3f803f80u, 0x3f803f80u, 0x3f803f80u};
#pragma unroll
      for (int ks = 0; ks < 4; ks++)
#pragma unroll
        for (int ft = 0; ft < 2; ft++) z[ft] = MFMA16(pf[ks][ft], __builtin_bit_cast(bf16x8, one4), z[ft]);
      const int tl = wc * 2 + wr;
      const unsigned pa = W_P + (cb * 16 + rb) * 16384 + tl * 4096 + (g4 >> 1) * 1024 + ((g4 & 1) * 32 + l15) * 16;
#pragma unroll
      for (int ft = 0; ft < 2; ft++)
#pragma unroll
        for (int et = 0; et < 2; et++) __builtin_amdgcn_raw_buffer_store_b128(tag4(kv[ft][et], T), rws, pa + ft * 2048 + et * 256, 0, SC1);
      const f32x4 zs = (lane & 1) ? z[1] : z[0];
      if (l15 < 2) __builtin_amdgcn_raw_buffer_store_b128(tag4(zs, T), rws, W_PZ + (cb * 16 + rb) * 512 + tl * 128 + (l15 * 4 + g4) * 16, 0, SC1);
    }
    bar0();
    STAMP(4);
    __builtin_amdgcn_sched_barrier(0);
    rd(0, 0, SLOT, 4, 6);
    __builtin_amdgcn_sched_barrier(0);
    rd(1, 1, SLOT, 4, 6); mm(0, 4, 6); SCHED(8, 8);
    rd(0, 0, 2 * SLOT, 4, 6); mm(1, 4, 6); SCHED(8, 8);
    rd(1, 1, 2 * SLOT, 4, 6); mm(0, 4, 6); SCHED(8, 8);
    rd(0, 0, 0, 4, 6); mm(1, 4, 6); SCHED(8, 8);
    rd(1, 1, 0, 4, 6); mm(0, 4, 6); SCHED(8, 8);
    mm(1, 4, 6);
    u32x4 phq[2][2][2];
#pragma unroll
    for (int rt = 0; rt < 2; rt++) {
      f32x16 p = MFMA32(wp[0], pack16to32(acc[4][2 * rt], acc[4][2 * rt + 1]), zero);
      p = MFMA32(wp[1], pack16to32(acc[5][2 * rt], acc[5][2 * rt + 1]), p);
      unsigned ph[2][8];
      dual_softmax(p, ph[0], ph[1]);
#pragma unroll
      for (int sg = 0; sg < 2; sg++)
#pragma unroll
        for (int st = 0; st < 2; st++) phq[rt][sg][st] = u32x4{ph[sg][4 * st], ph[sg][4 * st + 1], ph[sg][4 * st + 2], ph[sg][4 * st + 3]};
      asm volatile("" : "+v"(phq[rt][0][0]), "+v"(phq[rt][0][1]), "+v"(phq[rt][1][0]), "+v"(phq[rt][1][1]));
    }
    STAMP(5);
    __builtin_amdgcn_s_setprio(2);
    bar();
    STAMP(6);
    bf16x8 kf[2][2]; u32x4 zf[2][2];
#pragma unroll
    for (int sg = 0; sg < 2; sg++)
#pragma unroll
      for (int st = 0; st < 2; st++) {
        kf[sg][st] = lds_b128(sb + L_KV + ((wc * 2 + sg) * 2 + st) * 1024 + lane * 16);
        zf[sg][st] = *LP(const u32x4, sb + L_Z + (wc * 2 + sg) * 64 + hh * 32 + st * 16);
      }
#pragma unroll
    for (int rt = 0; rt < 2; rt++) {
      f32x16 num = MFMA32(kf[0][0], __builtin_bit_cast(bf16x8, phq[rt][0][0]), zero);
      num = MFMA32(kf[0][1], __builtin_bit_cast(bf16x8, phq[rt][0][1]), num);
      num = MFMA32(kf[1][0], __builtin_bit_cast(bf16x8, phq[rt][1][0]), num);
      num = MFMA32(kf[1][1], __builtin_bit_cast(bf16x8, phq[rt][1][1]), num);
      float d0 = 0.f, d1 = 0.f;
#pragma unroll
      for (int st = 0; st < 2; st++)
#pragma unroll
        for (int j = 0; j < 4; j++) {
          d0 = dot2(phq[rt][0][st][j], zf[0][st][j], d0); d1 = dot2(phq[rt][1][st][j], zf[1][st][j], d1);
        }
      const float r = __builtin_amdgcn_rcpf(xh_add(d0 + d1) + 1e-6f);
      num *= r;
#pragma unroll
      for (int st = 0; st < 2; st++)
        __builtin_amdgcn_raw_buffer_store_b128(__builtin_bit_cast(u32x4, pack8(num, st)), rws,
                                               W_O + ((rb * 8 + wr * 4 + rt * 2 + (g4 & 1)) * 32 + cb * 2 + wc) * 1024 + (2 * st + hh) * 256 + l15 * 16, 0, SC1);
    }
    STAMP(7);
    waitv<0>();
    if (lane == 0) {
      const unsigned n = __hip_atomic_fetch_add(LP(unsigned, sb + L_CNT), 1u, __ATOMIC_RELAXED, __HIP_MEMORY_SCOPE_WORKGROUP);
      if (n == 3) __builtin_amdgcn_raw_buffer_store_b32(T, rws, W_F + (rb * 16 + cb) * 256, 0, SC1);
    }
    __builtin_amdgcn_s_setprio(GPRIO);
    STAMP(8);
  }
  bar();
  STAMP(9);
  {
    const int rg = w & 3, kh = w >> 2;
    const int kr = 16 * (g4 >> 1) + 4 * (g4 & 1) + q4;
    unsigned wa[4];
#pragma unroll
    for (int nt = 0; nt < 4; nt++) wa[nt] = sb + L_WO + kh * 65536 + kr * 128 + ((nt ^ ((kr >> 1) & 3)) << 5) + p4 * 8;
    const unsigned ob = W_O + ((orb * 8 + rg * 2) * 32 + kh * 16) * 1024;
    f32x4 c[2][4] = {};
    constexpr int PF = OPF;
    u32x4 of[PF][2];
    bf16x8 a[2][4];
    const unsigned v0 = lane * 16 + kh * 32768, v1 = lane * 16 + (kh ^ 1) * 32768;
    auto ldo = [&](int i) {
      const unsigned so = ob + ((i + orot) & 15) * 1024;
      of[i % PF][0] = __builtin_amdgcn_raw_buffer_load_b128(rws, v0, so, SC1);
      of[i % PF][1] = __builtin_amdgcn_raw_buffer_load_b128(rws, v1, so, SC1);
    };
    auto ldw = [&](int i) {
      const unsigned ko = ((i + orot) & 15) * 4096;
#pragma unroll
      for (int nt = 0; nt < 4; nt++) a[i & 1][nt] = lds_tr2(wa[nt] + ko, wa[nt] + ko + 1024);
    };
#pragma unroll
    for (int i = 0; i < PF - 1; i++) ldo(i);
    ldw(0);
    __builtin_amdgcn_sched_barrier(0);
#pragma unroll
    for (int i = 0; i < 16; i++) {
      if (i + PF - 1 < 16) ldo(i + PF - 1);
      __builtin_amdgcn_sched_barrier(0);
      if (i < 15) ldw(i + 1);
      const bf16x8 b0 = __builtin_bit_cast(bf16x8, of[i % PF][0]), b1 = __builtin_bit_cast(bf16x8, of[i % PF][1]);
#pragma unroll
      for (int nt = 0; nt < 4; nt++) { c[0][nt] = MFMA16(a[i & 1][nt], b0, c[0][nt]); c[1][nt] = MFMA16(a[i & 1][nt], b1, c[1][nt]); }
#pragma unroll
      for (int q = 0; q < 8; q++) { __builtin_amdgcn_sched_group_barrier(0x8, 1, 0); __builtin_amdgcn_sched_group_barrier(0x100, 1, 0); }
      __builtin_amdgcn_sched_barrier(0);
    }
    STAMP(10);
#pragma unroll
    for (int nt = 0; nt < 4; nt++) *LP(f32x4, sb + L_FX + w * 4096 + nt * 1024 + lane * 16) = c[1][nt];
    bar();
    unsigned pk[8];
#pragma unroll
    for (int nt = 0; nt < 4; nt++) {
      const f32x4 v = *LP(const f32x4, sb + L_FX + (w ^ 4) * 4096 + nt * 1024 + lane * 16) + c[0][nt];
      pk[2 * nt] = pk2(v[0], v[1]); pk[2 * nt + 1] = pk2(v[2], v[3]);
    }
    const __amdgpu_buffer_rsrc_t rout = __builtin_amdgcn_make_buffer_rsrc(out, 0, 2048 * 1024 * 2, 0x00020000);
    const unsigned oa = (orb * 128 + rg * 32 + kh * 16 + l15) * 2048 + ocb * 128 + (g4 & 1) * 32 + (g4 >> 1) * 16;
#pragma unroll
    for (int nt = 0; nt < 4; nt += 2) {
      auto r0 = __builtin_amdgcn_permlane16_swap(pk[2 * nt], pk[2 * nt + 2], false, false);
      auto r1 = __builtin_amdgcn_permlane16_swap(pk[2 * nt + 1], pk[2 * nt + 3], false, false);
      u32x4 v = {r0[0], r1[0], r0[1], r1[1]};
      __builtin_amdgcn_raw_buffer_store_b128(v, rout, oa + nt * 32, 0, OSC);
    }
    STAMP(11);
    STAMP_DUMP();
  }
}

extern "C" void kernel_launch(void* const* d_in, const int* in_sizes, int n_in, void* d_out, int out_size, void* d_ws, size_t ws_size,
                              hipStream_t stream) {
  (void)hipFuncSetAttribute((const void*)hedgehog_fused, hipFuncAttributeMaxDynamicSharedMemorySize, LDS_BYTES);
  hedgehog_fused<<<256, 512, LDS_BYTES, stream>>>((const u16*)d_in[0], (const u16*)d_in[1], (const u16*)d_in[2], (const u16*)d_in[3],
                                                   (u16*)d_out, (char*)d_ws);
}
```

```cpp
#define WSO -10
#include <hip/hip_runtime.h>
#include <hip/hip_bf16.h>

typedef unsigned short u16;
typedef unsigned long long u64;
typedef __attribute__((ext_vector_type(8))) __bf16 bf16x8;
typedef __attribute__((ext_vector_type(4))) __bf16 bf16x4;
typedef __attribute__((ext_vector_type(2))) __bf16 bf16x2;
typedef __attribute__((ext_vector_type(16))) float f32x16;
typedef __attribute__((ext_vector_type(4))) float f32x4;
typedef __attribute__((ext_vector_type(2))) float f32x2;
typedef __attribute__((ext_vector_type(4))) unsigned u32x4;
typedef __attribute__((ext_vector_type(2))) unsigned u32x2;
#define LAS __attribute__((address_space(3)))
#define LP(T, a) ((LAS T*)(size_t)(a))
#define DI __device__ __forceinline__
#define MFMA32(a, b, c) __builtin_amdgcn_mfma_f32_32x32x16_bf16((a), (b), (c), 0, 0, 0)
#define MFMA16(a, b, c) __builtin_amdgcn_mfma_f32_16x16x32_bf16((a), (b), (c), 0, 0, 0)
#define SC1 16

#define ROT 0
#define SPF 0
#define OMAP 1
#define CODE_KB 22
#define OPF 2
#define PR 1
#define VPF 3
#define IPF 1
#define IPN 2
#define WPF 1
#define WROT 1
#define Y2 1
#define SL1 18
#define MASKED 0
#define SL2 0
#define SL3 0
#define HPRIO 0
#define WPT 9
#define IPD 0
#define CODE_BACK 4
#define PSL 2
#define TPF 1
#define GPRIO 0
#define SLW 2
#define OLAG 2
#define VTP 2
#define FTR 1
#define HINT 0
#define HW1 0
#define SLH 0
#define FSL 2
#define OSC 16

constexpr int SLOT = 40960;
constexpr int L_WO = 0;
constexpr int L_V = 122880;
constexpr int L_KV = 139264;
constexpr int L_PHI = 147456;
constexpr int L_Z = 151552;
constexpr int L_CNT = 151808;
constexpr int L_DUMMY = 152064;
constexpr int L_ZF = 151824;
constexpr int L_FX = 131072;
constexpr int LDS_BYTES = 163840;
constexpr unsigned W_O = 0;
constexpr unsigned W_P = 4u << 20;
constexpr unsigned W_PZ = 8u << 20;
constexpr unsigned W_R = W_PZ + (128u << 10);
constexpr unsigned W_F = W_R + (256u << 10);
constexpr unsigned W_H = W_F + (64u << 10);
constexpr unsigned W_E = W_H + (64u << 10);
constexpr unsigned W_TS = 9u  __H2__ ;
constexpr unsigned W_BYTES = 10u << 20;
constexpr int SPIN_CAP = 1 << 18;

DI void bar() { asm volatile("s_waitcnt lgkmcnt(0)\n\ts_barrier" ::: "memory"); }
DI void bar0() { asm volatile("s_barrier" ::: "memory"); }
template <int N> DI void waitv() { asm volatile("s_waitcnt vmcnt(%0)" ::"i"(N) : "memory"); }
DI unsigned pk2(float a, float b) { f32x2 v = {a, b}; return __builtin_bit_cast(unsigned, __builtin_convertvector(v, bf16x2)); }
DI unsigned pk2v(f32x2 v) { return __builtin_bit_cast(unsigned, __builtin_convertvector(v, bf16x2)); }
DI bf16x8 pack8(const f32x16& x, int s) {
  u32x4 p = {pk2(x[8 * s], x[8 * s + 1]), pk2(x[8 * s + 2], x[8 * s + 3]), pk2(x[8 * s + 4], x[8 * s + 5]), pk2(x[8 * s + 6], x[8 * s + 7])};
  return __builtin_bit_cast(bf16x8, p);
}
DI bf16x8 pack16to32(const f32x4& c0, const f32x4& c1) {
  auto a = __builtin_amdgcn_permlane16_swap(pk2(c0[0], c0[1]), pk2(c1[0], c1[1]), false, false);
  auto b = __builtin_amdgcn_permlane16_swap(pk2(c0[2], c0[3]), pk2(c1[2], c1[3]), false, false);
  u32x4 p = {a[0], b[0], a[1], b[1]};
  return __builtin_bit_cast(bf16x8, p);
}
DI float max3(float a, float b, float c) { float r; asm("v_max3_f32 %0, %1, %2, %3" : "=v"(r) : "v"(a), "v"(b), "v"(c)); return r; }
DI float min3(float a, float b, float c) { float r; asm("v_min3_f32 %0, %1, %2, %3" : "=v"(r) : "v"(a), "v"(b), "v"(c)); return r; }
DI float xh_add(float a) { auto r = __builtin_amdgcn_permlane32_swap(__float_as_uint(a), __float_as_uint(a), false, false); return __uint_as_float(r[0]) + __uint_as_float(r[1]); }
DI float x16_add(float a) { auto r = __builtin_amdgcn_permlane16_swap(__float_as_uint(a), __float_as_uint(a), false, false); return __uint_as_float(r[0]) + __uint_as_float(r[1]); }
DI float dot2(unsigned a, unsigned b, float c) { return __builtin_amdgcn_fdot2_f32_bf16(__builtin_bit_cast(bf16x2, a), __builtin_bit_cast(bf16x2, b), c, false); }
DI bf16x8 lds_b128(unsigned a) { return *LP(const bf16x8, a); }
DI bf16x8 lds_tr2(unsigned a0, unsigned a1) {
  bf16x4 lo = __builtin_amdgcn_ds_read_tr16_b64_v4bf16(LP(bf16x4, a0));
  bf16x4 hi = __builtin_amdgcn_ds_read_tr16_b64_v4bf16(LP(bf16x4, a1));
  return __builtin_shufflevector(lo, hi, 0, 1, 2, 3, 4, 5, 6, 7);
}
DI void dual_softmax(const f32x16& p, unsigned (&pos)[8], unsigned (&neg)[8]) {
#define FMX __builtin_elementwise_maximum
#define FMN __builtin_elementwise_minimum
  float mx = FMX(FMX(p[0], p[1]), p[2]), mn = FMN(FMN(p[0], p[1]), p[2]), mx2 = FMX(FMX(p[3], p[4]), p[5]), mn2 = FMN(FMN(p[3], p[4]), p[5]);
#pragma unroll
  for (int i = 6; i < 14; i += 4) {
    mx = FMX(FMX(mx, p[i]), p[i + 1]); mn = FMN(FMN(mn, p[i]), p[i + 1]);
    mx2 = FMX(FMX(mx2, p[i + 2]), p[i + 3]); mn2 = FMN(FMN(mn2, p[i + 2]), p[i + 3]);
  }
  mx = FMX(FMX(mx, p[14]), p[15]); mn = FMN(FMN(mn, p[14]), p[15]);
  mx = FMX(mx, mx2); mn = FMN(mn, mn2);
  {
    auto r = __builtin_amdgcn_permlane32_swap(__float_as_uint(mx), __float_as_uint(mx), false, false);
    auto s = __builtin_amdgcn_permlane32_swap(__float_as_uint(mn), __float_as_uint(mn), false, false);
    mx = FMX(__uint_as_float(r[0]), __uint_as_float(r[1])); mn = FMN(__uint_as_float(s[0]), __uint_as_float(s[1]));
  }
  const float c = 1.44269504088896341f;
  const f32x2 cp = {c, c}, cn = {-c, -c}, bx = {-mx * c, -mx * c}, bn = {mn * c, mn * c};
  f32x2 ep[8], en[8];
#pragma unroll
  for (int i = 0; i < 8; i++) {
    const f32x2 v = {p[2 * i], p[2 * i + 1]};
    const f32x2 a = __builtin_elementwise_fma(v, cp, bx), b = __builtin_elementwise_fma(v, cn, bn);
    ep[i] = f32x2{__builtin_amdgcn_exp2f(a[0]), __builtin_amdgcn_exp2f(a[1])};
    en[i] = f32x2{__builtin_amdgcn_exp2f(b[0]), __builtin_amdgcn_exp2f(b[1])};
  }
  const f32x2 s2p = ((ep[0] + ep[1]) + (ep[2] + ep[3])) + ((ep[4] + ep[5]) + (ep[6] + ep[7]));
  const f32x2 s2n = ((en[0] + en[1]) + (en[2] + en[3])) + ((en[4] + en[5]) + (en[6] + en[7]));
  const float rp = __builtin_amdgcn_rcpf(xh_add(s2p[0] + s2p[1])), rn = __builtin_amdgcn_rcpf(xh_add(s2n[0] + s2n[1]));
  const f32x2 r2p = {rp, rp}, r2n = {rn, rn};
#pragma unroll
  for (int i = 0; i < 8; i++) { pos[i] = pk2v(ep[i] * r2p); neg[i] = pk2v(en[i] * r2n); }
}
DI u32x4 tag4(const f32x4& v, unsigned T) {
  u32x4 r = {__builtin_amdgcn_perm(__float_as_uint(v[0]), T, 0x07060500u), __builtin_amdgcn_perm(__float_as_uint(v[1]), T, 0x07060501u),
             __builtin_amdgcn_perm(__float_as_uint(v[2]), T, 0x07060502u), __builtin_amdgcn_perm(__float_as_uint(v[3]), T, 0x07060503u)};
  return r;
}
DI bool tag_ok(const u32x4& g, unsigned T) {
  unsigned lo = __builtin_amdgcn_perm(g[1], g[0], 0x0c0c0400u), hi = __builtin_amdgcn_perm(g[3], g[2], 0x04000c0cu);
  return (lo | hi) == T;
}
template <int N> DI void gather(u32x4 (&g)[N], __amdgpu_buffer_rsrc_t r, unsigned a, unsigned stride, unsigned T) {
  unsigned off[N];
#pragma unroll
  for (int i = 0; i < N; i++) off[i] = a + i * stride;
#pragma unroll
  for (int i = 0; i < N; i++) g[i] = __builtin_amdgcn_raw_buffer_load_b128(r, off[i], 0, SC1);
#pragma unroll 1
  for (int sp = 0; sp < SPIN_CAP; sp++) {
    bool ok[N], all = true;
#pragma unroll
    for (int i = 0; i < N; i++) { ok[i] = tag_ok(g[i], T); all &= ok[i]; }
    if (__all(all)) break;
    __builtin_amdgcn_s_sleep(PSL);
    asm volatile("" ::: "memory");
    u32x4 n[N];
#pragma unroll
    for (int i = 0; i < N; i++) n[i] = __builtin_amdgcn_raw_buffer_load_b128(r, MASKED && ok[i] ? 0x80000000u : off[i], 0, SC1);
#pragma unroll
    for (int i = 0; i < N; i++) g[i] = MASKED && ok[i] ? g[i] : n[i];
  }
}
DI void wait16(__amdgpu_buffer_rsrc_t r, unsigned a, unsigned T, int lane) {
  unsigned fa = lane < 16 ? a + lane * 256 : 0x80000000u;
#pragma unroll 1
  for (int sp = 0; sp < SPIN_CAP; sp++) {
    const unsigned f = __builtin_amdgcn_raw_buffer_load_b32(r, fa, 0, SC1);
    asm volatile("" ::: "memory");
    if (f == T) fa = 0x80000000u;
    if (__all(fa == 0x80000000u)) break;
    __builtin_amdgcn_s_sleep(FSL);
  }
}
DI float untag(unsigned v) { return __uint_as_float(v & ~255u); }
DI unsigned epoch_tag(const char* ws) {
  unsigned e;
  asm volatile("s_load_dword %0, %1, 0x0\n\ts_waitcnt lgkmcnt(0)" : "=s"(e) : "s"(ws + W_E) : "memory");
  return e + 0x01010101u;
}

#define STAMP(i)
#define STAMPV(i)
#define STAMP_DUMP()

__global__ __launch_bounds__(512, 2) void hedgehog_fused(const u16* __restrict__ x, const u16* __restrict__ wqkv, const u16* __restrict__ wphi,
                                                         const u16* __restrict__ wo, u16* __restrict__ out, char* __restrict__ ws) {
  extern __shared__ __attribute__((aligned(16))) char smem[];
  const int tid = threadIdx.x, lane = tid & 63;
  const int w = __builtin_amdgcn_readfirstlane(tid >> 6);
  const int b = blockIdx.x, xcd = b & 7, idx = b >> 3;
  const int rl = idx & 7, cl = idx >> 3;
#define CBX 0
  const int rb = (xcd & 1) * 8 + rl, cb = CBX == 0 ? (xcd >> 1) * 4 + cl : CBX == 1 ? ((xcd >> 1) ^ 1) * 4 + cl : CBX == 2 ? (xcd >> 1) + cl * 4 : (xcd >> 2) * 8 + ((xcd >> 1) & 1) * 2 + (cl & 1) + (cl >> 1) * 4;
  const int orb = (xcd >> 1) * 4 + (idx & 3), ocb = (xcd & 1) * 8 + (idx >> 2), orot = (idx >> 2) * 2;
  const unsigned sb = (unsigned)(size_t)(LAS char*)smem;
  const __amdgpu_buffer_rsrc_t rws = __builtin_amdgcn_make_buffer_rsrc(ws, 0, W_BYTES, 0x00020000);
  const int hh = lane >> 5, l31 = lane & 31, l15 = lane & 15, g4 = lane >> 4, q4 = l15 >> 2, p4 = lane & 3;
  unsigned st_r = 0, st_c = 0; (void)st_r; (void)st_c;
  asm volatile("" :: "s"(x), "s"(wqkv), "s"(wphi), "s"(wo), "s"(out), "s"(ws));
  STAMP(0);
  const __amdgpu_buffer_rsrc_t rx = __builtin_amdgcn_make_buffer_rsrc((void*)x, 0, 2048 * 1024 * 2, 0x00020000);
  const __amdgpu_buffer_rsrc_t rw = __builtin_amdgcn_make_buffer_rsrc((void*)wqkv, 0, 1024 * 3072 * 2, 0x00020000);
  const int lw = w & 3;
  const int turn = (rl ^ xcd) & 7;
  auto pull = [&](int s) {
    const int u = cl * 4 + lw;
    const unsigned vx = lane < 16 ? lane * 2048 : 0x80000000u;
    __builtin_amdgcn_raw_ptr_buffer_load_lds(rx, (LAS char*)smem + L_DUMMY, 4, vx, ((xcd & 1) * 1024 + ((xcd >> 1) * 16 + u) * 16) * 2048 + s * 128, 0, 0);
    const int l3 = lane / 3;
    const unsigned vw = lane < 18 ? ((xcd & 1) * 96 + u * 6 + l3) * 2048 + (lane - l3 * 3) * 128 : lane < 30 ? (u * 12 + lane - 18) * 2048 + 384 : 0x80000000u;
    __builtin_amdgcn_raw_ptr_buffer_load_lds(rw, (LAS char*)smem + L_DUMMY, 4, vw, (xcd >> 1) * 512 + s * 64 * 6144, 0, 0);
  };

  const int r8 = lane >> 3, c8 = lane & 7;
  const unsigned vx0 = r8 * 2048 + ((c8 ^ (r8 >> 1)) << 4), vx1 = r8 * 2048 + ((c8 ^ (4 + (r8 >> 1))) << 4);
  const unsigned vw0 = r8 * 6144 + ((c8 ^ (r8 & 2)) << 4), vw1 = r8 * 6144 + ((c8 ^ (4 + (r8 & 2))) << 4);
  const unsigned sx = (rb * 128 + lw * 32) * 2048, sw = cb * 128 + lw * 16 * 6144;
  auto stage = [&](int t, int slot) {
    LAS char* dst = (LAS char*)smem + slot * SLOT;
#define LX(i) __builtin_amdgcn_raw_ptr_buffer_load_lds(rx, dst + (lw * 4 + i) * 1024, 16, (i & 1) ? vx1 : vx0, sx + i * 16384 + t * 128, 0, 0)
#define LW(i) __builtin_amdgcn_raw_ptr_buffer_load_lds(rw, dst + 16384 + (i >> 1) * 8192 + (lw * 2 + (i & 1)) * 1024, 16, (i & 1) ? vw1 : vw0, \
                                                     sw + (i >> 1) * 2048 + (t * 64 + (i & 1) * 8) * 6144, 0, 0)
    LW(2); LX(0); LW(3); LX(1); LW(4); LX(2); LW(5); LX(3); LW(0); LW(1);
  };
  u64 pc; asm volatile("s_getpc_b64 %0" : "=s"(pc));
  if (w < 4) {
    STAMP(12);
    stage(0, 0);
    STAMP(13);
  }
  int role = w; asm volatile("" : "+s"(role));

  if (role < 4) {
    const __amdgpu_buffer_rsrc_t ro = __builtin_amdgcn_make_buffer_rsrc((void*)wo, 0, 1024 * 1024 * 2, 0x00020000);
    const bool early = VPF && turn >= IPN && turn <= VPF;
    if (early) pull(turn);
    stage(1, 1);
    STAMP(14);
    if (early) waitv<12>(); else waitv<10>();
    STAMP(15);
    bar0();
    stage(2, 2);
    {
      const int u = idx * 4 + lw;
      const char* c = (const char*)(pc & ~127ull) + (u < CODE_KB * 4 ? u : 0) * 256;
      const char* p0 = ws + u * 65536;
      const char* p1 = u < 64 ? (const char*)out + u * 65536 : u < 96 ? (const char*)wo + (u - 64) * 65536 : ws + W_PZ + (u & 7) * 65536;
      asm volatile("s_load_dwordx2 s[100:101], %0, 0x3c\n\ts_load_dwordx2 s[100:101], %0, 0xbc\n\ts_load_dword s100, %1, 0x0\n\ts_load_dword s100, %2, 0x0"
                   :: "s"(c), "s"(p0), "s"(p1) : "s100", "s101");
    }
    int slot = 0; bool pfd = false;
#pragma unroll 1
    for (int t = 1; t < 14; t++) {
      if (pfd) waitv<12>(); else waitv<10>();
      pfd = false;
      bar0();
      STAMPV(t + 16);
      if (t + VPF < 16 && ((t + VPF) & 7) == turn) { pull(t + VPF); pfd = true; }
      if (t == WPT + turn && turn < 2) {
        const unsigned u = ((turn * 8 + xcd) * 4 + cl) * 4 + lw;
        __builtin_amdgcn_raw_ptr_buffer_load_lds(ro, (LAS char*)smem + L_DUMMY, 4, l31 * 128, u * 8192, 0, 0);
        __builtin_amdgcn_raw_ptr_buffer_load_lds(ro, (LAS char*)smem + L_DUMMY, 4, l31 * 128, u * 8192 + 4096, 0, 0);
        pfd = true;
      }
      stage(t + 2, slot);
      slot = slot == 2 ? 0 : slot + 1;
    }
    const __amdgpu_buffer_rsrc_t rp = __builtin_amdgcn_make_buffer_rsrc((void*)wphi, 0, 32 * 1024 * 2, 0x00020000);
    __builtin_amdgcn_raw_ptr_buffer_load_lds(rp, (LAS char*)smem + L_PHI + lw * 1024, 16, lane * 16, cb * 4096 + lw * 1024, 0, 0);
    waitv<11>(); bar0();
    u32x4 wr_[32];
    const int wrot = WROT ? (OMAP == 0 ? rl * 4 : OMAP == 1 ? (idx & 3) * 8 : (idx & 1) * 16) : 0;
    const unsigned vo = r8 * 2048 + c8 * 16, so_ = lw * 256 * 2048 + ocb * 128;
#pragma unroll
    for (int i = 0; i < 8; i++) wr_[i] = __builtin_amdgcn_raw_buffer_load_b128(ro, vo, so_ + ((i + wrot) & 31) * 16384, 0);
    waitv<8>(); bar0();
#pragma unroll
    for (int i = 8; i < 32; i++) wr_[i] = __builtin_amdgcn_raw_buffer_load_b128(ro, vo, so_ + ((i + wrot) & 31) * 16384, 0);
    if (lw < 2) {
      const unsigned q = lane & 3, r = lane >> 2;
      const unsigned vt = lw ? (lane < 4 ? (orb * 128 + q * 32) * 2048 + ocb * 128 : 0x80000000u)
                             : r == 0 ? W_P + cb * 262144 + q * 65536 : r == 1 ? W_O + orb * 262144 + q * 65536 : r == 2 ? W_O + rb * 262144 + q * 65536 + cb * 2048
                             : r == 3 ? (q == 0 ? W_PZ + (cb * 16 + rb) * 512 : q == 1 ? W_R + (cb * 16 + rb) * 1024 : W_F + q * 65536 - 131072 + 128) : 0x80000000u;
      __builtin_amdgcn_raw_ptr_buffer_load_lds(lw ? __builtin_amdgcn_make_buffer_rsrc(out, 0, 2048 * 1024 * 2, 0x00020000) : rws, (LAS char*)smem + L_DUMMY, 4,
                                               (VTP == 1 || q == 0 || r == 3) ? vt : 0x80000000u, 0, 0, 0);
    }
    const unsigned T = epoch_tag(ws);
    if (lane == 0) { *LP(unsigned, sb + L_CNT) = 0; *LP(unsigned, sb + L_ZF + lw * 4) = ~T; *LP(unsigned, sb + L_ZF + 16) = ~T; }
    {
      const int srow = 4 * g4 + q4;
      const int sw = ((srow >> 1) & 1) | ((srow >> 2) & 2) | (srow & 4);
      unsigned ia[2], va = sb + L_V + (lw >> 1) * 4096 + lane * 16;
#pragma unroll
      for (int ft = 0; ft < 2; ft++) ia[ft] = sb + (1 + (lw >> 1)) * SLOT + 24576 + (lw & 1) * 8192 + srow * 64 + (((4 * ft + p4) ^ sw) << 3);
      unsigned pa = W_P + (cb * 16 + rb) * 16384 + lw * 4096 + (g4 >> 1) * 1024 + ((g4 & 1) * 32 + l15) * 16;
      unsigned za = l15 < 2 ? W_PZ + (cb * 16 + rb) * 512 + lw * 128 + (l15 * 4 + g4) * 16 : 0x80000000u;
      u32x4 one4 = {0x3f803f80u, 0x3f803f80u, 0x3f803f80u, 0x3f803f80u};
      f32x4 kv[2][2] = {}, z[2] = {};
      unsigned ha = tid == 0 ? W_H + (cb * 16 + rb) * 256 : 0x80000000u, Tv = T;
      asm volatile("" : "+v"(ia[0]), "+v"(ia[1]), "+v"(va), "+v"(pa), "+v"(za), "+v"(one4), "+v"(ha), "+v"(Tv));
      STAMP(1);
      bar();
      bf16x8 vf[2][2][2];
#pragma unroll
      for (int i = 0; i < 8; i++) vf[i >> 2][(i >> 1) & 1][i & 1] = lds_b128(va + (i >> 2) * 8192 + (i & 3) * 1024);
#pragma unroll
      for (int rt = 0; rt < 2; rt++) {
        if (rt) {
          asm volatile("" : "+v"(kv[0][0]), "+v"(kv[0][1]), "+v"(kv[1][0]), "+v"(kv[1][1]), "+v"(z[0]), "+v"(z[1]));
          __builtin_amdgcn_s_setprio(3);
          bar();
          if (HINT == 1) __builtin_amdgcn_raw_buffer_store_b32(Tv, rws, ha, 0, SC1);
        }
        bf16x8 pf[2][2];
#pragma unroll
        for (int i = 0; i < 4; i++) { const unsigned a = ia[i & 1] + (i >> 1) * 4096 + rt * 2048; pf[i >> 1][i & 1] = lds_tr2(a, a + 1024); }
        __builtin_amdgcn_sched_barrier(0);
#pragma unroll
        for (int h = 0; h < 2; h++)
#pragma unroll
          for (int ft = 0; ft < 2; ft++)
#pragma unroll
            for (int et = 0; et < 2; et++) kv[ft][et] = MFMA16(pf[h][ft], vf[h][rt][et], kv[ft][et]);
        __builtin_amdgcn_sched_barrier(0);
#pragma unroll
        for (int h = 0; h < 2; h++)
#pragma unroll
          for (int ft = 0; ft < 2; ft++) z[ft] = MFMA16(pf[h][ft], __builtin_bit_cast(bf16x8, one4), z[ft]);
        __builtin_amdgcn_sched_barrier(0);
      }
      if (HINT == 2) __builtin_amdgcn_raw_buffer_store_b32(Tv, rws, ha, 0, SC1);
#pragma unroll
      for (int ft = 0; ft < 2; ft++)
#pragma unroll
        for (int et = 0; et < 2; et++) __builtin_amdgcn_raw_buffer_store_b128(tag4(kv[ft][et], T), rws, pa + ft * 2048 + et * 256, 0, SC1);
      f32x4 zs;
#pragma unroll
      for (int j = 0; j < 4; j++) zs[j] = (lane & 1) ? z[1][j] : z[0][j];
      __builtin_amdgcn_raw_buffer_store_b128(tag4(zs, T), rws, za, 0, SC1);
      if (HINT == 3) __builtin_amdgcn_raw_buffer_store_b32(Tv, rws, ha, 0, SC1);
    }
    {
      u32x4 g[4], gz[2];
      const unsigned pa = W_P + (cb * 16 + g4) * 16384 + rb * 1024 + (lw * 16 + l15) * 16;
      const unsigned za = W_PZ + (cb * 16 + r8) * 512 + lw * 128 + c8 * 16;
      STAMP(6);
      __builtin_amdgcn_s_sleep(SL1);
      gather<4>(g, rws, pa, 65536, T);
      STAMP(7);
      f32x4 s;
#pragma unroll
      for (int j = 0; j < 4; j++) s[j] = xh_add(x16_add((untag(g[0][j]) + untag(g[1][j])) + (untag(g[2][j]) + untag(g[3][j]))));
      if (lane < 16) __builtin_amdgcn_raw_buffer_store_b128(tag4(s, T), rws, W_R + (cb * 16 + rb) * 1024 + (lw * 16 + lane) * 16, 0, SC1);
      STAMP(2);
      gather<2>(gz, rws, za, 4096, T);
#pragma unroll
      for (int j = 0; j < 4; j++) {
        float v = untag(gz[0][j]) + untag(gz[1][j]);
        v += __uint_as_float(__builtin_amdgcn_update_dpp(0u, __float_as_uint(v), 0x128, 0xf, 0xf, false));
        s[j] = xh_add(x16_add(v));
      }
      if (lane < 8) { u32x2 zz = {pk2(s[0], s[1]), pk2(s[2], s[3])};
        *LP(u32x2, sb + L_Z + lw * 64 + (lane & 1) * 32 + (lane >> 2) * 16 + ((lane >> 1) & 1) * 8) = zz; }
      asm volatile("" ::: "memory");
      if (lane == 0) *LP(volatile unsigned, sb + L_ZF + lw * 4) = T;
      STAMP(3);
      __builtin_amdgcn_s_sleep(SL2);
      gather<4>(g, rws, W_R + (cb * 16 + lw * 4) * 1024 + lane * 16, 1024, T);
#pragma unroll
      for (int st = 0; st < 2; st++) {
        u32x4 f = {pk2(untag(g[2 * st][0]), untag(g[2 * st][1])), pk2(untag(g[2 * st][2]), untag(g[2 * st][3])),
                   pk2(untag(g[2 * st + 1][0]), untag(g[2 * st + 1][1])), pk2(untag(g[2 * st + 1][2]), untag(g[2 * st + 1][3]))};
        *LP(u32x4, sb + L_KV + (lw * 2 + st) * 1024 + lane * 16) = f;
      }
    }
    STAMP(4);
    __builtin_amdgcn_s_setprio(0);
    bar();
    __builtin_amdgcn_s_sleep(SLW);
    {
      const unsigned da = sb + L_WO + lw * 32768 + r8 * 128 + ((c8 ^ (r8 & 6)) << 4);
#pragma unroll
      for (int i = 0; i < 32; i++) *LP(u32x4, da + ((i + wrot) & 31) * 1024) = wr_[i];
    }
    STAMP(5);
    if (w == 0) {
      __builtin_amdgcn_s_sleep(SL3);
      wait16(rws, W_F + orb * 4096, T, lane);
      if (b == 0 && lane == 0) __builtin_amdgcn_raw_buffer_store_b32(T, rws, W_E, 0, SC1);
    }
  } else {
    const int wr = lw >> 1, wc = lw & 1;
    __builtin_amdgcn_s_waitcnt(0x0f70);
    if (lw < 2) {
      const unsigned vo = lw ? (b * 12 + (lane & 15)) * 128 + g4 * 64 * 6144 : (b * 8 + (lane & 7)) * 2048 + g4 * 128;
      const u16* base = lw ? wqkv : x;
      if (g4 < IPN && l15 < (lw ? 12 : 8)) {
        unsigned keep;
        asm volatile("s_mov_b32 %0, m0\n\ts_mov_b32 m0, %3\n\ts_nop 0\n\tglobal_load_lds_dword %1, %2\n\ts_mov_b32 m0, %0"
                     : "=&s"(keep) : "v"(vo), "s"(base), "s"(sb + L_DUMMY) : "memory");
      }
    }
    f32x4 acc[6][4] = {};
    unsigned xa[2];
#pragma unroll
    for (int ks = 0; ks < 2; ks++) xa[ks] = sb + (wr * 64 + l15) * 128 + (((4 * ks + g4) ^ (l15 >> 1)) << 4);
    unsigned wa[2];
#pragma unroll
    for (int nt = 0; nt < 2; nt++) wa[nt] = sb + 16384 + (8 * g4 + q4) * 128 + (((wc * 2 + nt) ^ ((q4 >> 1) | ((g4 & 1) << 1))) << 5) + p4 * 8;
    bf16x8 fx[2][4], fw[2][6];
    unsigned xs[2], wsl[2];
    auto slot_at = [&](unsigned so) {
      xs[0] = xa[0] + so; xs[1] = xa[1] + so; wsl[0] = wa[0] + so; wsl[1] = wa[1] + so;
      asm volatile("" : "+v"(xs[0]), "+v"(xs[1]), "+v"(wsl[0]), "+v"(wsl[1]));
    };
    auto rdw = [&](int buf, int j, int ks) {
      const unsigned a = wsl[j & 1] + (j < 4 ? 8192 + (j >> 1) * 8192 : 0) + ks * 4096;
      fw[buf][j] = lds_tr2(a, a + 512);
    };
    auto rd = [&](int buf, int ks, unsigned so, int j0, int j1) {
      if (ks == 0) slot_at(so);
      rdw(buf, j0, ks);
#pragma unroll
      for (int m = 0; m < 4; m++) fx[buf][m] = lds_b128(xs[ks] + m * 2048);
#pragma unroll
      for (int j = j0 + 1; j < j1; j++) rdw(buf, j, ks);
    };
    auto mm = [&](int buf, int j0, int j1) {
#pragma unroll
      for (int j = j0; j < j1; j++)
#pragma unroll
        for (int m0 = 0; m0 < 4; m0++) {
          const int m = (j & 1) ? 3 - m0 : m0;
          acc[j][m] = (j == 2 || j == 3) ? MFMA16(fx[buf][m], fw[buf][j], acc[j][m]) : MFMA16(fw[buf][j], fx[buf][m], acc[j][m]);
        }
    };
#define SCHED(nds, nmf) do { _Pragma("unroll") for (int i_ = 0; i_ < (nds); i_++) { __builtin_amdgcn_sched_group_barrier(0x8, 1, 0); __builtin_amdgcn_sched_group_barrier(0x100, 1, 0); } \
      if ((nmf) > (nds)) __builtin_amdgcn_sched_group_barrier(0x8, (nmf) - (nds), 0); __builtin_amdgcn_sched_barrier(0); } while (0)
#pragma unroll
    for (int j = 0; j < 6; j++) asm volatile("" : "+v"(acc[j][0]), "+v"(acc[j][1]), "+v"(acc[j][2]), "+v"(acc[j][3]));
    unsigned pia[4];
    {
      const int sw = ((l31 >> 1) & 1) | ((l31 >> 2) & 2) | (l31 & 4);
#pragma unroll
      for (int q = 0; q < 4; q++) pia[q] = sb + (1 + wc) * SLOT + 24576 + (wr * 64 + l31) * 64 + (((2 * q + hh) ^ sw) << 3);
      asm volatile("" : "+v"(pia[0]), "+v"(pia[1]), "+v"(pia[2]), "+v"(pia[3]));
    }
    const unsigned T = epoch_tag(ws);
    bar();
    STAMP(1);
    unsigned so = 0;
    rd(0, 0, so, 0, 6);
    __builtin_amdgcn_sched_barrier(0);
#pragma unroll 1
    for (int t = 0; t < 12; t++) {
      rd(1, 1, so, 0, 6); mm(0, 0, 6); SCHED(16, 24);
      bar();
      so = so == 2 * SLOT ? 0 : so + SLOT;
      rd(0, 0, so, 0, 6); mm(1, 0, 6); SCHED(16, 24);
    }
    rd(1, 1, 0, 0, 6); mm(0, 0, 6); SCHED(16, 24);
    bar();
    rd(0, 0, SLOT, 0, 4); mm(1, 0, 6); SCHED(12, 24);
    rd(1, 1, SLOT, 0, 4); mm(0, 0, 4); SCHED(12, 16);
    bar();
    rd(0, 0, 2 * SLOT, 0, 4); mm(1, 0, 4); SCHED(12, 16);
    rd(1, 1, 2 * SLOT, 0, 4); mm(0, 0, 4); SCHED(12, 16);
    bar();
    rd(0, 0, 0, 0, 4); mm(1, 0, 4); SCHED(12, 16);
    rd(1, 1, 0, 0, 4); mm(0, 0, 4); SCHED(12, 16);
    bf16x8 wp[2];
    {
      const unsigned pa = sb + L_PHI + wc * 2048 + (8 * hh + q4) * 64 + (g4 & 1) * 32 + p4 * 8;
      wp[0] = lds_tr2(pa, pa + 256); wp[1] = lds_tr2(pa + 1024, pa + 1280);
    }
    mm(1, 0, 4);
    STAMP(2);
    const f32x16 zero = {};
    f32x16 pk[2];
#pragma unroll
    for (int rt = 0; rt < 2; rt++) {
      pk[rt] = MFMA32(wp[0], pack16to32(acc[0][2 * rt], acc[0][2 * rt + 1]), zero);
      pk[rt] = MFMA32(wp[1], pack16to32(acc[1][2 * rt], acc[1][2 * rt + 1]), pk[rt]);
    }
#pragma unroll
    for (int rt = 0; rt < 2; rt++)
#pragma unroll
      for (int et = 0; et < 2; et++) {
        const f32x4 a = acc[2 + et][2 * rt], c = acc[2 + et][2 * rt + 1];
        *LP(u32x4, sb + L_V + lw * 4096 + (rt * 2 + et) * 1024 + lane * 16) = u32x4{pk2(a[0], a[1]), pk2(a[2], a[3]), pk2(c[0], c[1]), pk2(c[2], c[3])};
      }
#pragma unroll
    for (int rt = 0; rt < 2; rt++) {
      unsigned ph[2][8];
      dual_softmax(pk[rt], ph[0], ph[1]);
#pragma unroll
      for (int q = 0; q < 4; q++) {
        *LP(u32x2, pia[q] + rt * 2048) = u32x2{ph[0][2 * q], ph[0][2 * q + 1]};
        *LP(u32x2, pia[q] + rt * 2048 + 8192) = u32x2{ph[1][2 * q], ph[1][2 * q + 1]};
      }
      if (rt) STAMP(3);
      bar();
    }
    STAMP(4);
    __builtin_amdgcn_sched_barrier(0);
    rd(0, 0, SLOT, 4, 6);
    __builtin_amdgcn_sched_barrier(0);
    rd(1, 1, SLOT, 4, 6); mm(0, 4, 6); SCHED(8, 8);
    rd(0, 0, 2 * SLOT, 4, 6); mm(1, 4, 6); SCHED(8, 8);
    rd(1, 1, 2 * SLOT, 4, 6); mm(0, 4, 6); SCHED(8, 8);
    rd(0, 0, 0, 4, 6); mm(1, 4, 6); SCHED(8, 8);
    rd(1, 1, 0, 4, 6); mm(0, 4, 6); SCHED(8, 8);
    mm(1, 4, 6);
    u32x4 phq[2][2][2];
#pragma unroll
    for (int rt = 0; rt < 2; rt++) {
      f32x16 p = MFMA32(wp[0], pack16to32(acc[4][2 * rt], acc[4][2 * rt + 1]), zero);
      p = MFMA32(wp[1], pack16to32(acc[5][2 * rt], acc[5][2 * rt + 1]), p);
      unsigned ph[2][8];
      dual_softmax(p, ph[0], ph[1]);
#pragma unroll
      for (int sg = 0; sg < 2; sg++)
#pragma unroll
        for (int st = 0; st < 2; st++) phq[rt][sg][st] = u32x4{ph[sg][4 * st], ph[sg][4 * st + 1], ph[sg][4 * st + 2], ph[sg][4 * st + 3]};
      asm volatile("" : "+v"(phq[rt][0][0]), "+v"(phq[rt][0][1]), "+v"(phq[rt][1][0]), "+v"(phq[rt][1][1]));
    }
    STAMP(5);
#pragma unroll 1
    for (int sp = 0; sp < SPIN_CAP * 8; sp++) {
      const u32x2 f = *LP(const volatile u32x2, sb + L_ZF + wc * 8);
      if (__builtin_amdgcn_readfirstlane(f[0] == T && f[1] == T)) break;
      __builtin_amdgcn_s_sleep(1);
    }
    asm volatile("" ::: "memory");
    float rden[2];
    {
      u32x4 zf[2][2];
#pragma unroll
      for (int i = 0; i < 4; i++) zf[i >> 1][i & 1] = *LP(const u32x4, sb + L_Z + (wc * 2 + (i >> 1)) * 64 + hh * 32 + (i & 1) * 16);
#pragma unroll
      for (int rt = 0; rt < 2; rt++) {
        float d0 = 0.f, d1 = 0.f;
#pragma unroll
        for (int st = 0; st < 2; st++)
#pragma unroll
          for (int j = 0; j < 4; j++) { d0 = dot2(phq[rt][0][st][j], zf[0][st][j], d0); d1 = dot2(phq[rt][1][st][j], zf[1][st][j], d1); }
        rden[rt] = __builtin_amdgcn_rcpf(xh_add(d0 + d1) + 1e-6f);
      }
    }
    unsigned ka = sb + L_KV + wc * 4096 + lane * 16, ca = sb + L_CNT, one = 1;
    unsigned oa = W_O + ((rb * 8 + wr * 4 + (g4 & 1)) * 32 + cb * 2 + wc) * 1024 + hh * 256 + l15 * 16, oa1 = oa + 65536;
    unsigned fa = lane == 0 ? W_F + (rb * 16 + cb) * 256 : 0x80000000u, Tv = T;
    asm volatile("" : "+v"(rden[0]), "+v"(rden[1]), "+v"(ka), "+v"(oa), "+v"(oa1), "+v"(ca), "+v"(one), "+v"(fa), "+v"(Tv));
    STAMP(16);
    __builtin_amdgcn_s_setprio(2);
    bar();
    STAMP(6);
    {
      bf16x8 kf[4];
#pragma unroll
      for (int i = 0; i < 4; i++) kf[i] = lds_b128(ka + i * 1024);
#define SB __builtin_amdgcn_sched_barrier(0)
#define PQ(rt, i) __builtin_bit_cast(bf16x8, phq[rt][(i) >> 1][(i) & 1])
      auto put = [&](const f32x16& n, float r, int st, unsigned a) {
        const f32x2 r2 = {r, r};
        u32x4 v;
#pragma unroll
        for (int j = 0; j < 4; j++) v[j] = pk2v(f32x2{n[8 * st + 2 * j], n[8 * st + 2 * j + 1]} * r2);
        __builtin_amdgcn_raw_buffer_store_b128(v, rws, a + st * 512, 0, SC1);
      };
      SB;
      f32x16 n0 = MFMA32(kf[0], PQ(0, 0), zero); SB;
      n0 = MFMA32(kf[1], PQ(0, 1), n0); SB; n0 = MFMA32(kf[2], PQ(0, 2), n0); SB; n0 = MFMA32(kf[3], PQ(0, 3), n0); SB;
      f32x16 n1 = MFMA32(kf[0], PQ(1, 0), zero); SB;
      n1 = MFMA32(kf[1], PQ(1, 1), n1); SB; n1 = MFMA32(kf[2], PQ(1, 2), n1); SB;
      put(n0, rden[0], 0, oa); SB;
      n1 = MFMA32(kf[3], PQ(1, 3), n1); SB;
      put(n0, rden[0], 1, oa); SB;
      put(n1, rden[1], 0, oa1); SB;
      put(n1, rden[1], 1, oa1); SB;
    }
    STAMP(7);
    {
      unsigned n;
      asm volatile("s_mov_b64 exec, 1\n\ts_waitcnt vmcnt(0)\n\tds_add_rtn_u32 %0, %1, %2\n\ts_mov_b64 exec, -1\n\ts_waitcnt lgkmcnt(0)"
                   : "=&v"(n) : "v"(ca), "v"(one) : "memory");
      if (__builtin_amdgcn_readfirstlane(n) == 3) __builtin_amdgcn_raw_buffer_store_b32(Tv, rws, fa, 0, SC1);
    }
    __builtin_amdgcn_s_setprio(GPRIO);
    STAMP(8);
  }
  {
    const int rg = w & 3, kh = w >> 2;
    const int kr = 16 * (g4 >> 1) + 4 * (g4 & 1) + q4;
    unsigned wa[4];
#pragma unroll
    for (int nt = 0; nt < 4; nt++) wa[nt] = sb + L_WO + kh * 65536 + kr * 128 + ((nt ^ ((kr >> 1) & 3)) << 5) + p4 * 8;
    const unsigned ob = W_O + ((orb * 8 + rg * 2) * 32 + kh * 16) * 1024;
    constexpr int LAG = OLAG, PF = OPF;
    f32x4 c0[4] = {}, c1[4] = {}, px[4];
    u32x4 o0[PF + 1], o1[PF + 1];
    bf16x8 a[LAG + 2][4];
    unsigned v0 = lane * 16 + kh * 32768, v1 = lane * 16 + (kh ^ 1) * 32768;
    unsigned xw = sb + L_FX + w * 4096 + lane * 16, xr = sb + L_FX + (w ^ 4) * 4096 + lane * 16;
    unsigned oa = (orb * 128 + rg * 32 + kh * 16 + r8) * 2048 + ocb * 128 + c8 * 16;
    unsigned tw = sb + L_WO + w * 2048 + l15 * 128 + (((g4 >> 1) ^ (l15 & 7)) << 4) + (((g4 & 1) ^ (l15 >> 3)) << 3);
    unsigned tr = sb + L_WO + w * 2048 + r8 * 128 + ((c8 ^ r8) << 4);
    asm volatile("" : "+v"(tw), "+v"(tr));
    asm volatile("" : "+v"(wa[0]), "+v"(wa[1]), "+v"(wa[2]), "+v"(wa[3]), "+v"(v0), "+v"(v1), "+v"(xw), "+v"(xr), "+v"(oa));
    asm volatile("" : "+v"(c0[0]), "+v"(c0[1]), "+v"(c0[2]), "+v"(c0[3]), "+v"(c1[0]), "+v"(c1[1]), "+v"(c1[2]), "+v"(c1[3]));
    auto ldo = [&](int i) {
      if (i < 16) o1[i % (PF + 1)] = __builtin_amdgcn_raw_buffer_load_b128(rws, v1, ob + i * 1024, SC1);
      if (i >= LAG && i < 16 + LAG) o0[i % (PF + 1)] = __builtin_amdgcn_raw_buffer_load_b128(rws, v0, ob + (i - LAG) * 1024, SC1);
    };
    auto ldw = [&](int i) {
#pragma unroll
      for (int nt = 0; nt < 4; nt++) a[i % (LAG + 2)][nt] = lds_tr2(wa[nt] + i * 4096, wa[nt] + i * 4096 + 1024);
    };
    bar();
    STAMP(9);
#pragma unroll
    for (int i = 0; i < PF; i++) ldo(i);
    ldw(0);
    __builtin_amdgcn_sched_barrier(0);
#pragma unroll
    for (int i = 0; i < 16 + LAG; i++) {
      ldo(i + PF);
      __builtin_amdgcn_sched_barrier(0);
      if (i < 15) ldw(i + 1);
      if (i < 16) {
#pragma unroll
        for (int nt = 0; nt < 4; nt++) c1[nt] = MFMA16(a[i % (LAG + 2)][nt], __builtin_bit_cast(bf16x8, o1[i % (PF + 1)]), c1[nt]);
      }
      if (i >= LAG) {
#pragma unroll
        for (int nt = 0; nt < 4; nt++) c0[nt] = MFMA16(a[(i - LAG) % (LAG + 2)][nt], __builtin_bit_cast(bf16x8, o0[i % (PF + 1)]), c0[nt]);
      }
#pragma unroll
      for (int q = 0; q < 8; q++) { __builtin_amdgcn_sched_group_barrier(0x8, 1, 0); __builtin_amdgcn_sched_group_barrier(0x100, 1, 0); }
      __builtin_amdgcn_sched_barrier(0);
      if (i == 15) {
        STAMP(10);
#pragma unroll
        for (int nt = 0; nt < 4; nt++) *LP(f32x4, xw + nt * 1024) = c1[nt];
      }
      if (i == (LAG ? 14 + LAG : 15)) {
        bar();
#pragma unroll
        for (int nt = 0; nt < 4; nt++) px[nt] = *LP(const f32x4, xr + nt * 1024);
        __builtin_amdgcn_sched_barrier(0);
      }
    }
    const __amdgpu_buffer_rsrc_t rout = __builtin_amdgcn_make_buffer_rsrc(out, 0, 2048 * 1024 * 2, 0x00020000);
#pragma unroll
    for (int nt = 0; nt < 4; nt++) {
      const f32x4 v = px[nt] + c0[nt];
      *LP(u32x2, tw ^ (nt << 5)) = u32x2{pk2(v[0], v[1]), pk2(v[2], v[3])};
    }
    asm volatile("" ::: "memory");
    const u32x4 t0 = *LP(const u32x4, tr), t1 = *LP(const u32x4, tr + 1024);
    __builtin_amdgcn_raw_buffer_store_b128(t0, rout, oa, 0, OSC);
    __builtin_amdgcn_raw_buffer_store_b128(u32x4{t1[2], t1[3], t1[0], t1[1]}, rout, oa, 16384, OSC);
    STAMP(11);
    STAMP(30);
    STAMP_DUMP();
  }
}

extern "C" void kernel_launch(void* const* d_in, const int* in_sizes, int n_in, void* d_out, int out_size, void* d_ws, size_t ws_size,
                              hipStream_t stream) {
  const size_t ws_off = WSO < 0 ? (ws_size - (size_t)(-WSO) * (1u << 20)) & ~(size_t)((2u << 20) - 1) : (size_t)WSO << 20;
  (void)hipFuncSetAttribute((const void*)hedgehog_fused, hipFuncAttributeMaxDynamicSharedMemorySize, LDS_BYTES);
  hedgehog_fused<<<256, 512, LDS_BYTES, stream>>>((const u16*)d_in[0], (const u16*)d_in[1], (const u16*)d_in[2], (const u16*)d_in[3],
                                                   (u16*)d_out, (char*)d_ws + ws_off);
}
```

```cpp
#define WN1 20
#define WN2 26
#define D1 230
#define D2 85
#define D3 130
#include <hip/hip_runtime.h>
#include <hip/hip_bf16.h>

typedef unsigned short u16;
typedef unsigned long long u64;
typedef __attribute__((ext_vector_type(8))) __bf16 bf16x8;
typedef __attribute__((ext_vector_type(4))) __bf16 bf16x4;
typedef __attribute__((ext_vector_type(2))) __bf16 bf16x2;
typedef __attribute__((ext_vector_type(16))) float f32x16;
typedef __attribute__((ext_vector_type(4))) float f32x4;
typedef __attribute__((ext_vector_type(2))) float f32x2;
typedef __attribute__((ext_vector_type(4))) unsigned u32x4;
typedef __attribute__((ext_vector_type(2))) unsigned u32x2;
typedef __amdgpu_buffer_rsrc_t rsrc_t;
#define LAS __attribute__((address_space(3)))
#define LP(T, a) ((LAS T*)(size_t)(a))
#define DI __device__ __forceinline__
#define MFMA32(a, b, c) __builtin_amdgcn_mfma_f32_32x32x16_bf16((a), (b), (c), 0, 0, 0)
#define MFMA16(a, b, c) __builtin_amdgcn_mfma_f32_16x16x32_bf16((a), (b), (c), 0, 0, 0)
#define SB __builtin_amdgcn_sched_barrier(0)
#define BF8(v) __builtin_bit_cast(bf16x8, v)
constexpr int SC1 = 16;
constexpr unsigned OOB = 0x80000000u;

#define VPF 3
#define IPN 2
#define WPT 9
#define PSL 2
#define HSL 8
#define OLAG 2
#define OPF 2
#define WSO -10
#define CL0 1
#define SLW 3
#define WN0 4
#define CLO 0
#define WI1 6
#define WI2 10

constexpr int SLOT = 40960;
constexpr int L_WO = 0;
constexpr int L_V = 122880;
constexpr int L_PHI = 147456;
constexpr int L_Z = 151552;
constexpr int L_CNT = 151808;
constexpr int L_KV = 139264;
constexpr int L_DUMMY = 152064;
constexpr int L_FX = 131072;
constexpr int LDS_BYTES = 163840;
constexpr unsigned W_O = 0;
constexpr unsigned W_P = 4u << 20;
constexpr unsigned W_PZ = 6u << 20;
constexpr unsigned W_R = W_PZ + (128u << 10);
constexpr unsigned W_H1 = W_R + (128u << 10);
constexpr unsigned W_H2 = W_H1 + (64u << 10);
constexpr unsigned W_F = W_H2 + (64u << 10);
constexpr unsigned W_E = W_F + (64u << 10);
constexpr unsigned W_TS = 9u << 20;
constexpr unsigned W_BYTES = 10u << 20;
constexpr int SPIN_CAP = 1 << 18;

DI void bar() { asm volatile("s_waitcnt lgkmcnt(0)\n\ts_barrier" ::: "memory"); }
DI void bar0() { asm volatile("s_barrier" ::: "memory"); }
template <int N> DI void waitv() { asm volatile("s_waitcnt vmcnt(%0)" ::"i"(N) : "memory"); }
DI unsigned pk2v(f32x2 v) { return __builtin_bit_cast(unsigned, __builtin_convertvector(v, bf16x2)); }
DI unsigned pk2(float a, float b) { return pk2v(f32x2{a, b}); }
DI bf16x8 pack16to32(const f32x4& c0, const f32x4& c1) {
  auto a = __builtin_amdgcn_permlane16_swap(pk2(c0[0], c0[1]), pk2(c1[0], c1[1]), false, false);
  auto b = __builtin_amdgcn_permlane16_swap(pk2(c0[2], c0[3]), pk2(c1[2], c1[3]), false, false);
  return BF8((u32x4{a[0], b[0], a[1], b[1]}));
}
DI float fold32(float a, float b) { auto r = __builtin_amdgcn_permlane32_swap(__float_as_uint(a), __float_as_uint(b), false, false); return __uint_as_float(r[0]) + __uint_as_float(r[1]); }
DI float fold16(float a, float b) { auto r = __builtin_amdgcn_permlane16_swap(__float_as_uint(a), __float_as_uint(b), false, false); return __uint_as_float(r[0]) + __uint_as_float(r[1]); }
DI float fold8(float a) { return a + __uint_as_float(__builtin_amdgcn_update_dpp(0u, __float_as_uint(a), 0x128, 0xf, 0xf, false)); }
DI float dot2(unsigned a, unsigned b, float c) { return __builtin_amdgcn_fdot2_f32_bf16(__builtin_bit_cast(bf16x2, a), __builtin_bit_cast(bf16x2, b), c, false); }
DI bf16x8 lds_b128(unsigned a) { return *LP(const bf16x8, a); }
DI bf16x8 lds_tr2(unsigned a0, unsigned a1) {
  bf16x4 lo = __builtin_amdgcn_ds_read_tr16_b64_v4bf16(LP(bf16x4, a0));
  bf16x4 hi = __builtin_amdgcn_ds_read_tr16_b64_v4bf16(LP(bf16x4, a1));
  return __builtin_shufflevector(lo, hi, 0, 1, 2, 3, 4, 5, 6, 7);
}
DI void dual_softmax(const f32x16& p, unsigned (&pos)[8], unsigned (&neg)[8]) {
#define FMX __builtin_elementwise_maximum
#define FMN __builtin_elementwise_minimum
  float mx = FMX(FMX(p[0], p[1]), p[2]), mn = FMN(FMN(p[0], p[1]), p[2]), mx2 = FMX(FMX(p[3], p[4]), p[5]), mn2 = FMN(FMN(p[3], p[4]), p[5]);
#pragma unroll
  for (int i = 6; i < 14; i += 4) {
    mx = FMX(FMX(mx, p[i]), p[i + 1]); mn = FMN(FMN(mn, p[i]), p[i + 1]);
    mx2 = FMX(FMX(mx2, p[i + 2]), p[i + 3]); mn2 = FMN(FMN(mn2, p[i + 2]), p[i + 3]);
  }
  mx = FMX(FMX(mx, p[14]), p[15]); mn = FMN(FMN(mn, p[14]), p[15]);
  mx = FMX(mx, mx2); mn = FMN(mn, mn2);
  {
    auto r = __builtin_amdgcn_permlane32_swap(__float_as_uint(mx), __float_as_uint(mx), false, false);
    auto s = __builtin_amdgcn_permlane32_swap(__float_as_uint(mn), __float_as_uint(mn), false, false);
    mx = FMX(__uint_as_float(r[0]), __uint_as_float(r[1])); mn = FMN(__uint_as_float(s[0]), __uint_as_float(s[1]));
  }
  const float c = 1.44269504088896341f;
  const f32x2 cp = {c, c}, cn = {-c, -c}, bx = {-mx * c, -mx * c}, bn = {mn * c, mn * c};
  f32x2 ep[8], en[8];
#pragma unroll
  for (int i = 0; i < 8; i++) {
    const f32x2 v = {p[2 * i], p[2 * i + 1]};
    const f32x2 a = __builtin_elementwise_fma(v, cp, bx), b = __builtin_elementwise_fma(v, cn, bn);
    ep[i] = f32x2{__builtin_amdgcn_exp2f(a[0]), __builtin_amdgcn_exp2f(a[1])};
    en[i] = f32x2{__builtin_amdgcn_exp2f(b[0]), __builtin_amdgcn_exp2f(b[1])};
  }
  const f32x2 s2p = ((ep[0] + ep[1]) + (ep[2] + ep[3])) + ((ep[4] + ep[5]) + (ep[6] + ep[7]));
  const f32x2 s2n = ((en[0] + en[1]) + (en[2] + en[3])) + ((en[4] + en[5]) + (en[6] + en[7]));
  const float sp = s2p[0] + s2p[1], sn = s2n[0] + s2n[1];
  const float rp = __builtin_amdgcn_rcpf(fold32(sp, sp)), rn = __builtin_amdgcn_rcpf(fold32(sn, sn));
  const f32x2 r2p = {rp, rp}, r2n = {rn, rn};
#pragma unroll
  for (int i = 0; i < 8; i++) { pos[i] = pk2v(ep[i] * r2p); neg[i] = pk2v(en[i] * r2n); }
}

constexpr unsigned EMPTY = 0xffffffffu;
DI unsigned seal(unsigned v) { return min(v, EMPTY - 1u); }
DI u32x4 seal4(u32x4 v) { return u32x4{seal(v[0]), seal(v[1]), seal(v[2]), seal(v[3])}; }
DI unsigned umax4(unsigned m, u32x4 v) { return max(max(max(max(m, v[0]), v[1]), v[2]), v[3]); }
template <int N> DI int settle(u32x4 (&g)[N], rsrc_t r, unsigned a, unsigned stride) {
  auto pending = [&]() { unsigned m = 0;
#pragma unroll
    for (int i = 0; i < N; i++) m = umax4(m, g[i]);
    return __any(m == EMPTY); };
  if (__builtin_expect(!pending(), 1)) return 0;
  int sp = 1;
#pragma unroll 1
  for (; sp < SPIN_CAP; sp++) {
    __builtin_amdgcn_s_sleep(PSL);
    asm volatile("" ::: "memory");
#pragma unroll
    for (int i = 0; i < N; i++) g[i] = __builtin_amdgcn_raw_buffer_load_b128(r, a + i * stride, 0, SC1);
    if (!pending()) break;
  }
  return sp;
}
DI unsigned now32() { u64 t; asm volatile("s_memrealtime %0\n\ts_waitcnt lgkmcnt(0)" : "=s"(t) :: "memory"); return (unsigned)t; }
DI void wait_until(unsigned t) {
#pragma unroll 1
  for (int sp = 0; sp < 1000; sp++) { if (t - now32() - 1u >= 1000u) break; }
}
DI int latest(u32x2 h, rsrc_t r, unsigned a, unsigned t0, unsigned T) {
  int d = -0x10000;
  if (a != OOB && h[1] == T) { d = (int)(h[0] - t0); a = OOB; }
  if (!__all(a == OOB)) {
#pragma unroll 1
    for (int sp = 0; sp < SPIN_CAP; sp++) {
      asm volatile("" ::: "memory");
      h = __builtin_amdgcn_raw_buffer_load_b64(r, a, 0, SC1);
      if (a != OOB && h[1] == T) { d = (int)(h[0] - t0); a = OOB; }
      if (__all(a == OOB)) break;
      __builtin_amdgcn_s_sleep(PSL);
    }
  }
  d = max(d, __builtin_amdgcn_update_dpp(d, d, 0x128, 0xf, 0xf, false));
  d = max(d, __builtin_amdgcn_update_dpp(d, d, 0x124, 0xf, 0xf, false));
  d = max(d, __builtin_amdgcn_update_dpp(d, d, 0x122, 0xf, 0xf, false));
  d = max(d, __builtin_amdgcn_update_dpp(d, d, 0x121, 0xf, 0xf, false));
  asm volatile("" ::: "memory");
  return __builtin_amdgcn_readfirstlane(d);
}
DI void count_in(unsigned lds_addr) {
  asm volatile("s_mov_b64 exec, 1\n\tds_add_u32 %0, %1\n\ts_mov_b64 exec, -1" :: "v"(lds_addr), "v"(1u) : "memory");
}
DI void lds_wait(unsigned lds_addr, unsigned n) {
#pragma unroll 1
  for (int sp = 0; sp < SPIN_CAP * 8; sp++) { if (__builtin_amdgcn_readfirstlane(*LP(const volatile unsigned, lds_addr)) >= n) break; __builtin_amdgcn_s_sleep(1); }
  asm volatile("" ::: "memory");
}
DI unsigned epoch_tag(const char* ws) {
  unsigned e;
  asm volatile("s_load_dword %0, %1, 0x0\n\ts_waitcnt lgkmcnt(0)" : "=s"(e) : "s"(ws + W_E) : "memory");
  return e + 1u;
}

#define STAMP(i)
#define STAMPV(i)
#define RETRY(i) retry_
#define STAMP_DUMP()

__global__ __launch_bounds__(512, 2) void hedgehog_fused(const u16* __restrict__ x, const u16* __restrict__ wqkv, const u16* __restrict__ wphi,
                                                         const u16* __restrict__ wo, u16* __restrict__ out, char* __restrict__ ws) {
  extern __shared__ __attribute__((aligned(16))) char smem[];
  const int tid = threadIdx.x, lane = tid & 63;
  const int w = __builtin_amdgcn_readfirstlane(tid >> 6), lw = w & 3;
  const int b = blockIdx.x, xcd = b & 7, idx = b >> 3;
  const int rl = idx & 7, cl = idx >> 3;
  const int rb = (xcd & 1) * 8 + rl, cb = (xcd >> 1) * 4 + cl;
  const int orb = (xcd >> 1) * 4 + (idx & 3), ocb = (xcd & 1) * 8 + (idx >> 2);
  const unsigned sb = (unsigned)(size_t)(LAS char*)smem;
  const int hh = lane >> 5, l31 = lane & 31, l15 = lane & 15, g4 = lane >> 4, q4 = l15 >> 2, p4 = lane & 3, r8 = lane >> 3, c8 = lane & 7;
  unsigned st_r = 0, st_c = 0, retry_ = 0; (void)st_r; (void)st_c; (void)retry_;
  asm volatile("" :: "s"(x), "s"(wqkv), "s"(wphi), "s"(wo), "s"(out), "s"(ws));
  STAMP(0);
  const rsrc_t rws = __builtin_amdgcn_make_buffer_rsrc(ws, 0, W_BYTES, 0x00020000);
  const rsrc_t rx = __builtin_amdgcn_make_buffer_rsrc((void*)x, 0, 2048 * 1024 * 2, 0x00020000);
  const rsrc_t rw = __builtin_amdgcn_make_buffer_rsrc((void*)wqkv, 0, 1024 * 3072 * 2, 0x00020000);
  const rsrc_t rout = __builtin_amdgcn_make_buffer_rsrc(out, 0, 2048 * 1024 * 2, 0x00020000);

  const unsigned vx0 = r8 * 2048 + ((c8 ^ (r8 >> 1)) << 4), vx1 = r8 * 2048 + ((c8 ^ (4 + (r8 >> 1))) << 4);
  const unsigned vw0 = r8 * 6144 + ((c8 ^ (r8 & 2)) << 4), vw1 = r8 * 6144 + ((c8 ^ (4 + (r8 & 2))) << 4);
  const unsigned sx = (rb * 128 + lw * 32) * 2048, sw = cb * 128 + lw * 16 * 6144;
  auto stage = [&](int t, int slot) {
    LAS char* dst = (LAS char*)smem + slot * SLOT;
#define LX(i) __builtin_amdgcn_raw_ptr_buffer_load_lds(rx, dst + (lw * 4 + i) * 1024, 16, (i & 1) ? vx1 : vx0, sx + i * 16384 + t * 128, 0, 0)
#define LW(i) __builtin_amdgcn_raw_ptr_buffer_load_lds(rw, dst + 16384 + (i >> 1) * 8192 + (lw * 2 + (i & 1)) * 1024, 16, (i & 1) ? vw1 : vw0, \
                                                     sw + (i >> 1) * 2048 + (t * 64 + (i & 1) * 8) * 6144, 0, 0)
    LW(2); LX(0); LW(3); LX(1); LW(4); LX(2); LW(5); LX(3); LW(0); LW(1);
  };
  u64 pc; unsigned code_len;
  asm volatile("s_getpc_b64 %0\n.Lhedgehog_begin:\n\ts_mov_b32 %1, .Lhedgehog_end-.Lhedgehog_begin" : "=s"(pc), "=s"(code_len));
  if (w < 4) stage(0, 0);
  int role = w; asm volatile("" : "+s"(role));

  if (role < 4) {
    const rsrc_t ro = __builtin_amdgcn_make_buffer_rsrc((void*)wo, 0, 1024 * 1024 * 2, 0x00020000);
    const int turn = (rl ^ xcd) & 7;
    auto pull = [&](int s) {
      const int u = cl * 4 + lw, l3 = lane / 3;
      __builtin_amdgcn_raw_ptr_buffer_load_lds(rx, (LAS char*)smem + L_DUMMY, 4, lane < 16 ? lane * 2048 : OOB,
                                               ((xcd & 1) * 1024 + ((xcd >> 1) * 16 + u) * 16) * 2048 + s * 128, 0, 0);
      const unsigned vw = lane < 18 ? ((xcd & 1) * 96 + u * 6 + l3) * 2048 + (lane - l3 * 3) * 128 : lane < 30 ? (u * 12 + lane - 18) * 2048 + 384 : OOB;
      __builtin_amdgcn_raw_ptr_buffer_load_lds(rw, (LAS char*)smem + L_DUMMY, 4, vw, (xcd >> 1) * 512 + s * 64 * 6144, 0, 0);
    };
    const bool early = turn >= IPN && turn <= VPF;
    if (early) pull(turn);
    stage(1, 1);
    if (early) waitv<12>(); else waitv<10>();
    bar0();
    stage(2, 2);
    {
      const int u = idx * 4 + lw;
      const char* c = (const char*)(pc & ~127ull) + (u * 256 < code_len ? u : 0) * 256;
      const char* p1 = u < 64 ? (const char*)out + u * 65536 : (const char*)wo + (u & 31) * 65536;
      asm volatile("s_load_dwordx2 s[100:101], %0, 0x3c\n\ts_load_dwordx2 s[100:101], %0, 0xbc\n\ts_load_dword s100, %1, 0x0"
                   :: "s"(c), "s"(p1) : "s100", "s101");
    }
    u32x4 e4 = {EMPTY, EMPTY, EMPTY, EMPTY};
    asm volatile("" : "+v"(e4));
    int slot = 0, extra = 0;
#pragma unroll 1
    for (int t = 1; t < 14; t++) {
      if (extra == 0) waitv<10>(); else waitv<12>();
      extra = 0;
      bar0();
      STAMPV(t + 16);
      if (t + VPF < 16 && ((t + VPF) & 7) == turn) {
        pull(t + VPF); extra = 2;
      }
      if (t == WPT + turn && turn < 2) {
        const unsigned u = ((turn * 8 + xcd) * 4 + cl) * 4 + lw;
        __builtin_amdgcn_raw_ptr_buffer_load_lds(ro, (LAS char*)smem + L_DUMMY, 4, l31 * 128, u * 8192, 0, 0);
        __builtin_amdgcn_raw_ptr_buffer_load_lds(ro, (LAS char*)smem + L_DUMMY, 4, l31 * 128, u * 8192 + 4096, 0, 0);
        extra = 2;
      }
      stage(t + 2, slot);
      slot = slot == 2 ? 0 : slot + 1;
    }
    const rsrc_t rp = __builtin_amdgcn_make_buffer_rsrc((void*)wphi, 0, 32 * 1024 * 2, 0x00020000);
    __builtin_amdgcn_raw_ptr_buffer_load_lds(rp, (LAS char*)smem + L_PHI + lw * 1024, 16, lane * 16, cb * 4096 + lw * 1024, 0, 0);
    waitv<11>(); bar0();
#pragma unroll
    for (int i = 0; i < 2; i++) __builtin_amdgcn_raw_buffer_store_b128(e4, rws, W_P + (cb * 16 + rb) * 8192 + lw * 2048 + i * 1024 + lane * 16, 0, SC1);
    __builtin_amdgcn_raw_buffer_store_b128(e4, rws, (cb * 16 + rb) * 512 + lw * 128 + (lane < 8 ? W_PZ + c8 * 16 : lane < 16 ? W_R + c8 * 16 : OOB), 0, SC1);
    u32x4 wr_[32];
    const int wrot = (idx & 3) * 8;
    const unsigned vo = r8 * 2048 + c8 * 16, so_ = lw * 256 * 2048 + ocb * 128;
    auto wo_load = [&](int i0, int i1) {
#pragma unroll
      for (int i = i0; i < i1; i++) wr_[i] = __builtin_amdgcn_raw_buffer_load_b128(ro, vo, so_ + ((i + wrot) & 31) * 16384, 0);
      SB;
    };
    const unsigned T = epoch_tag(ws);
    if (tid == 0) *LP(unsigned, sb + L_CNT) = 0;
    __builtin_amdgcn_s_waitcnt(0x0070 | 3);
    bar0();
    const unsigned t15 = now32();
    wo_load(0, WN0);
    waitv<WN0>();
    count_in(sb + L_CNT);
    if (lw == 0) lds_wait(sb + L_CNT, 4);
    __builtin_amdgcn_raw_buffer_store_b64(u32x2{t15, T}, rws, tid == 0 ? W_H1 + (cb * 16 + rb) * 256 : OOB, 0, SC1);
    STAMP(12);
    wo_load(WN0, WN1);
    unsigned touch = __builtin_amdgcn_raw_buffer_load_b32(lw & 1 ? rout : rws, lane == 0 ? 0 : OOB, orb * 262144 + ocb * 128, SC1);
    unsigned ha = lane < 16 && lane != rb ? W_H1 + (cb * 16 + lane) * 256 : OOB;
    u32x2 h1;
    {
      const int srow = 4 * g4 + q4;
      const int sw = ((srow >> 1) & 1) | ((srow >> 2) & 2) | (srow & 4);
      unsigned ia[2], va = sb + L_V + (lw >> 1) * 4096 + lane * 16;
#pragma unroll
      for (int ft = 0; ft < 2; ft++) ia[ft] = sb + (1 + (lw >> 1)) * SLOT + 24576 + (lw & 1) * 8192 + srow * 64 + (((4 * ft + p4) ^ sw) << 3);
      unsigned pa = W_P + (cb * 16 + rb) * 8192 + lw * 2048 + g4 * 512 + l15 * 16;
      unsigned za = l15 < 2 ? W_PZ + (cb * 16 + rb) * 512 + lw * 128 + (l15 * 4 + g4) * 16 : OOB;
      u32x4 one4 = {0x3f803f80u, 0x3f803f80u, 0x3f803f80u, 0x3f803f80u};
      f32x4 kv[2][2] = {}, z[2] = {};
      asm volatile("" : "+v"(ia[0]), "+v"(ia[1]), "+v"(va), "+v"(pa), "+v"(za), "+v"(one4), "+v"(ha));
      STAMP(1);
      bar();
      bf16x8 vf[2][2][2];
#pragma unroll
      for (int i = 0; i < 8; i++) vf[i >> 2][(i >> 1) & 1][i & 1] = lds_b128(va + (i >> 2) * 8192 + (i & 3) * 1024);
#pragma unroll
      for (int rt = 0; rt < 2; rt++) {
        if (rt) {
          asm volatile("" : "+v"(kv[0][0]), "+v"(kv[0][1]), "+v"(kv[1][0]), "+v"(kv[1][1]), "+v"(z[0]), "+v"(z[1]));
          __builtin_amdgcn_s_setprio(3);
          STAMP(13);
          bar();
          h1 = __builtin_amdgcn_raw_buffer_load_b64(rws, ha, 0, SC1);
        }
        bf16x8 pf[2][2];
#pragma unroll
        for (int i = 0; i < 4; i++) { const unsigned a = ia[i & 1] + (i >> 1) * 4096 + rt * 2048; pf[i >> 1][i & 1] = lds_tr2(a, a + 1024); }
        if (rt) count_in(sb + L_CNT);
        SB;
#pragma unroll
        for (int h = 0; h < 2; h++)
#pragma unroll
          for (int et = 0; et < 2; et++)
#pragma unroll
            for (int ft = 0; ft < 2; ft++) kv[ft][et] = MFMA16(pf[h][ft], vf[h][rt][et], kv[ft][et]);
        SB;
#pragma unroll
        for (int h = 0; h < 2; h++)
#pragma unroll
          for (int ft = 0; ft < 2; ft++) z[ft] = MFMA16(pf[h][ft], BF8(one4), z[ft]);
        SB;
      }
#pragma unroll
      for (int et = 0; et < 2; et++)
        __builtin_amdgcn_raw_buffer_store_b128(seal4(u32x4{pk2(kv[0][et][0], kv[0][et][1]), pk2(kv[0][et][2], kv[0][et][3]),
                                                           pk2(kv[1][et][0], kv[1][et][1]), pk2(kv[1][et][2], kv[1][et][3])}), rws, pa + et * 256, 0, SC1);
      u32x4 zs;
#pragma unroll
      for (int j = 0; j < 4; j++) zs[j] = seal(__float_as_uint((lane & 1) ? z[1][j] : z[0][j]));
      __builtin_amdgcn_raw_buffer_store_b128(zs, rws, za, 0, SC1);
      SB;
    }
    STAMP(6);
    const unsigned da = sb + L_WO + lw * 32768 + r8 * 128 + ((c8 ^ (r8 & 6)) << 4);
    auto wo_image = [&](int i0, int i1) {
#pragma unroll
      for (int i = i0; i < i1; i++) *LP(u32x4, da + ((i + wrot) & 31) * 1024) = wr_[i];
      SB;
    };
    const unsigned tl = t15 + max(latest(h1, rws, ha, t15, T), 0);
    {
      u32x4 g[2], gz[2];
      const unsigned pa = W_P + (cb * 16 + r8) * 8192 + rb * 512 + lw * 128 + c8 * 16;
      const unsigned za = W_PZ + (cb * 16 + r8) * 512 + lw * 128 + c8 * 16;
      const unsigned ra = (lane & 8) ? OOB : W_R + cb * 8192 + rb * 512 + lw * 128 + c8 * 16 + g4 * 4;
      STAMP(8);
      wait_until(tl + D1);
      STAMP(16);
#pragma unroll
      for (int i = 0; i < 2; i++) g[i] = __builtin_amdgcn_raw_buffer_load_b128(rws, pa + i * 65536, 0, SC1);
#pragma unroll
      for (int i = 0; i < 2; i++) gz[i] = __builtin_amdgcn_raw_buffer_load_b128(rws, za + i * 4096, 0, SC1);
      SB;
      wo_load(WN1, WN2);
      lds_wait(sb + L_CNT, 12);
      wo_image(0, WI1);
      RETRY(31) += settle<2>(g, rws, pa, 65536) ? 1 : 0;
      STAMP(7);
      float s[8];
#pragma unroll
      for (int j = 0; j < 4; j++) {
        s[2 * j] = __uint_as_float(g[0][j] << 16) + __uint_as_float(g[1][j] << 16);
        s[2 * j + 1] = __uint_as_float(g[0][j] & 0xffff0000u) + __uint_as_float(g[1][j] & 0xffff0000u);
      }
      const float a0 = fold16(fold32(s[0], s[4]), fold32(s[2], s[6])), a1 = fold16(fold32(s[1], s[5]), fold32(s[3], s[7]));
      __builtin_amdgcn_raw_buffer_store_b32(seal(pk2(fold8(a0), fold8(a1))), rws, ra, 0, SC1);
      SB;
      STAMP(2);
      settle<2>(gz, rws, za, 4096);
#pragma unroll
      for (int j = 0; j < 4; j++) s[j] = __uint_as_float(gz[0][j]) + __uint_as_float(gz[1][j]);
      float z0 = fold32(s[0], s[2]), z1 = fold32(s[1], s[3]);
      z0 = fold8(fold16(z0, z0)); z1 = fold8(fold16(z1, z1));
      if ((lane & 24) == 0) *LP(unsigned, sb + L_Z + lw * 64 + (c8 & 1) * 32 + ((c8 >> 1) & 1) * 16 + (c8 >> 2) * 8 + hh * 4) = pk2(z0, z1);
      count_in(sb + L_CNT);
      STAMP(3);
      const unsigned ka = W_R + cb * 8192 + lw * 2048 + lane * 16;
      wait_until(tl + D1 + D2);
#pragma unroll
      for (int i = 0; i < 2; i++) g[i] = __builtin_amdgcn_raw_buffer_load_b128(rws, ka + i * 1024, 0, SC1);
      __builtin_amdgcn_raw_buffer_store_b64(u32x2{now32(), T}, rws, tid == 0 ? W_H2 + (rb * 16 + cb) * 256 : OOB, 0, SC1);
      SB;
      wo_load(WN2, 32);
      wo_image(WI1, WI2);
      RETRY(31) += settle<2>(g, rws, ka, 1024) ? 10 : 0;
#pragma unroll
      for (int i = 0; i < 2; i++) *LP(u32x4, sb + L_KV + (lw * 2 + i) * 1024 + lane * 16) = g[i];
    }
    STAMP(4);
    __builtin_amdgcn_s_setprio(0);
    bar();
    ha = tid < 16 ? W_H2 + (orb * 16 + lane) * 256 : OOB;
    h1 = __builtin_amdgcn_raw_buffer_load_b64(rws, ha, 0, SC1);
    __builtin_amdgcn_s_sleep(SLW);
    SB;
    wo_image(WI2, 32);
    asm volatile("" :: "v"(touch));
    STAMP(5);
    asm volatile("" : "+v"(h1));
    if (w == 0) {
      const unsigned t2 = tl + latest(h1, rws, ha, tl, T);
      if (b == 0) __builtin_amdgcn_raw_buffer_store_b32(T, rws, lane == 0 ? W_E : OOB, 0, SC1);
      STAMP(14);
      wait_until(t2 + D3);
      STAMP(15);
      unsigned fa = lane < 16 ? W_F + (orb * 16 + lane) * 256 : OOB;
#pragma unroll 1
      for (int sp = 0; sp < SPIN_CAP; sp++) {
        const u32x4 f = __builtin_amdgcn_raw_buffer_load_b128(rws, fa, 0, SC1);
        asm volatile("" ::: "memory");
        if (f[0] == T && f[1] == T && f[2] == T && f[3] == T) fa = OOB;
        if (__all(fa == OOB)) break;
        RETRY(31) = 100 + RETRY(31) % 100;
        __builtin_amdgcn_s_sleep(PSL);
      }
    }
  } else {
    const int wr = lw >> 1, wc = lw & 1;
    __builtin_amdgcn_s_waitcnt(0x0f70);
    if (lw < 2) {
      const unsigned vo = lw ? (b * 12 + l15) * 128 + g4 * 64 * 6144 : (b * 8 + c8) * 2048 + g4 * 128;
      const u16* base = lw ? wqkv : x;
      if (g4 < IPN && l15 < (lw ? 12 : 8)) {
        unsigned keep;
        asm volatile("s_mov_b32 %0, m0\n\ts_mov_b32 m0, %3\n\ts_nop 0\n\tglobal_load_lds_dword %1, %2\n\ts_mov_b32 m0, %0"
                     : "=&s"(keep) : "v"(vo), "s"(base), "s"(sb + L_DUMMY) : "memory");
      }
    }
    f32x4 acc[6][4] = {};
    unsigned xa[2], wa[2];
#pragma unroll
    for (int ks = 0; ks < 2; ks++) xa[ks] = sb + (wr * 64 + l15) * 128 + (((4 * ks + g4) ^ (l15 >> 1)) << 4);
#pragma unroll
    for (int nt = 0; nt < 2; nt++) wa[nt] = sb + 16384 + (8 * g4 + q4) * 128 + (((wc * 2 + nt) ^ ((q4 >> 1) | ((g4 & 1) << 1))) << 5) + p4 * 8;
    bf16x8 fx[2][4], fw[2][6];
    unsigned xs[2], wsl[2];
    auto rdw = [&](int buf, int j, int ks) {
      const unsigned a = wsl[j & 1] + (j < 4 ? 8192 + (j >> 1) * 8192 : 0) + ks * 4096;
      fw[buf][j] = lds_tr2(a, a + 512);
    };
    auto rd = [&](int buf, int ks, unsigned so, int j0, int j1) {
      if (ks == 0) {
        xs[0] = xa[0] + so; xs[1] = xa[1] + so; wsl[0] = wa[0] + so; wsl[1] = wa[1] + so;
        asm volatile("" : "+v"(xs[0]), "+v"(xs[1]), "+v"(wsl[0]), "+v"(wsl[1]));
      }
      rdw(buf, j0, ks);
#pragma unroll
      for (int m = 0; m < 4; m++) fx[buf][m] = lds_b128(xs[ks] + m * 2048);
#pragma unroll
      for (int j = j0 + 1; j < j1; j++) rdw(buf, j, ks);
    };
    auto mm = [&](int buf, int j0, int j1) {
#pragma unroll
      for (int j = j0; j < j1; j++)
#pragma unroll
        for (int m0 = 0; m0 < 4; m0++) {
          const int m = (j & 1) ? 3 - m0 : m0;
          acc[j][m] = (j == 2 || j == 3) ? MFMA16(fx[buf][m], fw[buf][j], acc[j][m]) : MFMA16(fw[buf][j], fx[buf][m], acc[j][m]);
        }
    };
#define SCHED(nds, nmf) do { _Pragma("unroll") for (int i_ = 0; i_ < (nds); i_++) { __builtin_amdgcn_sched_group_barrier(0x8, 1, 0); __builtin_amdgcn_sched_group_barrier(0x100, 1, 0); } \
      if ((nmf) > (nds)) __builtin_amdgcn_sched_group_barrier(0x8, (nmf) - (nds), 0); SB; } while (0)
#pragma unroll
    for (int j = 0; j < 6; j++) asm volatile("" : "+v"(acc[j][0]), "+v"(acc[j][1]), "+v"(acc[j][2]), "+v"(acc[j][3]));
    unsigned pia[4];
    {
      const int sw = ((l31 >> 1) & 1) | ((l31 >> 2) & 2) | (l31 & 4);
#pragma unroll
      for (int q = 0; q < 4; q++) pia[q] = sb + (1 + wc) * SLOT + 24576 + (wr * 64 + l31) * 64 + (((2 * q + hh) ^ sw) << 3);
      asm volatile("" : "+v"(pia[0]), "+v"(pia[1]), "+v"(pia[2]), "+v"(pia[3]));
    }
    const unsigned T = epoch_tag(ws);
    bar();
    STAMP(1);
    unsigned so = 0;
    rd(0, 0, so, 0, 6);
    SB;
#pragma unroll 1
    for (int t = 0; t < 12; t++) {
      rd(1, 1, so, 0, 6); mm(0, 0, 6); SCHED(16, 24);
      bar();
      so = so == 2 * SLOT ? 0 : so + SLOT;
      rd(0, 0, so, 0, 6); mm(1, 0, 6); SCHED(16, 24);
    }
    rd(1, 1, 0, 0, 6); mm(0, 0, 6); SCHED(16, 24);
    bar();
    rd(0, 0, SLOT, 0, 4); mm(1, 0, 6); SCHED(12, 24);
    rd(1, 1, SLOT, 0, 4); mm(0, 0, 4); SCHED(12, 16);
    bar();
    rd(0, 0, 2 * SLOT, 0, 4); mm(1, 0, 4); SCHED(12, 16);
    rd(1, 1, 2 * SLOT, 0, 4); mm(0, 0, 4); SCHED(12, 16);
    bar();
    rd(0, 0, 0, 0, 4); mm(1, 0, 4); SCHED(12, 16);
    rd(1, 1, 0, 0, 4); mm(0, 0, 4); SCHED(12, 16);
    bf16x8 wp[2];
    {
      const unsigned pa = sb + L_PHI + wc * 2048 + (8 * hh + q4) * 64 + (g4 & 1) * 32 + p4 * 8;
      wp[0] = lds_tr2(pa, pa + 256); wp[1] = lds_tr2(pa + 1024, pa + 1280);
    }
    mm(1, 0, 4);
    STAMP(2);
    const f32x16 zero = {};
    f32x16 pk[2];
#pragma unroll
    for (int rt = 0; rt < 2; rt++) {
      pk[rt] = MFMA32(wp[0], pack16to32(acc[0][2 * rt], acc[0][2 * rt + 1]), zero);
      pk[rt] = MFMA32(wp[1], pack16to32(acc[1][2 * rt], acc[1][2 * rt + 1]), pk[rt]);
    }
#pragma unroll
    for (int rt = 0; rt < 2; rt++)
#pragma unroll
      for (int et = 0; et < 2; et++) {
        const f32x4 a = acc[2 + et][2 * rt], c = acc[2 + et][2 * rt + 1];
        *LP(u32x4, sb + L_V + lw * 4096 + (rt * 2 + et) * 1024 + lane * 16) = u32x4{pk2(a[0], a[1]), pk2(a[2], a[3]), pk2(c[0], c[1]), pk2(c[2], c[3])};
      }
#pragma unroll
    for (int rt = 0; rt < 2; rt++) {
      unsigned ph[2][8];
      dual_softmax(pk[rt], ph[0], ph[1]);
#pragma unroll
      for (int q = 0; q < 4; q++) {
        *LP(u32x2, pia[q] + rt * 2048) = u32x2{ph[0][2 * q], ph[0][2 * q + 1]};
        *LP(u32x2, pia[q] + rt * 2048 + 8192) = u32x2{ph[1][2 * q], ph[1][2 * q + 1]};
      }
      if (rt) STAMP(3);
      bar();
    }
    STAMP(4);
    SB;
    rd(0, 0, SLOT, 4, 6);
    SB;
    rd(1, 1, SLOT, 4, 6); mm(0, 4, 6); SCHED(8, 8);
    rd(0, 0, 2 * SLOT, 4, 6); mm(1, 4, 6); SCHED(8, 8);
    rd(1, 1, 2 * SLOT, 4, 6); mm(0, 4, 6); SCHED(8, 8);
    rd(0, 0, 0, 4, 6); mm(1, 4, 6); SCHED(8, 8);
    rd(1, 1, 0, 4, 6); mm(0, 4, 6); SCHED(8, 8);
    count_in(sb + L_CNT);
    mm(1, 4, 6);
    u32x4 phq[2][4];
#pragma unroll
    for (int rt = 0; rt < 2; rt++) {
      f32x16 p = MFMA32(wp[0], pack16to32(acc[4][2 * rt], acc[4][2 * rt + 1]), zero);
      p = MFMA32(wp[1], pack16to32(acc[5][2 * rt], acc[5][2 * rt + 1]), p);
      unsigned ph[2][8];
      dual_softmax(p, ph[0], ph[1]);
#pragma unroll
      for (int i = 0; i < 4; i++) { const int sg = i >> 1, st = i & 1; phq[rt][i] = u32x4{ph[sg][2 * st], ph[sg][2 * st + 1], ph[sg][4 + 2 * st], ph[sg][5 + 2 * st]}; }
      asm volatile("" : "+v"(phq[rt][0]), "+v"(phq[rt][1]), "+v"(phq[rt][2]), "+v"(phq[rt][3]));
    }
    unsigned ka = sb + L_KV + wc * 4096 + lane * 16, zl = sb + L_Z + wc * 128 + hh * 32;
    unsigned oa = W_O + ((rb * 8 + wr * 4 + (g4 & 1)) * 32 + cb * 2 + wc) * 1024 + hh * 256 + l15 * 16, oa1 = oa + 65536;
    unsigned fa = lane == 0 ? W_F + (rb * 16 + cb) * 256 + lw * 4 : OOB, Tv = T;
    asm volatile("" : "+v"(ka), "+v"(zl), "+v"(oa), "+v"(oa1), "+v"(fa), "+v"(Tv));
    STAMP(5);
    lds_wait(sb + L_CNT, 16);
    float rden[2];
    {
      u32x4 zf[4];
#pragma unroll
      for (int i = 0; i < 4; i++) zf[i] = *LP(const u32x4, zl + (i >> 1) * 64 + (i & 1) * 16);
      float dn[2][2] = {};
#pragma unroll
      for (int i = 0; i < 4; i++)
#pragma unroll
        for (int j = 0; j < 4; j++)
#pragma unroll
          for (int rt = 0; rt < 2; rt++) dn[rt][j & 1] = dot2(phq[rt][i][j], zf[i][j], dn[rt][j & 1]);
#pragma unroll
      for (int rt = 0; rt < 2; rt++) { const float d = dn[rt][0] + dn[rt][1]; rden[rt] = __builtin_amdgcn_rcpf(fold32(d, d) + 1e-6f); }
      asm volatile("" : "+v"(rden[0]), "+v"(rden[1]));
    }
    STAMP(16);
    __builtin_amdgcn_s_setprio(2);
    bar();
    STAMP(6);
    {
      bf16x8 kf[4];
#pragma unroll
      for (int i = 0; i < 4; i++) kf[i] = lds_b128(ka + i * 1024);
      auto put = [&](const f32x16& n, float r, int st, unsigned a) {
        const f32x2 r2 = {r, r};
        u32x4 v;
#pragma unroll
        for (int j = 0; j < 4; j++) v[j] = pk2v(f32x2{n[8 * st + 2 * j], n[8 * st + 2 * j + 1]} * r2);
        __builtin_amdgcn_raw_buffer_store_b128(v, rws, a + st * 512, 0, SC1);
      };
      SB;
      f32x16 n0 = zero, n1 = zero;
#pragma unroll
      for (int i = 0; i < 4; i++) { n0 = MFMA32(kf[i], BF8(phq[0][i]), n0); SB; }
#pragma unroll
      for (int i = 0; i < 3; i++) { n1 = MFMA32(kf[i], BF8(phq[1][i]), n1); SB; }
      put(n0, rden[0], 0, oa); SB;
      n1 = MFMA32(kf[3], BF8(phq[1][3]), n1); SB;
      put(n0, rden[0], 1, oa); SB;
      put(n1, rden[1], 0, oa1); SB; put(n1, rden[1], 1, oa1); SB;
    }
    STAMP(7);
    waitv<0>();
    __builtin_amdgcn_raw_buffer_store_b32(Tv, rws, fa, 0, SC1);
    __builtin_amdgcn_s_setprio(0);
    STAMP(8);
  }
  {
    const int rg = lw, kh = w >> 2;
    const int kr = 16 * (g4 >> 1) + 4 * (g4 & 1) + q4;
    unsigned wa[4];
#pragma unroll
    for (int nt = 0; nt < 4; nt++) wa[nt] = sb + L_WO + kh * 65536 + kr * 128 + ((nt ^ ((kr >> 1) & 3)) << 5) + p4 * 8;
    const unsigned ob = W_O + ((orb * 8 + rg * 2) * 32 + kh * 16) * 1024;
    constexpr int LAG = OLAG, PF = OPF;
    f32x4 c0[4] = {}, c1[4] = {}, px[4];
    u32x4 o0[PF + 1], o1[PF + 1];
    bf16x8 a[LAG + 2][4];
    unsigned v0 = lane * 16 + kh * 32768, v1 = lane * 16 + (kh ^ 1) * 32768;
    unsigned xw = sb + L_FX + w * 4096 + lane * 16, xr = sb + L_FX + (w ^ 4) * 4096 + lane * 16;
    unsigned oa = (orb * 128 + rg * 32 + kh * 16 + r8) * 2048 + ocb * 128 + c8 * 16;
    unsigned tw = sb + L_WO + w * 2048 + l15 * 128 + (((g4 >> 1) ^ (l15 & 7)) << 4) + (((g4 & 1) ^ (l15 >> 3)) << 3);
    unsigned tr = sb + L_WO + w * 2048 + r8 * 128 + ((c8 ^ r8) << 4);
    asm volatile("" : "+v"(wa[0]), "+v"(wa[1]), "+v"(wa[2]), "+v"(wa[3]), "+v"(v0), "+v"(v1), "+v"(xw), "+v"(xr), "+v"(oa), "+v"(tw), "+v"(tr));
    asm volatile("" : "+v"(c0[0]), "+v"(c0[1]), "+v"(c0[2]), "+v"(c0[3]), "+v"(c1[0]), "+v"(c1[1]), "+v"(c1[2]), "+v"(c1[3]));
    auto ldo = [&](int i) {
      if (i < 16) o1[i % (PF + 1)] = __builtin_amdgcn_raw_buffer_load_b128(rws, v1, ob + i * 1024, SC1);
      if (i >= LAG && i < 16 + LAG) o0[i % (PF + 1)] = __builtin_amdgcn_raw_buffer_load_b128(rws, v0, ob + (i - LAG) * 1024, SC1);
    };
    auto ldw = [&](int i) {
#pragma unroll
      for (int nt = 0; nt < 4; nt++) a[i % (LAG + 2)][nt] = lds_tr2(wa[nt] + i * 4096, wa[nt] + i * 4096 + 1024);
    };
    bar();
    STAMP(9);
#pragma unroll
    for (int i = 0; i <= PF; i++) { ldo(i); SB; }
    SB;
    ldw(0);
    SB;
#pragma unroll
    for (int i = 0; i < 16 + LAG; i++) {
      if (i) ldo(i + PF);
      SB;
      if (i < 15) ldw(i + 1);
      if (i < 16) {
#pragma unroll
        for (int nt = 0; nt < 4; nt++) c1[nt] = MFMA16(a[i % (LAG + 2)][nt], BF8(o1[i % (PF + 1)]), c1[nt]);
      }
      if (i >= LAG) {
#pragma unroll
        for (int nt = 0; nt < 4; nt++) c0[nt] = MFMA16(a[(i - LAG) % (LAG + 2)][nt], BF8(o0[i % (PF + 1)]), c0[nt]);
      }
#pragma unroll
      for (int q = 0; q < 8; q++) { __builtin_amdgcn_sched_group_barrier(0x8, 1, 0); __builtin_amdgcn_sched_group_barrier(0x100, 1, 0); }
      SB;
      if (i == 15) {
        STAMP(10);
#pragma unroll
        for (int nt = 0; nt < 4; nt++) *LP(f32x4, xw + nt * 1024) = c1[nt];
      }
      if (i == (LAG ? 14 + LAG : 15)) {
        bar();
#pragma unroll
        for (int nt = 0; nt < 4; nt++) px[nt] = *LP(const f32x4, xr + nt * 1024);
        SB;
      }
    }
#pragma unroll
    for (int nt = 0; nt < 4; nt++) {
      const f32x4 v = px[nt] + c0[nt];
      *LP(u32x2, tw ^ (nt << 5)) = u32x2{pk2(v[0], v[1]), pk2(v[2], v[3])};
    }
    asm volatile("" ::: "memory");
    const u32x4 t0 = *LP(const u32x4, tr), t1 = *LP(const u32x4, tr + 1024);
    __builtin_amdgcn_raw_buffer_store_b128(t0, rout, oa, 0, SC1);
    __builtin_amdgcn_raw_buffer_store_b128(u32x4{t1[2], t1[3], t1[0], t1[1]}, rout, oa + 16384, 0, SC1);
    asm volatile(".Lhedgehog_end:");
    STAMP(11);
    STAMP(30);
    STAMP_DUMP();
  }
}

extern "C" void kernel_launch(void* const* d_in, const int* in_sizes, int n_in, void* d_out, int out_size, void* d_ws, size_t ws_size,
                              hipStream_t stream) {
  const size_t ws_off = WSO < 0 ? (ws_size - (size_t)(-WSO) * (1u << 20)) & ~(size_t)((2u << 20) - 1) : (size_t)WSO << 20;
  (void)hipFuncSetAttribute((const void*)hedgehog_fused, hipFuncAttributeMaxDynamicSharedMemorySize, LDS_BYTES);
  hedgehog_fused<<<256, 512, LDS_BYTES, stream>>>((const u16*)d_in[0], (const u16*)d_in[1], (const u16*)d_in[2], (const u16*)d_in[3],
                                                   (u16*)d_out, (char*)d_ws + ws_off);
}
```

```cpp
#define WN1 22
#define WN2 22
#define D1 225
#define D2 80
#define D3 135
#include <hip/hip_runtime.h>
#include <hip/hip_bf16.h>

typedef unsigned short u16;
typedef unsigned long long u64;
typedef __attribute__((ext_vector_type(8))) __bf16 bf16x8;
typedef __attribute__((ext_vector_type(4))) __bf16 bf16x4;
typedef __attribute__((ext_vector_type(2))) __bf16 bf16x2;
typedef __attribute__((ext_vector_type(16))) float f32x16;
typedef __attribute__((ext_vector_type(4))) float f32x4;
typedef __attribute__((ext_vector_type(2))) float f32x2;
typedef __attribute__((ext_vector_type(4))) unsigned u32x4;
typedef __attribute__((ext_vector_type(2))) unsigned u32x2;
typedef __amdgpu_buffer_rsrc_t rsrc_t;
#define LAS __attribute__((address_space(3)))
#define LP(T, a) ((LAS T*)(size_t)(a))
#define DI __device__ __forceinline__
#define MFMA32(a, b, c) __builtin_amdgcn_mfma_f32_32x32x16_bf16((a), (b), (c), 0, 0, 0)
#define MFMA16(a, b, c) __builtin_amdgcn_mfma_f32_16x16x32_bf16((a), (b), (c), 0, 0, 0)
#define SB __builtin_amdgcn_sched_barrier(0)
#define BF8(v) __builtin_bit_cast(bf16x8, v)
constexpr int SC1 = 16;
constexpr unsigned OOB = 0x80000000u;

#define VPF 3
#define IPN 2
#define WPT 9
#define PSL 2
#define HSL 8
#define OLAG 2
#define OPF 2
#define WSO -10
#define CL0 1
#define SLW 3
#define WN0 4
#define CLO 0
#define WI1 6
#define WI2 10

constexpr int SLOT = 40960;
constexpr int L_WO = 0;
constexpr int L_V = 122880;
constexpr int L_PHI = 147456;
constexpr int L_Z = 151552;
constexpr int L_CNT = 151808;
constexpr int L_KV = 139264;
constexpr int L_DUMMY = 152064;
constexpr int L_FX = 131072;
constexpr int LDS_BYTES = 163840;
constexpr unsigned W_O = 0;
constexpr unsigned W_P = 4u << 20;
constexpr unsigned W_PZ = 6u << 20;
constexpr unsigned W_R = W_PZ + (128u << 10);
constexpr unsigned W_H1 = W_R + (128u << 10);
constexpr unsigned W_H2 = W_H1 + (64u << 10);
constexpr unsigned W_F = W_H2 + (64u << 10);
constexpr unsigned W_E = W_F + (64u << 10);
constexpr unsigned W_TS = 9u << 20;
constexpr unsigned W_BYTES = 10u << 20;
constexpr int SPIN_CAP = 1 << 18;

DI void bar() { asm volatile("s_waitcnt lgkmcnt(0)\n\ts_barrier" ::: "memory"); }
DI void bar0() { asm volatile("s_barrier" ::: "memory"); }
template <int N> DI void waitv() { asm volatile("s_waitcnt vmcnt(%0)" ::"i"(N) : "memory"); }
DI unsigned pk2v(f32x2 v) { return __builtin_bit_cast(unsigned, __builtin_convertvector(v, bf16x2)); }
DI unsigned pk2(float a, float b) { return pk2v(f32x2{a, b}); }
DI bf16x8 pack16to32(const f32x4& c0, const f32x4& c1) {
  auto a = __builtin_amdgcn_permlane16_swap(pk2(c0[0], c0[1]), pk2(c1[0], c1[1]), false, false);
  auto b = __builtin_amdgcn_permlane16_swap(pk2(c0[2], c0[3]), pk2(c1[2], c1[3]), false, false);
  return BF8((u32x4{a[0], b[0], a[1], b[1]}));
}
DI float fold32(float a, float b) { auto r = __builtin_amdgcn_permlane32_swap(__float_as_uint(a), __float_as_uint(b), false, false); return __uint_as_float(r[0]) + __uint_as_float(r[1]); }
DI float fold16(float a, float b) { auto r = __builtin_amdgcn_permlane16_swap(__float_as_uint(a), __float_as_uint(b), false, false); return __uint_as_float(r[0]) + __uint_as_float(r[1]); }
DI float fold8(float a) { return a + __uint_as_float(__builtin_amdgcn_update_dpp(0u, __float_as_uint(a), 0x128, 0xf, 0xf, false)); }
DI float dot2(unsigned a, unsigned b, float c) { return __builtin_amdgcn_fdot2_f32_bf16(__builtin_bit_cast(bf16x2, a), __builtin_bit_cast(bf16x2, b), c, false); }
DI bf16x8 lds_b128(unsigned a) { return *LP(const bf16x8, a); }
DI bf16x8 lds_tr2(unsigned a0, unsigned a1) {
  bf16x4 lo = __builtin_amdgcn_ds_read_tr16_b64_v4bf16(LP(bf16x4, a0));
  bf16x4 hi = __builtin_amdgcn_ds_read_tr16_b64_v4bf16(LP(bf16x4, a1));
  return __builtin_shufflevector(lo, hi, 0, 1, 2, 3, 4, 5, 6, 7);
}
DI void dual_softmax(const f32x16& p, unsigned (&pos)[8], unsigned (&neg)[8]) {
#define FMX __builtin_elementwise_maximum
#define FMN __builtin_elementwise_minimum
  float mx = FMX(FMX(p[0], p[1]), p[2]), mn = FMN(FMN(p[0], p[1]), p[2]), mx2 = FMX(FMX(p[3], p[4]), p[5]), mn2 = FMN(FMN(p[3], p[4]), p[5]);
#pragma unroll
  for (int i = 6; i < 14; i += 4) {
    mx = FMX(FMX(mx, p[i]), p[i + 1]); mn = FMN(FMN(mn, p[i]), p[i + 1]);
    mx2 = FMX(FMX(mx2, p[i + 2]), p[i + 3]); mn2 = FMN(FMN(mn2, p[i + 2]), p[i + 3]);
  }
  mx = FMX(FMX(mx, p[14]), p[15]); mn = FMN(FMN(mn, p[14]), p[15]);
  mx = FMX(mx, mx2); mn = FMN(mn, mn2);
  {
    auto r = __builtin_amdgcn_permlane32_swap(__float_as_uint(mx), __float_as_uint(mx), false, false);
    auto s = __builtin_amdgcn_permlane32_swap(__float_as_uint(mn), __float_as_uint(mn), false, false);
    mx = FMX(__uint_as_float(r[0]), __uint_as_float(r[1])); mn = FMN(__uint_as_float(s[0]), __uint_as_float(s[1]));
  }
  const float c = 1.44269504088896341f;
  const f32x2 cp = {c, c}, cn = {-c, -c}, bx = {-mx * c, -mx * c}, bn = {mn * c, mn * c};
  f32x2 ep[8], en[8];
#pragma unroll
  for (int i = 0; i < 8; i++) {
    const f32x2 v = {p[2 * i], p[2 * i + 1]};
    const f32x2 a = __builtin_elementwise_fma(v, cp, bx), b = __builtin_elementwise_fma(v, cn, bn);
    ep[i] = f32x2{__builtin_amdgcn_exp2f(a[0]), __builtin_amdgcn_exp2f(a[1])};
    en[i] = f32x2{__builtin_amdgcn_exp2f(b[0]), __builtin_amdgcn_exp2f(b[1])};
  }
  const f32x2 s2p = ((ep[0] + ep[1]) + (ep[2] + ep[3])) + ((ep[4] + ep[5]) + (ep[6] + ep[7]));
  const f32x2 s2n = ((en[0] + en[1]) + (en[2] + en[3])) + ((en[4] + en[5]) + (en[6] + en[7]));
  const float sp = s2p[0] + s2p[1], sn = s2n[0] + s2n[1];
  const float rp = __builtin_amdgcn_rcpf(fold32(sp, sp)), rn = __builtin_amdgcn_rcpf(fold32(sn, sn));
  const f32x2 r2p = {rp, rp}, r2n = {rn, rn};
#pragma unroll
  for (int i = 0; i < 8; i++) { pos[i] = pk2v(ep[i] * r2p); neg[i] = pk2v(en[i] * r2n); }
}

constexpr unsigned EMPTY = 0xffffffffu;
DI unsigned seal(unsigned v) { return min(v, EMPTY - 1u); }
DI u32x4 seal4(u32x4 v) { return u32x4{seal(v[0]), seal(v[1]), seal(v[2]), seal(v[3])}; }
DI unsigned umax4(unsigned m, u32x4 v) { return max(max(max(max(m, v[0]), v[1]), v[2]), v[3]); }
template <int N> DI int settle(u32x4 (&g)[N], rsrc_t r, unsigned a, unsigned stride) {
  auto pending = [&]() { unsigned m = 0;
#pragma unroll
    for (int i = 0; i < N; i++) m = umax4(m, g[i]);
    return __any(m == EMPTY); };
  if (__builtin_expect(!pending(), 1)) return 0;
  int sp = 1;
#pragma unroll 1
  for (; sp < SPIN_CAP; sp++) {
    __builtin_amdgcn_s_sleep(PSL);
    asm volatile("" ::: "memory");
#pragma unroll
    for (int i = 0; i < N; i++) g[i] = __builtin_amdgcn_raw_buffer_load_b128(r, a + i * stride, 0, SC1);
    if (!pending()) break;
  }
  return sp;
}
DI unsigned now32() { u64 t; asm volatile("s_memrealtime %0\n\ts_waitcnt lgkmcnt(0)" : "=s"(t) :: "memory"); return (unsigned)t; }
DI void wait_until(unsigned t) {
#pragma unroll 1
  for (int sp = 0; sp < 1000; sp++) { if (t - now32() - 1u >= 1000u) break; }
}
DI int latest(u32x2 h, rsrc_t r, unsigned a, unsigned t0, unsigned T) {
  int d = -0x10000;
  if (a != OOB && h[1] == T) { d = (int)(h[0] - t0); a = OOB; }
  if (!__all(a == OOB)) {
#pragma unroll 1
    for (int sp = 0; sp < SPIN_CAP; sp++) {
      asm volatile("" ::: "memory");
      h = __builtin_amdgcn_raw_buffer_load_b64(r, a, 0, SC1);
      if (a != OOB && h[1] == T) { d = (int)(h[0] - t0); a = OOB; }
      if (__all(a == OOB)) break;
      __builtin_amdgcn_s_sleep(PSL);
    }
  }
  d = max(d, __builtin_amdgcn_update_dpp(d, d, 0x128, 0xf, 0xf, false));
  d = max(d, __builtin_amdgcn_update_dpp(d, d, 0x124, 0xf, 0xf, false));
  d = max(d, __builtin_amdgcn_update_dpp(d, d, 0x122, 0xf, 0xf, false));
  d = max(d, __builtin_amdgcn_update_dpp(d, d, 0x121, 0xf, 0xf, false));
  asm volatile("" ::: "memory");
  return __builtin_amdgcn_readfirstlane(d);
}
DI void count_in(unsigned lds_addr) {
  asm volatile("s_mov_b64 exec, 1\n\tds_add_u32 %0, %1\n\ts_mov_b64 exec, -1" :: "v"(lds_addr), "v"(1u) : "memory");
}
DI void lds_wait(unsigned lds_addr, unsigned n) {
#pragma unroll 1
  for (int sp = 0; sp < SPIN_CAP * 8; sp++) { if (__builtin_amdgcn_readfirstlane(*LP(const volatile unsigned, lds_addr)) >= n) break; __builtin_amdgcn_s_sleep(1); }
  asm volatile("" ::: "memory");
}
DI unsigned epoch_tag(const char* ws) {
  unsigned e;
  asm volatile("s_load_dword %0, %1, 0x0\n\ts_waitcnt lgkmcnt(0)" : "=s"(e) : "s"(ws + W_E) : "memory");
  return e + 1u;
}

#define STAMP(i)
#define STAMPV(i)
#define RETRY(i) retry_
#define STAMP_DUMP()

__global__ __launch_bounds__(512, 2) void hedgehog_fused(const u16* __restrict__ x, const u16* __restrict__ wqkv, const u16* __restrict__ wphi,
                                                         const u16* __restrict__ wo, u16* __restrict__ out, char* __restrict__ ws) {
  extern __shared__ __attribute__((aligned(16))) char smem[];
  const int tid = threadIdx.x, lane = tid & 63;
  const int w = __builtin_amdgcn_readfirstlane(tid >> 6), lw = w & 3;
  const int b = blockIdx.x, xcd = b & 7, idx = b >> 3;
  const int rl = idx & 7, cl = idx >> 3;
  const int rb = (xcd & 1) * 8 + rl, cb = (xcd >> 1) * 4 + cl;
  const int orb = (xcd >> 1) * 4 + (idx & 3), ocb = (xcd & 1) * 8 + (idx >> 2);
  const unsigned sb = (unsigned)(size_t)(LAS char*)smem;
  const int hh = lane >> 5, l31 = lane & 31, l15 = lane & 15, g4 = lane >> 4, q4 = l15 >> 2, p4 = lane & 3, r8 = lane >> 3, c8 = lane & 7;
  unsigned st_r = 0, st_c = 0, retry_ = 0; (void)st_r; (void)st_c; (void)retry_;
  asm volatile("" :: "s"(x), "s"(wqkv), "s"(wphi), "s"(wo), "s"(out), "s"(ws));
  STAMP(0);
  const rsrc_t rws = __builtin_amdgcn_make_buffer_rsrc(ws, 0, W_BYTES, 0x00020000);
  const rsrc_t rx = __builtin_amdgcn_make_buffer_rsrc((void*)x, 0, 2048 * 1024 * 2, 0x00020000);
  const rsrc_t rw = __builtin_amdgcn_make_buffer_rsrc((void*)wqkv, 0, 1024 * 3072 * 2, 0x00020000);
  const rsrc_t rout = __builtin_amdgcn_make_buffer_rsrc(out, 0, 2048 * 1024 * 2, 0x00020000);

  const unsigned vx0 = r8 * 2048 + ((c8 ^ (r8 >> 1)) << 4), vx1 = r8 * 2048 + ((c8 ^ (4 + (r8 >> 1))) << 4);
  const unsigned vw0 = r8 * 6144 + ((c8 ^ (r8 & 2)) << 4), vw1 = r8 * 6144 + ((c8 ^ (4 + (r8 & 2))) << 4);
  const unsigned sx = (rb * 128 + lw * 32) * 2048, sw = cb * 128 + lw * 16 * 6144;
  auto stage = [&](int t, int slot) {
    LAS char* dst = (LAS char*)smem + slot * SLOT;
#define LX(i) __builtin_amdgcn_raw_ptr_buffer_load_lds(rx, dst + (lw * 4 + i) * 1024, 16, (i & 1) ? vx1 : vx0, sx + i * 16384 + t * 128, 0, 0)
#define LW(i) __builtin_amdgcn_raw_ptr_buffer_load_lds(rw, dst + 16384 + (i >> 1) * 8192 + (lw * 2 + (i & 1)) * 1024, 16, (i & 1) ? vw1 : vw0, \
                                                     sw + (i >> 1) * 2048 + (t * 64 + (i & 1) * 8) * 6144, 0, 0)
    LW(2); LX(0); LW(3); LX(1); LW(4); LX(2); LW(5); LX(3); LW(0); LW(1);
  };
  u64 pc; unsigned code_len;
  asm volatile("s_getpc_b64 %0\n.Lhedgehog_begin:\n\ts_mov_b32 %1, .Lhedgehog_end-.Lhedgehog_begin" : "=s"(pc), "=s"(code_len));
  if (w < 4) stage(0, 0);
  int role = w; asm volatile("" : "+s"(role));

  if (role < 4) {
    const rsrc_t ro = __builtin_amdgcn_make_buffer_rsrc((void*)wo, 0, 1024 * 1024 * 2, 0x00020000);
    const int turn = (rl ^ xcd) & 7;
    auto pull = [&](int s) {
      const int u = cl * 4 + lw, l3 = lane / 3;
      __builtin_amdgcn_raw_ptr_buffer_load_lds(rx, (LAS char*)smem + L_DUMMY, 4, lane < 16 ? lane * 2048 : OOB,
                                               ((xcd & 1) * 1024 + ((xcd >> 1) * 16 + u) * 16) * 2048 + s * 128, 0, 0);
      const unsigned vw = lane < 18 ? ((xcd & 1) * 96 + u * 6 + l3) * 2048 + (lane - l3 * 3) * 128 : lane < 30 ? (u * 12 + lane - 18) * 2048 + 384 : OOB;
      __builtin_amdgcn_raw_ptr_buffer_load_lds(rw, (LAS char*)smem + L_DUMMY, 4, vw, (xcd >> 1) * 512 + s * 64 * 6144, 0, 0);
    };
    const bool early = turn >= IPN && turn <= VPF;
    if (early) pull(turn);
    stage(1, 1);
    if (early) waitv<12>(); else waitv<10>();
    bar0();
    stage(2, 2);
    {
      const int u = idx * 4 + lw;
      const char* c = (const char*)(pc & ~127ull) + (u * 256 < code_len ? u : 0) * 256;
      const char* p1 = u < 64 ? (const char*)out + u * 65536 : (const char*)wo + (u & 31) * 65536;
      asm volatile("s_load_dwordx2 s[100:101], %0, 0x3c\n\ts_load_dwordx2 s[100:101], %0, 0xbc\n\ts_load_dword s100, %1, 0x0"
                   :: "s"(c), "s"(p1) : "s100", "s101");
    }
    u32x4 e4 = {EMPTY, EMPTY, EMPTY, EMPTY};
    asm volatile("" : "+v"(e4));
    int slot = 0, extra = 0;
#pragma unroll 1
    for (int t = 1; t < 14; t++) {
      if (extra == 0) waitv<10>(); else waitv<12>();
      extra = 0;
      bar0();
      STAMPV(t + 16);
      if (t + VPF < 16 && ((t + VPF) & 7) == turn) {
        pull(t + VPF); extra = 2;
      }
      if (t == WPT + turn && turn < 2) {
        const unsigned u = ((turn * 8 + xcd) * 4 + cl) * 4 + lw;
        __builtin_amdgcn_raw_ptr_buffer_load_lds(ro, (LAS char*)smem + L_DUMMY, 4, l31 * 128, u * 8192, 0, 0);
        __builtin_amdgcn_raw_ptr_buffer_load_lds(ro, (LAS char*)smem + L_DUMMY, 4, l31 * 128, u * 8192 + 4096, 0, 0);
        extra = 2;
      }
      stage(t + 2, slot);
      slot = slot == 2 ? 0 : slot + 1;
    }
    const rsrc_t rp = __builtin_amdgcn_make_buffer_rsrc((void*)wphi, 0, 32 * 1024 * 2, 0x00020000);
    __builtin_amdgcn_raw_ptr_buffer_load_lds(rp, (LAS char*)smem + L_PHI + lw * 1024, 16, lane * 16, cb * 4096 + lw * 1024, 0, 0);
    waitv<11>(); bar0();
#pragma unroll
    for (int i = 0; i < 2; i++) __builtin_amdgcn_raw_buffer_store_b128(e4, rws, W_P + (cb * 16 + rb) * 8192 + lw * 2048 + i * 1024 + lane * 16, 0, SC1);
    __builtin_amdgcn_raw_buffer_store_b128(e4, rws, (cb * 16 + rb) * 512 + lw * 128 + (lane < 8 ? W_PZ + c8 * 16 : lane < 16 ? W_R + c8 * 16 : OOB), 0, SC1);
    u32x4 wr_[32];
    const int wrot = (idx & 3) * 8;
    const unsigned vo = r8 * 2048 + c8 * 16, so_ = lw * 256 * 2048 + ocb * 128;
    auto wo_load = [&](int i0, int i1) {
#pragma unroll
      for (int i = i0; i < i1; i++) wr_[i] = __builtin_amdgcn_raw_buffer_load_b128(ro, vo, so_ + ((i + wrot) & 31) * 16384, 0);
      SB;
    };
    const unsigned T = epoch_tag(ws);
    if (tid == 0) *LP(unsigned, sb + L_CNT) = 0;
    __builtin_amdgcn_s_waitcnt(0x0070 | 3);
    bar0();
    const unsigned t15 = now32();
    wo_load(0, WN0);
    waitv<WN0>();
    count_in(sb + L_CNT);
    if (lw == 0) lds_wait(sb + L_CNT, 4);
    __builtin_amdgcn_raw_buffer_store_b64(u32x2{t15, T}, rws, tid == 0 ? W_H1 + (cb * 16 + rb) * 256 : OOB, 0, SC1);
    STAMP(12);
    wo_load(WN0, WN1);
    unsigned touch = __builtin_amdgcn_raw_buffer_load_b32(lw & 1 ? rout : rws, lane == 0 ? 0 : OOB, orb * 262144 + ocb * 128, SC1);
    unsigned ha = lane < 16 && lane != rb ? W_H1 + (cb * 16 + lane) * 256 : OOB;
    u32x2 h1;
    {
      const int srow = 4 * g4 + q4;
      const int sw = ((srow >> 1) & 1) | ((srow >> 2) & 2) | (srow & 4);
      unsigned ia[2], va = sb + L_V + (lw >> 1) * 4096 + lane * 16;
#pragma unroll
      for (int ft = 0; ft < 2; ft++) ia[ft] = sb + (1 + (lw >> 1)) * SLOT + 24576 + (lw & 1) * 8192 + srow * 64 + (((4 * ft + p4) ^ sw) << 3);
      unsigned pa = W_P + (cb * 16 + rb) * 8192 + lw * 2048 + g4 * 512 + l15 * 16;
      unsigned za = l15 < 2 ? W_PZ + (cb * 16 + rb) * 512 + lw * 128 + (l15 * 4 + g4) * 16 : OOB;
      u32x4 one4 = {0x3f803f80u, 0x3f803f80u, 0x3f803f80u, 0x3f803f80u};
      f32x4 kv[2][2] = {}, z[2] = {};
      asm volatile("" : "+v"(ia[0]), "+v"(ia[1]), "+v"(va), "+v"(pa), "+v"(za), "+v"(one4), "+v"(ha));
      STAMP(1);
      bar();
      bf16x8 vf[2][2][2];
#pragma unroll
      for (int i = 0; i < 8; i++) vf[i >> 2][(i >> 1) & 1][i & 1] = lds_b128(va + (i >> 2) * 8192 + (i & 3) * 1024);
#pragma unroll
      for (int rt = 0; rt < 2; rt++) {
        if (rt) {
          asm volatile("" : "+v"(kv[0][0]), "+v"(kv[0][1]), "+v"(kv[1][0]), "+v"(kv[1][1]), "+v"(z[0]), "+v"(z[1]));
          __builtin_amdgcn_s_setprio(3);
          STAMP(13);
          bar();
          h1 = __builtin_amdgcn_raw_buffer_load_b64(rws, ha, 0, SC1);
        }
        bf16x8 pf[2][2];
#pragma unroll
        for (int i = 0; i < 4; i++) { const unsigned a = ia[i & 1] + (i >> 1) * 4096 + rt * 2048; pf[i >> 1][i & 1] = lds_tr2(a, a + 1024); }
        if (rt) count_in(sb + L_CNT);
        SB;
#pragma unroll
        for (int h = 0; h < 2; h++)
#pragma unroll
          for (int et = 0; et < 2; et++)
#pragma unroll
            for (int ft = 0; ft < 2; ft++) kv[ft][et] = MFMA16(pf[h][ft], vf[h][rt][et], kv[ft][et]);
        SB;
#pragma unroll
        for (int h = 0; h < 2; h++)
#pragma unroll
          for (int ft = 0; ft < 2; ft++) z[ft] = MFMA16(pf[h][ft], BF8(one4), z[ft]);
        SB;
      }
#pragma unroll
      for (int et = 0; et < 2; et++)
        __builtin_amdgcn_raw_buffer_store_b128(seal4(u32x4{pk2(kv[0][et][0], kv[0][et][1]), pk2(kv[0][et][2], kv[0][et][3]),
                                                           pk2(kv[1][et][0], kv[1][et][1]), pk2(kv[1][et][2], kv[1][et][3])}), rws, pa + et * 256, 0, SC1);
      u32x4 zs;
#pragma unroll
      for (int j = 0; j < 4; j++) zs[j] = seal(__float_as_uint((lane & 1) ? z[1][j] : z[0][j]));
      __builtin_amdgcn_raw_buffer_store_b128(zs, rws, za, 0, SC1);
      SB;
    }
    STAMP(6);
    const unsigned da = sb + L_WO + lw * 32768 + r8 * 128 + ((c8 ^ (r8 & 6)) << 4);
    auto wo_image = [&](int i0, int i1) {
#pragma unroll
      for (int i = i0; i < i1; i++) *LP(u32x4, da + ((i + wrot) & 31) * 1024) = wr_[i];
      SB;
    };
    const unsigned tl = t15 + max(latest(h1, rws, ha, t15, T), 0);
    {
      u32x4 g[2], gz[2];
      const unsigned pa = W_P + (cb * 16 + r8) * 8192 + rb * 512 + lw * 128 + c8 * 16;
      const unsigned za = W_PZ + (cb * 16 + r8) * 512 + lw * 128 + c8 * 16;
      const unsigned ra = (lane & 8) ? OOB : W_R + cb * 8192 + rb * 512 + lw * 128 + c8 * 16 + g4 * 4;
      STAMP(8);
      wait_until(tl + D1);
      STAMP(16);
#pragma unroll
      for (int i = 0; i < 2; i++) g[i] = __builtin_amdgcn_raw_buffer_load_b128(rws, pa + i * 65536, 0, SC1);
#pragma unroll
      for (int i = 0; i < 2; i++) gz[i] = __builtin_amdgcn_raw_buffer_load_b128(rws, za + i * 4096, 0, SC1);
      SB;
      wo_load(WN1, WN2);
      lds_wait(sb + L_CNT, 12);
      wo_image(0, WI1);
      RETRY(31) += settle<2>(g, rws, pa, 65536) ? 1 : 0;
      STAMP(7);
      float s[8];
#pragma unroll
      for (int j = 0; j < 4; j++) {
        s[2 * j] = __uint_as_float(g[0][j] << 16) + __uint_as_float(g[1][j] << 16);
        s[2 * j + 1] = __uint_as_float(g[0][j] & 0xffff0000u) + __uint_as_float(g[1][j] & 0xffff0000u);
      }
      const float a0 = fold16(fold32(s[0], s[4]), fold32(s[2], s[6])), a1 = fold16(fold32(s[1], s[5]), fold32(s[3], s[7]));
      __builtin_amdgcn_raw_buffer_store_b32(seal(pk2(fold8(a0), fold8(a1))), rws, ra, 0, SC1);
      SB;
      STAMP(2);
      settle<2>(gz, rws, za, 4096);
#pragma unroll
      for (int j = 0; j < 4; j++) s[j] = __uint_as_float(gz[0][j]) + __uint_as_float(gz[1][j]);
      float z0 = fold32(s[0], s[2]), z1 = fold32(s[1], s[3]);
      z0 = fold8(fold16(z0, z0)); z1 = fold8(fold16(z1, z1));
      if ((lane & 24) == 0) *LP(unsigned, sb + L_Z + lw * 64 + (c8 & 1) * 32 + ((c8 >> 1) & 1) * 16 + (c8 >> 2) * 8 + hh * 4) = pk2(z0, z1);
      count_in(sb + L_CNT);
      STAMP(3);
      const unsigned ka = W_R + cb * 8192 + lw * 2048 + lane * 16;
      wait_until(tl + D1 + D2);
#pragma unroll
      for (int i = 0; i < 2; i++) g[i] = __builtin_amdgcn_raw_buffer_load_b128(rws, ka + i * 1024, 0, SC1);
      __builtin_amdgcn_raw_buffer_store_b64(u32x2{now32(), T}, rws, tid == 0 ? W_H2 + (rb * 16 + cb) * 256 : OOB, 0, SC1);
      SB;
      wo_load(WN2, 32);
      wo_image(WI1, WI2);
      RETRY(31) += settle<2>(g, rws, ka, 1024) ? 10 : 0;
#pragma unroll
      for (int i = 0; i < 2; i++) *LP(u32x4, sb + L_KV + (lw * 2 + i) * 1024 + lane * 16) = g[i];
    }
    STAMP(4);
    __builtin_amdgcn_s_setprio(0);
    bar();
    ha = tid < 16 ? W_H2 + (orb * 16 + lane) * 256 : OOB;
    h1 = __builtin_amdgcn_raw_buffer_load_b64(rws, ha, 0, SC1);
    __builtin_amdgcn_s_sleep(SLW);
    SB;
    wo_image(WI2, 32);
    asm volatile("" :: "v"(touch));
    STAMP(5);
    asm volatile("" : "+v"(h1));
    if (w == 0) {
      const unsigned t2 = tl + latest(h1, rws, ha, tl, T);
      if (b == 0) __builtin_amdgcn_raw_buffer_store_b32(T, rws, lane == 0 ? W_E : OOB, 0, SC1);
      STAMP(14);
      wait_until(t2 + D3);
      STAMP(15);
      unsigned fa = lane < 16 ? W_F + (orb * 16 + lane) * 256 : OOB;
#pragma unroll 1
      for (int sp = 0; sp < SPIN_CAP; sp++) {
        const u32x4 f = __builtin_amdgcn_raw_buffer_load_b128(rws, fa, 0, SC1);
        asm volatile("" ::: "memory");
        if (f[0] == T && f[1] == T && f[2] == T && f[3] == T) fa = OOB;
        if (__all(fa == OOB)) break;
        RETRY(31) = 100 + RETRY(31) % 100;
        __builtin_amdgcn_s_sleep(PSL);
      }
    }
  } else {
    const int wr = lw >> 1, wc = lw & 1;
    __builtin_amdgcn_s_waitcnt(0x0f70);
    if (lw < 2) {
      const unsigned vo = lw ? (b * 12 + l15) * 128 + g4 * 64 * 6144 : (b * 8 + c8) * 2048 + g4 * 128;
      const u16* base = lw ? wqkv : x;
      if (g4 < IPN && l15 < (lw ? 12 : 8)) {
        unsigned keep;
        asm volatile("s_mov_b32 %0, m0\n\ts_mov_b32 m0, %3\n\ts_nop 0\n\tglobal_load_lds_dword %1, %2\n\ts_mov_b32 m0, %0"
                     : "=&s"(keep) : "v"(vo), "s"(base), "s"(sb + L_DUMMY) : "memory");
      }
    }
    f32x4 acc[6][4] = {};
    unsigned xa[2], wa[2];
#pragma unroll
    for (int ks = 0; ks < 2; ks++) xa[ks] = sb + (wr * 64 + l15) * 128 + (((4 * ks + g4) ^ (l15 >> 1)) << 4);
#pragma unroll
    for (int nt = 0; nt < 2; nt++) wa[nt] = sb + 16384 + (8 * g4 + q4) * 128 + (((wc * 2 + nt) ^ ((q4 >> 1) | ((g4 & 1) << 1))) << 5) + p4 * 8;
    bf16x8 fx[2][4], fw[2][6];
    unsigned xs[2], wsl[2];
    auto rdw = [&](int buf, int j, int ks) {
      const unsigned a = wsl[j & 1] + (j < 4 ? 8192 + (j >> 1) * 8192 : 0) + ks * 4096;
      fw[buf][j] = lds_tr2(a, a + 512);
    };
    auto rd = [&](int buf, int ks, unsigned so, int j0, int j1) {
      if (ks == 0) {
        xs[0] = xa[0] + so; xs[1] = xa[1] + so; wsl[0] = wa[0] + so; wsl[1] = wa[1] + so;
        asm volatile("" : "+v"(xs[0]), "+v"(xs[1]), "+v"(wsl[0]), "+v"(wsl[1]));
      }
      rdw(buf, j0, ks);
#pragma unroll
      for (int m = 0; m < 4; m++) fx[buf][m] = lds_b128(xs[ks] + m * 2048);
#pragma unroll
      for (int j = j0 + 1; j < j1; j++) rdw(buf, j, ks);
    };
    auto mm = [&](int buf, int j0, int j1) {
#pragma unroll
      for (int j = j0; j < j1; j++)
#pragma unroll
        for (int m0 = 0; m0 < 4; m0++) {
          const int m = (j & 1) ? 3 - m0 : m0;
          acc[j][m] = (j == 2 || j == 3) ? MFMA16(fx[buf][m], fw[buf][j], acc[j][m]) : MFMA16(fw[buf][j], fx[buf][m], acc[j][m]);
        }
    };
#define SCHED(nds, nmf) do { _Pragma("unroll") for (int i_ = 0; i_ < (nds); i_++) { __builtin_amdgcn_sched_group_barrier(0x8, 1, 0); __builtin_amdgcn_sched_group_barrier(0x100, 1, 0); } \
      if ((nmf) > (nds)) __builtin_amdgcn_sched_group_barrier(0x8, (nmf) - (nds), 0); SB; } while (0)
#pragma unroll
    for (int j = 0; j < 6; j++) asm volatile("" : "+v"(acc[j][0]), "+v"(acc[j][1]), "+v"(acc[j][2]), "+v"(acc[j][3]));
    unsigned pia[4];
    {
      const int sw = ((l31 >> 1) & 1) | ((l31 >> 2) & 2) | (l31 & 4);
#pragma unroll
      for (int q = 0; q < 4; q++) pia[q] = sb + (1 + wc) * SLOT + 24576 + (wr * 64 + l31) * 64 + (((2 * q + hh) ^ sw) << 3);
      asm volatile("" : "+v"(pia[0]), "+v"(pia[1]), "+v"(pia[2]), "+v"(pia[3]));
    }
    const unsigned T = epoch_tag(ws);
    bar();
    STAMP(1);
    unsigned so = 0;
    rd(0, 0, so, 0, 6);
    SB;
#pragma unroll 1
    for (int t = 0; t < 12; t++) {
      rd(1, 1, so, 0, 6); mm(0, 0, 6); SCHED(16, 24);
      bar();
      so = so == 2 * SLOT ? 0 : so + SLOT;
      rd(0, 0, so, 0, 6); mm(1, 0, 6); SCHED(16, 24);
    }
    rd(1, 1, 0, 0, 6); mm(0, 0, 6); SCHED(16, 24);
    bar();
    rd(0, 0, SLOT, 0, 4); mm(1, 0, 6); SCHED(12, 24);
    rd(1, 1, SLOT, 0, 4); mm(0, 0, 4); SCHED(12, 16);
    bar();
    rd(0, 0, 2 * SLOT, 0, 4); mm(1, 0, 4); SCHED(12, 16);
    rd(1, 1, 2 * SLOT, 0, 4); mm(0, 0, 4); SCHED(12, 16);
    bar();
    rd(0, 0, 0, 0, 4); mm(1, 0, 4); SCHED(12, 16);
    rd(1, 1, 0, 0, 4); mm(0, 0, 4); SCHED(12, 16);
    bf16x8 wp[2];
    {
      const unsigned pa = sb + L_PHI + wc * 2048 + (8 * hh + q4) * 64 + (g4 & 1) * 32 + p4 * 8;
      wp[0] = lds_tr2(pa, pa + 256); wp[1] = lds_tr2(pa + 1024, pa + 1280);
    }
    mm(1, 0, 4);
    STAMP(2);
    const f32x16 zero = {};
    f32x16 pk[2];
#pragma unroll
    for (int rt = 0; rt < 2; rt++) {
      pk[rt] = MFMA32(wp[0], pack16to32(acc[0][2 * rt], acc[0][2 * rt + 1]), zero);
      pk[rt] = MFMA32(wp[1], pack16to32(acc[1][2 * rt], acc[1][2 * rt + 1]), pk[rt]);
    }
#pragma unroll
    for (int rt = 0; rt < 2; rt++)
#pragma unroll
      for (int et = 0; et < 2; et++) {
        const f32x4 a = acc[2 + et][2 * rt], c = acc[2 + et][2 * rt + 1];
        *LP(u32x4, sb + L_V + lw * 4096 + (rt * 2 + et) * 1024 + lane * 16) = u32x4{pk2(a[0], a[1]), pk2(a[2], a[3]), pk2(c[0], c[1]), pk2(c[2], c[3])};
      }
#pragma unroll
    for (int rt = 0; rt < 2; rt++) {
      unsigned ph[2][8];
      dual_softmax(pk[rt], ph[0], ph[1]);
#pragma unroll
      for (int q = 0; q < 4; q++) {
        *LP(u32x2, pia[q] + rt * 2048) = u32x2{ph[0][2 * q], ph[0][2 * q + 1]};
        *LP(u32x2, pia[q] + rt * 2048 + 8192) = u32x2{ph[1][2 * q], ph[1][2 * q + 1]};
      }
      if (rt) STAMP(3);
      bar();
    }
    STAMP(4);
    SB;
    rd(0, 0, SLOT, 4, 6);
    SB;
    rd(1, 1, SLOT, 4, 6); mm(0, 4, 6); SCHED(8, 8);
    rd(0, 0, 2 * SLOT, 4, 6); mm(1, 4, 6); SCHED(8, 8);
    rd(1, 1, 2 * SLOT, 4, 6); mm(0, 4, 6); SCHED(8, 8);
    rd(0, 0, 0, 4, 6); mm(1, 4, 6); SCHED(8, 8);
    rd(1, 1, 0, 4, 6); mm(0, 4, 6); SCHED(8, 8);
    count_in(sb + L_CNT);
    mm(1, 4, 6);
    u32x4 phq[2][4];
#pragma unroll
    for (int rt = 0; rt < 2; rt++) {
      f32x16 p = MFMA32(wp[0], pack16to32(acc[4][2 * rt], acc[4][2 * rt + 1]), zero);
      p = MFMA32(wp[1], pack16to32(acc[5][2 * rt], acc[5][2 * rt + 1]), p);
      unsigned ph[2][8];
      dual_softmax(p, ph[0], ph[1]);
#pragma unroll
      for (int i = 0; i < 4; i++) { const int sg = i >> 1, st = i & 1; phq[rt][i] = u32x4{ph[sg][2 * st], ph[sg][2 * st + 1], ph[sg][4 + 2 * st], ph[sg][5 + 2 * st]}; }
      asm volatile("" : "+v"(phq[rt][0]), "+v"(phq[rt][1]), "+v"(phq[rt][2]), "+v"(phq[rt][3]));
    }
    unsigned ka = sb + L_KV + wc * 4096 + lane * 16, zl = sb + L_Z + wc * 128 + hh * 32;
    unsigned oa = W_O + ((rb * 8 + wr * 4 + (g4 & 1)) * 32 + cb * 2 + wc) * 1024 + hh * 256 + l15 * 16, oa1 = oa + 65536;
    unsigned fa = lane == 0 ? W_F + (rb * 16 + cb) * 256 + lw * 4 : OOB, Tv = T;
    asm volatile("" : "+v"(ka), "+v"(zl), "+v"(oa), "+v"(oa1), "+v"(fa), "+v"(Tv));
    STAMP(5);
    lds_wait(sb + L_CNT, 16);
    float rden[2];
    {
      u32x4 zf[4];
#pragma unroll
      for (int i = 0; i < 4; i++) zf[i] = *LP(const u32x4, zl + (i >> 1) * 64 + (i & 1) * 16);
      float dn[2][2] = {};
#pragma unroll
      for (int i = 0; i < 4; i++)
#pragma unroll
        for (int j = 0; j < 4; j++)
#pragma unroll
          for (int rt = 0; rt < 2; rt++) dn[rt][j & 1] = dot2(phq[rt][i][j], zf[i][j], dn[rt][j & 1]);
#pragma unroll
      for (int rt = 0; rt < 2; rt++) { const float d = dn[rt][0] + dn[rt][1]; rden[rt] = __builtin_amdgcn_rcpf(fold32(d, d) + 1e-6f); }
      asm volatile("" : "+v"(rden[0]), "+v"(rden[1]));
    }
    STAMP(16);
    __builtin_amdgcn_s_setprio(2);
    bar();
    STAMP(6);
    {
      bf16x8 kf[4];
#pragma unroll
      for (int i = 0; i < 4; i++) kf[i] = lds_b128(ka + i * 1024);
      auto put = [&](const f32x16& n, float r, int st, unsigned a) {
        const f32x2 r2 = {r, r};
        u32x4 v;
#pragma unroll
        for (int j = 0; j < 4; j++) v[j] = pk2v(f32x2{n[8 * st + 2 * j], n[8 * st + 2 * j + 1]} * r2);
        __builtin_amdgcn_raw_buffer_store_b128(v, rws, a + st * 512, 0, SC1);
      };
      SB;
      f32x16 n0 = zero, n1 = zero;
#pragma unroll
      for (int i = 0; i < 4; i++) { n0 = MFMA32(kf[i], BF8(phq[0][i]), n0); SB; }
#pragma unroll
      for (int i = 0; i < 3; i++) { n1 = MFMA32(kf[i], BF8(phq[1][i]), n1); SB; }
      put(n0, rden[0], 0, oa); SB;
      n1 = MFMA32(kf[3], BF8(phq[1][3]), n1); SB;
      put(n0, rden[0], 1, oa); SB;
      put(n1, rden[1], 0, oa1); SB; put(n1, rden[1], 1, oa1); SB;
    }
    STAMP(7);
    waitv<0>();
    __builtin_amdgcn_raw_buffer_store_b32(Tv, rws, fa, 0, SC1);
    __builtin_amdgcn_s_setprio(0);
    STAMP(8);
  }
  {
    const int rg = lw, kh = w >> 2;
    const int kr = 16 * (g4 >> 1) + 4 * (g4 & 1) + q4;
    unsigned wa[4];
#pragma unroll
    for (int nt = 0; nt < 4; nt++) wa[nt] = sb + L_WO + kh * 65536 + kr * 128 + ((nt ^ ((kr >> 1) & 3)) << 5) + p4 * 8;
    const unsigned ob = W_O + ((orb * 8 + rg * 2) * 32 + kh * 16) * 1024;
    constexpr int LAG = OLAG, PF = OPF;
    f32x4 c0[4] = {}, c1[4] = {}, px[4];
    u32x4 o0[PF + 1], o1[PF + 1];
    bf16x8 a[LAG + 2][4];
    unsigned v0 = lane * 16 + kh * 32768, v1 = lane * 16 + (kh ^ 1) * 32768;
    unsigned xw = sb + L_FX + w * 4096 + lane * 16, xr = sb + L_FX + (w ^ 4) * 4096 + lane * 16;
    unsigned oa = (orb * 128 + rg * 32 + kh * 16 + r8) * 2048 + ocb * 128 + c8 * 16;
    unsigned tw = sb + L_WO + w * 2048 + l15 * 128 + (((g4 >> 1) ^ (l15 & 7)) << 4) + (((g4 & 1) ^ (l15 >> 3)) << 3);
    unsigned tr = sb + L_WO + w * 2048 + r8 * 128 + ((c8 ^ r8) << 4);
    asm volatile("" : "+v"(wa[0]), "+v"(wa[1]), "+v"(wa[2]), "+v"(wa[3]), "+v"(v0), "+v"(v1), "+v"(xw), "+v"(xr), "+v"(oa), "+v"(tw), "+v"(tr));
    asm volatile("" : "+v"(c0[0]), "+v"(c0[1]), "+v"(c0[2]), "+v"(c0[3]), "+v"(c1[0]), "+v"(c1[1]), "+v"(c1[2]), "+v"(c1[3]));
    auto ldo = [&](int i) {
      if (i < 16) o1[i % (PF + 1)] = __builtin_amdgcn_raw_buffer_load_b128(rws, v1, ob + i * 1024, SC1);
      if (i >= LAG && i < 16 + LAG) o0[i % (PF + 1)] = __builtin_amdgcn_raw_buffer_load_b128(rws, v0, ob + (i - LAG) * 1024, SC1);
    };
    auto ldw = [&](int i) {
#pragma unroll
      for (int nt = 0; nt < 4; nt++) a[i % (LAG + 2)][nt] = lds_tr2(wa[nt] + i * 4096, wa[nt] + i * 4096 + 1024);
    };
    bar();
    STAMP(9);
#pragma unroll
    for (int i = 0; i <= PF; i++) { ldo(i); SB; }
    SB;
    ldw(0);
    SB;
#pragma unroll
    for (int i = 0; i < 16 + LAG; i++) {
      if (i) ldo(i + PF);
      SB;
      if (i < 15) ldw(i + 1);
      if (i < 16) {
#pragma unroll
        for (int nt = 0; nt < 4; nt++) c1[nt] = MFMA16(a[i % (LAG + 2)][nt], BF8(o1[i % (PF + 1)]), c1[nt]);
      }
      if (i >= LAG) {
#pragma unroll
        for (int nt = 0; nt < 4; nt++) c0[nt] = MFMA16(a[(i - LAG) % (LAG + 2)][nt], BF8(o0[i % (PF + 1)]), c0[nt]);
      }
#pragma unroll
      for (int q = 0; q < 8; q++) { __builtin_amdgcn_sched_group_barrier(0x8, 1, 0); __builtin_amdgcn_sched_group_barrier(0x100, 1, 0); }
      SB;
      if (i == 15) {
        STAMP(10);
#pragma unroll
        for (int nt = 0; nt < 4; nt++) *LP(f32x4, xw + nt * 1024) = c1[nt];
      }
      if (i == (LAG ? 14 + LAG : 15)) {
        bar();
#pragma unroll
        for (int nt = 0; nt < 4; nt++) px[nt] = *LP(const f32x4, xr + nt * 1024);
        SB;
      }
    }
#pragma unroll
    for (int nt = 0; nt < 4; nt++) {
      const f32x4 v = px[nt] + c0[nt];
      *LP(u32x2, tw ^ (nt << 5)) = u32x2{pk2(v[0], v[1]), pk2(v[2], v[3])};
    }
    asm volatile("" ::: "memory");
    const u32x4 t0 = *LP(const u32x4, tr), t1 = *LP(const u32x4, tr + 1024);
    __builtin_amdgcn_raw_buffer_store_b128(t0, rout, oa, 0, SC1);
    __builtin_amdgcn_raw_buffer_store_b128(u32x4{t1[2], t1[3], t1[0], t1[1]}, rout, oa + 16384, 0, SC1);
    asm volatile(".Lhedgehog_end:");
    STAMP(11);
    STAMP(30);
    STAMP_DUMP();
  }
}

__global__ void probe(const char* p, unsigned* sink) {
  unsigned acc = 0;
  for (int i = 0; i < 32; i++) { unsigned v; asm volatile("global_load_dword %0, %1, off\n\ts_waitcnt vmcnt(0)" : "=v"(v) : "v"(p + (size_t)i * 2097280) : "memory"); acc += v; }
  if (acc == 0x12345678u) *sink = acc;
}
extern "C" void kernel_launch(void* const* d_in, const int* in_sizes, int n_in, void* d_out, int out_size, void* d_ws, size_t ws_size,
                              hipStream_t stream) {
  const size_t ws_off = WSO < 0 ? (ws_size - (size_t)(-WSO) * (1u << 20)) & ~(size_t)((2u << 20) - 1) : (size_t)WSO << 20;
  (void)hipFuncSetAttribute((const void*)hedgehog_fused, hipFuncAttributeMaxDynamicSharedMemorySize, LDS_BYTES);
  probe<<<1, 64, 0, stream>>>((const char*)d_ws, (unsigned*)((char*)d_ws + (64u << 20)));
  hedgehog_fused<<<256, 512, LDS_BYTES, stream>>>((const u16*)d_in[0], (const u16*)d_in[1], (const u16*)d_in[2], (const u16*)d_in[3],
                                                   (u16*)d_out, (char*)d_ws + ws_off);
}
```

```cpp
#define TLBT 0
#include <hip/hip_runtime.h>
#include <hip/hip_bf16.h>

typedef unsigned short u16;
typedef unsigned long long u64;
typedef __attribute__((ext_vector_type(8))) __bf16 bf16x8;
typedef __attribute__((ext_vector_type(4))) __bf16 bf16x4;
typedef __attribute__((ext_vector_type(2))) __bf16 bf16x2;
typedef __attribute__((ext_vector_type(16))) float f32x16;
typedef __attribute__((ext_vector_type(4))) float f32x4;
typedef __attribute__((ext_vector_type(2))) float f32x2;
typedef __attribute__((ext_vector_type(4))) unsigned u32x4;
typedef __attribute__((ext_vector_type(2))) unsigned u32x2;
typedef __amdgpu_buffer_rsrc_t rsrc_t;
#define LAS __attribute__((address_space(3)))
#define LP(T, a) ((LAS T*)(size_t)(a))
#define DI __device__ __forceinline__
#define MFMA32(a, b, c) __builtin_amdgcn_mfma_f32_32x32x16_bf16((a), (b), (c), 0, 0, 0)
#define MFMA16(a, b, c) __builtin_amdgcn_mfma_f32_16x16x32_bf16((a), (b), (c), 0, 0, 0)
#define SB __builtin_amdgcn_sched_barrier(0)
#define BF8(v) __builtin_bit_cast(bf16x8, v)
constexpr int SC1 = 16;
constexpr unsigned OOB = 0x80000000u;

#define VPF 3
#define IPN 2
#define WPT 9
#define D1 120
#define D2 20
#define D3 90
#define PSL 2
#define HSL 8
#define OLAG 2
#define OPF 2
#define WSO -12
#define CL0 1
#define SLW 3
#define WN0 4
#define WN1 18
#define WN2 22
#define CLO 0
#define PULL 0
#define WSM 1
#define XFIRST 0
#define OS 2
#define WNA 10
#define WI3 20
#define HY 1
#define D2B 70
#define IP0 0
#define UNR 3
#define QD 0
#define WI1 6
#define WI2 10

constexpr int SLOT = 40960;
constexpr int L_WO = 0;
constexpr int L_V = 122880;
constexpr int L_PHI = 147456;
constexpr int L_Z = 151552;
constexpr int L_CNT = 151808;
constexpr int L_YC = 151816;
constexpr int L_KV = 139264;
constexpr int L_DUMMY = 152064;
constexpr int L_FX = 131072;
constexpr int LDS_BYTES = 163840;
constexpr unsigned W_O = 0;
constexpr unsigned W_P = 8u << 20;
constexpr unsigned W_PZ = 10u << 20;
constexpr unsigned W_R = W_PZ + (128u << 10);
constexpr unsigned W_H1 = W_R + (128u << 10);
constexpr unsigned W_H2 = W_H1 + (64u << 10);
constexpr unsigned W_F = W_H2 + (64u << 10);
constexpr unsigned W_E = W_F + (64u << 10);
constexpr unsigned W_TS = 9u << 20;
constexpr unsigned W_BYTES = 12u << 20;
constexpr int SPIN_CAP = 1 << 18;

DI void bar() { asm volatile("s_waitcnt lgkmcnt(0)\n\ts_barrier" ::: "memory"); }
DI void bar0() { asm volatile("s_barrier" ::: "memory"); }
template <int N> DI void waitv() { asm volatile("s_waitcnt vmcnt(%0)" ::"i"(N) : "memory"); }
DI unsigned pk2v(f32x2 v) { return __builtin_bit_cast(unsigned, __builtin_convertvector(v, bf16x2)); }
DI unsigned pk2(float a, float b) { return pk2v(f32x2{a, b}); }
DI bf16x8 pack16to32(const f32x4& c0, const f32x4& c1) {
  auto a = __builtin_amdgcn_permlane16_swap(pk2(c0[0], c0[1]), pk2(c1[0], c1[1]), false, false);
  auto b = __builtin_amdgcn_permlane16_swap(pk2(c0[2], c0[3]), pk2(c1[2], c1[3]), false, false);
  return BF8((u32x4{a[0], b[0], a[1], b[1]}));
}
DI float fold32(float a, float b) { auto r = __builtin_amdgcn_permlane32_swap(__float_as_uint(a), __float_as_uint(b), false, false); return __uint_as_float(r[0]) + __uint_as_float(r[1]); }
DI float fold16(float a, float b) { auto r = __builtin_amdgcn_permlane16_swap(__float_as_uint(a), __float_as_uint(b), false, false); return __uint_as_float(r[0]) + __uint_as_float(r[1]); }
DI float fold8(float a) { return a + __uint_as_float(__builtin_amdgcn_update_dpp(0u, __float_as_uint(a), 0x128, 0xf, 0xf, false)); }
DI float dot2(unsigned a, unsigned b, float c) { return __builtin_amdgcn_fdot2_f32_bf16(__builtin_bit_cast(bf16x2, a), __builtin_bit_cast(bf16x2, b), c, false); }
DI bf16x8 lds_b128(unsigned a) { return *LP(const bf16x8, a); }
DI bf16x8 lds_tr2(unsigned a0, unsigned a1) {
  bf16x4 lo = __builtin_amdgcn_ds_read_tr16_b64_v4bf16(LP(bf16x4, a0));
  bf16x4 hi = __builtin_amdgcn_ds_read_tr16_b64_v4bf16(LP(bf16x4, a1));
  return __builtin_shufflevector(lo, hi, 0, 1, 2, 3, 4, 5, 6, 7);
}
DI void dual_softmax(const f32x16& p, unsigned (&pos)[8], unsigned (&neg)[8]) {
#define FMX __builtin_elementwise_maximum
#define FMN __builtin_elementwise_minimum
  float mx = FMX(FMX(p[0], p[1]), p[2]), mn = FMN(FMN(p[0], p[1]), p[2]), mx2 = FMX(FMX(p[3], p[4]), p[5]), mn2 = FMN(FMN(p[3], p[4]), p[5]);
#pragma unroll
  for (int i = 6; i < 14; i += 4) {
    mx = FMX(FMX(mx, p[i]), p[i + 1]); mn = FMN(FMN(mn, p[i]), p[i + 1]);
    mx2 = FMX(FMX(mx2, p[i + 2]), p[i + 3]); mn2 = FMN(FMN(mn2, p[i + 2]), p[i + 3]);
  }
  mx = FMX(FMX(mx, p[14]), p[15]); mn = FMN(FMN(mn, p[14]), p[15]);
  mx = FMX(mx, mx2); mn = FMN(mn, mn2);
  {
    auto r = __builtin_amdgcn_permlane32_swap(__float_as_uint(mx), __float_as_uint(mx), false, false);
    auto s = __builtin_amdgcn_permlane32_swap(__float_as_uint(mn), __float_as_uint(mn), false, false);
    mx = FMX(__uint_as_float(r[0]), __uint_as_float(r[1])); mn = FMN(__uint_as_float(s[0]), __uint_as_float(s[1]));
  }
  const float c = 1.44269504088896341f;
  const f32x2 cp = {c, c}, cn = {-c, -c}, bx = {-mx * c, -mx * c}, bn = {mn * c, mn * c};
  f32x2 ep[8], en[8];
#pragma unroll
  for (int i = 0; i < 8; i++) {
    const f32x2 v = {p[2 * i], p[2 * i + 1]};
    const f32x2 a = __builtin_elementwise_fma(v, cp, bx), b = __builtin_elementwise_fma(v, cn, bn);
    ep[i] = f32x2{__builtin_amdgcn_exp2f(a[0]), __builtin_amdgcn_exp2f(a[1])};
    en[i] = f32x2{__builtin_amdgcn_exp2f(b[0]), __builtin_amdgcn_exp2f(b[1])};
  }
  const f32x2 s2p = ((ep[0] + ep[1]) + (ep[2] + ep[3])) + ((ep[4] + ep[5]) + (ep[6] + ep[7]));
  const f32x2 s2n = ((en[0] + en[1]) + (en[2] + en[3])) + ((en[4] + en[5]) + (en[6] + en[7]));
  const float sp = s2p[0] + s2p[1], sn = s2n[0] + s2n[1];
  const float rp = __builtin_amdgcn_rcpf(fold32(sp, sp)), rn = __builtin_amdgcn_rcpf(fold32(sn, sn));
  const f32x2 r2p = {rp, rp}, r2n = {rn, rn};
#pragma unroll
  for (int i = 0; i < 8; i++) { pos[i] = pk2v(ep[i] * r2p); neg[i] = pk2v(en[i] * r2n); }
}

constexpr unsigned EMPTY = 0xffffffffu;
DI unsigned seal(unsigned v) { return min(v, EMPTY - 1u); }
DI u32x4 seal4(u32x4 v) { return u32x4{seal(v[0]), seal(v[1]), seal(v[2]), seal(v[3])}; }
DI unsigned umax4(unsigned m, u32x4 v) { return max(max(max(max(m, v[0]), v[1]), v[2]), v[3]); }
template <int N> DI int settle(u32x4 (&g)[N], rsrc_t r, unsigned a, unsigned stride) {
  auto pending = [&]() { unsigned m = 0;
#pragma unroll
    for (int i = 0; i < N; i++) m = umax4(m, g[i]);
    return __any(m == EMPTY); };
  if (__builtin_expect(!pending(), 1)) return 0;
  int sp = 1;
#pragma unroll 1
  for (; sp < SPIN_CAP; sp++) {
    __builtin_amdgcn_s_sleep(PSL);
    asm volatile("" ::: "memory");
#pragma unroll
    for (int i = 0; i < N; i++) g[i] = __builtin_amdgcn_raw_buffer_load_b128(r, a + i * stride, 0, SC1);
    if (!pending()) break;
  }
  return sp;
}
DI unsigned now32() { u64 t; asm volatile("s_memrealtime %0\n\ts_waitcnt lgkmcnt(0)" : "=s"(t) :: "memory"); return (unsigned)t; }
DI void wait_until(unsigned t) {
#pragma unroll 1
  for (int sp = 0; sp < 1000; sp++) { if (t - now32() - 1u >= 1000u) break; }
}
DI int latest(u32x2 h, rsrc_t r, unsigned a, unsigned t0, unsigned T) {
  int d = -0x10000;
  if (a != OOB && h[1] == T) { d = (int)(h[0] - t0); a = OOB; }
  if (!__all(a == OOB)) {
#pragma unroll 1
    for (int sp = 0; sp < SPIN_CAP; sp++) {
      asm volatile("" ::: "memory");
      h = __builtin_amdgcn_raw_buffer_load_b64(r, a, 0, SC1);
      if (a != OOB && h[1] == T) { d = (int)(h[0] - t0); a = OOB; }
      if (__all(a == OOB)) break;
      __builtin_amdgcn_s_sleep(PSL);
    }
  }
  d = max(d, __builtin_amdgcn_update_dpp(d, d, 0x128, 0xf, 0xf, false));
  d = max(d, __builtin_amdgcn_update_dpp(d, d, 0x124, 0xf, 0xf, false));
  d = max(d, __builtin_amdgcn_update_dpp(d, d, 0x122, 0xf, 0xf, false));
  d = max(d, __builtin_amdgcn_update_dpp(d, d, 0x121, 0xf, 0xf, false));
  asm volatile("" ::: "memory");
  return __builtin_amdgcn_readfirstlane(d);
}
DI void count_in(unsigned lds_addr, unsigned n = 1) {
  asm volatile("s_mov_b64 exec, 1\n\tds_add_u32 %0, %1\n\ts_mov_b64 exec, -1" :: "v"(lds_addr), "v"(n) : "memory");
}
template <int SL = 1> DI void lds_wait(unsigned lds_addr, unsigned n, unsigned mask = ~0u) {
#pragma unroll 1
  for (int sp = 0; sp < SPIN_CAP * 8; sp++) { if ((__builtin_amdgcn_readfirstlane(*LP(const volatile unsigned, lds_addr)) & mask) >= n) break; __builtin_amdgcn_s_sleep(SL); }
  asm volatile("" ::: "memory");
}
DI unsigned epoch_tag(const char* ws) {
  unsigned e;
  asm volatile("s_load_dword %0, %1, 0x0\n\ts_waitcnt lgkmcnt(0)" : "=s"(e) : "s"(ws + W_E) : "memory");
  return e + 1u;
}

#define STAMP(i)
#define STAMPV(i)
#define RETRY(i) retry_
#define STAMP_DUMP()

__global__ __launch_bounds__(512, 2) void hedgehog_fused(const u16* __restrict__ x, const u16* __restrict__ wqkv, const u16* __restrict__ wphi,
                                                         const u16* __restrict__ wo, u16* __restrict__ out, char* __restrict__ ws) {
  extern __shared__ __attribute__((aligned(16))) char smem[];
  const int tid = threadIdx.x, lane = tid & 63;
  const int w = __builtin_amdgcn_readfirstlane(tid >> 6), lw = w & 3;
  const int b = blockIdx.x, xcd = b & 7, idx = b >> 3;
  const int rl = idx & 7, cl = idx >> 3;
  const int rb = (xcd & 1) * 8 + rl, cb = (xcd >> 1) * 4 + cl;
  const int orb = (xcd >> 1) * 4 + (idx & 3), ocb = (xcd & 1) * 8 + (idx >> 2);
  const unsigned sb = (unsigned)(size_t)(LAS char*)smem;
  const int hh = lane >> 5, l31 = lane & 31, l15 = lane & 15, g4 = lane >> 4, q4 = l15 >> 2, p4 = lane & 3, r8 = lane >> 3, c8 = lane & 7;
  unsigned st_r = 0, st_c = 0, retry_ = 0; (void)st_r; (void)st_c; (void)retry_;
  const unsigned pw = lw & 1;
  unsigned sp = (b * 2 + (lw >> 1)) * (8192 - pw * 7424);
  unsigned vp = __umul24(l15, 2048 - pw * 1920) + __umul24(g4, 128 + pw * (64 * 6144 - 128));
  vp = g4 < IPN && g4 >= IP0 && l15 < 4 + 2 * pw ? vp : OOB;
  unsigned vx0 = r8 * 2048 + ((c8 ^ (r8 >> 1)) << 4), vx1 = r8 * 2048 + ((c8 ^ (4 + (r8 >> 1))) << 4);
  unsigned vw0 = r8 * 6144 + ((c8 ^ (r8 & 2)) << 4), vw1 = r8 * 6144 + ((c8 ^ (4 + (r8 & 2))) << 4);
  unsigned sx = (rb * 128 + lw * 32) * 2048, sw = cb * 128 + lw * 16 * 6144;
  asm volatile("" : "+v"(vp), "+v"(vx0), "+v"(vx1), "+v"(vw0), "+v"(vw1));
  asm volatile("" : "+s"(sx), "+s"(sw), "+s"(sp));
  asm volatile("" :: "s"(x), "s"(wqkv), "s"(wphi), "s"(wo), "s"(out), "s"(ws));
  STAMP(0);
  const rsrc_t rws = __builtin_amdgcn_make_buffer_rsrc(ws, 0, W_BYTES, 0x00020000);
  const rsrc_t rx = __builtin_amdgcn_make_buffer_rsrc((void*)x, 0, 2048 * 1024 * 2, 0x00020000);
  const rsrc_t rw = __builtin_amdgcn_make_buffer_rsrc((void*)wqkv, 0, 1024 * 3072 * 2, 0x00020000);
  const rsrc_t rout = __builtin_amdgcn_make_buffer_rsrc(out, 0, 2048 * 1024 * 2, 0x00020000);

  auto stage = [&](int t, int slot) {
    LAS char* dst = (LAS char*)smem + slot * SLOT;
#define LX(i) __builtin_amdgcn_raw_ptr_buffer_load_lds(rx, dst + (lw * 4 + i) * 1024, 16, (i & 1) ? vx1 : vx0, sx + i * 16384 + t * 128, 0, 0)
#define LW(i) __builtin_amdgcn_raw_ptr_buffer_load_lds(rw, dst + 16384 + (i >> 1) * 8192 + (lw * 2 + (i & 1)) * 1024, 16, (i & 1) ? vw1 : vw0, \
                                                     sw + (i >> 1) * 2048 + (t * 64 + (i & 1) * 8) * 6144, 0, 0)
    LW(2); LX(0); LW(3); LX(1); LW(4); LX(2); LW(5); LX(3);
    if (!QD || t < 13) { LW(0); LW(1); }
  };
  auto stage_q = [&](int t, int slot) { LAS char* dst = (LAS char*)smem + slot * SLOT; LW(0); LW(1); };
  u64 pc; unsigned code_len;
  asm volatile("s_getpc_b64 %0\n.Lhedgehog_begin:\n\ts_mov_b32 %1, .Lhedgehog_end-.Lhedgehog_begin" : "=s"(pc), "=s"(code_len));
  if (w < 4) {
    __builtin_amdgcn_raw_ptr_buffer_load_lds(lw & 1 ? rw : rx, (LAS char*)smem + L_DUMMY, 4, vp, sp, 0, 0);
    stage(0, 0);
    SB;
    const int turn = (rl ^ xcd) & 7, u = cl * 4 + lw, l3 = lane / 3;
    const unsigned px = lane < 16 ? lane * 2048 + ((xcd & 1) * 1024 + ((xcd >> 1) * 16 + u) * 16) * 2048 : OOB;
    unsigned pa_ = lane * 128 + l3 * (2048 - 384) + ((xcd & 1) * 96 + u * 6) * 2048, pb_ = lane * 2048 + (u * 12 - 18) * 2048 + 384;
    asm volatile("" : "+v"(pa_), "+v"(pb_));
    const unsigned pq = (lane < 18 ? pa_ : lane < 30 ? pb_ : OOB) + (xcd >> 1) * 512;
    const int uc = idx * 4 + lw;
    const char* sc = (const char*)(pc & ~127ull) + (uc * 256 < code_len ? uc : 0) * 256;
    unsigned o0 = cb * 16 + rb, o1 = rb * 16 + cb, o2 = W_H1 + lw * 65536;
    asm volatile("" : "+s"(o0), "+s"(o1), "+s"(o2));
    const char* s1 = (lw == 3 ? (const char*)wphi : ws) + (lw == 3 ? b * 256 : o2 + (lw == 0 ? o0 : o1) * 256);
    unsigned o3 = W_P + o0 * 8192, o4 = (lw == 1 ? rb : orb) * 262144 + cb * 2048;
    asm volatile("" : "+s"(o3), "+s"(o4));
    const char* s2 = (lw == 3 ? (const char*)out : ws) + (lw == 0 ? o3 : lw == 3 ? o4 : o4 * OS);
    int slot = 1, extra = 0;
#pragma unroll 1
    for (int t = -1; t < 14; t++) {
      if (t >= 0) {
        if (QD && t >= 12) extra--;
        if (extra == 0) waitv<10>(); else if (extra > 0) waitv<12>(); else waitv<8>();
        bar0();
      }
      STAMPV(t + 16);
      const int s = t < 0 ? turn : t + VPF;
      extra = t < 0 ? turn >= IPN && turn < VPF : s >= IPN && s < 16 && (s & 7) == turn;
      if (extra) {
        __builtin_amdgcn_raw_ptr_buffer_load_lds(rx, (LAS char*)smem + L_DUMMY, 4, px, s * 128, 0, 0);
        __builtin_amdgcn_raw_ptr_buffer_load_lds(rw, (LAS char*)smem + L_DUMMY, 4, pq, s * 64 * 6144, 0, 0);
      }
      stage(t + 2, slot);
      slot = slot == 2 ? 0 : slot + 1;
      if (t < 10) {
        asm volatile("s_load_dwordx2 s[100:101], %0, 0x3c\n\ts_load_dwordx2 s[100:101], %0, 0xbc" :: "s"(sc) : "s100", "s101");
        sc = t < 0 ? s1 : t == 0 ? (TLBT ? s2 : s1) : t == 1 ? (const char*)wo + (b * 4 + lw) * 2048 : sc + 256;
      }
    }
    const rsrc_t rp = __builtin_amdgcn_make_buffer_rsrc((void*)wphi, 0, 32 * 1024 * 2, 0x00020000);
    __builtin_amdgcn_raw_ptr_buffer_load_lds(rp, (LAS char*)smem + L_PHI + lw * 1024, 16, lane * 16, cb * 4096 + lw * 1024, 0, 0);
    waitv<QD ? 9 : 11>(); bar0();
    if (QD == 1) { stage_q(13, 1); stage_q(14, 2); stage_q(15, 0); }
    waitv<QD == 1 ? 6 : 0>(); bar0();
  }
  int role = w; asm volatile("" : "+s"(role));

  if (role < 4) {
    const rsrc_t ro = __builtin_amdgcn_make_buffer_rsrc((void*)wo, 0, 1024 * 1024 * 2, 0x00020000);
    const u32x4 e4 = {EMPTY, EMPTY, EMPTY, EMPTY};
    if (QD == 2) { stage_q(13, 1); stage_q(14, 2); stage_q(15, 0); }
#pragma unroll
    for (int i = 0; i < 2; i++) __builtin_amdgcn_raw_buffer_store_b128(e4, rws, W_P + (cb * 16 + rb) * 8192 + lw * 2048 + i * 1024 + lane * 16, 0, SC1);
    __builtin_amdgcn_raw_buffer_store_b128(e4, rws, (cb * 16 + rb) * 512 + lw * 128 + (lane < 8 ? W_PZ + c8 * 16 : lane < 16 ? W_R + c8 * 16 : OOB), 0, SC1);
    u32x4 wr_[32];
    const int wrot = (idx & 3) * 8;
    const unsigned vo = r8 * 2048 + c8 * 16, so_ = lw * 256 * 2048 + ocb * 128;
    auto wo_load = [&](int i0, int i1) {
#pragma unroll
      for (int i = i0; i < i1; i++) wr_[i] = __builtin_amdgcn_raw_buffer_load_b128(ro, vo, so_ + ((i + wrot) & 31) * 16384, 0);
      SB;
    };
    const unsigned T = epoch_tag(ws);
    if (tid < 4) *LP(unsigned, sb + L_CNT + lane * 4) = 0;
    unsigned tya;
    STAMP(12);
    wo_load(0, WNA);
    unsigned touch = __builtin_amdgcn_raw_buffer_load_b32(lw & 1 ? rout : rws, lane == 0 ? 0 : OOB, (orb * 262144 + ocb * 128) * (lw & 1 ? 1 : OS), SC1);
    unsigned ha = lane < 16 && lane != rb ? W_H1 + (cb * 16 + lane) * 256 : OOB;
    u32x2 h1;
    {
      const int srow = 4 * g4 + q4;
      const int sw = ((srow >> 1) & 1) | ((srow >> 2) & 2) | (srow & 4);
      unsigned ia[2], va = sb + L_V + (lw >> 1) * 4096 + lane * 16;
#pragma unroll
      for (int ft = 0; ft < 2; ft++) ia[ft] = sb + (1 + (lw >> 1)) * SLOT + 24576 + (lw & 1) * 8192 + srow * 64 + (((4 * ft + p4) ^ sw) << 3);
      unsigned pa = W_P + (cb * 16 + rb) * 8192 + lw * 2048 + g4 * 512 + l15 * 16;
      unsigned za = l15 < 2 ? W_PZ + (cb * 16 + rb) * 512 + lw * 128 + (l15 * 4 + g4) * 16 : OOB;
      u32x4 one4 = {0x3f803f80u, 0x3f803f80u, 0x3f803f80u, 0x3f803f80u};
      f32x4 kv[2][2] = {}, z[2] = {};
      asm volatile("" : "+v"(ia[0]), "+v"(ia[1]), "+v"(va), "+v"(pa), "+v"(za), "+v"(one4), "+v"(ha));
      __builtin_amdgcn_s_waitcnt(0x0f70 | (WNA + 1));
      STAMP(1);
      bar();
      tya = now32();
      __builtin_amdgcn_raw_buffer_store_b64(u32x2{tya, T}, rws, tid == 0 ? W_H1 + (cb * 16 + rb) * 256 : OOB, 0, SC1);
      bf16x8 vf[2][2][2];
#pragma unroll
      for (int i = 0; i < 8; i++) vf[i >> 2][(i >> 1) & 1][i & 1] = lds_b128(va + (i >> 2) * 8192 + (i & 3) * 1024);
#pragma unroll
      for (int rt = 0; rt < 2; rt++) {
        if (rt) {
          asm volatile("" : "+v"(kv[0][0]), "+v"(kv[0][1]), "+v"(kv[1][0]), "+v"(kv[1][1]), "+v"(z[0]), "+v"(z[1]));
          wo_load(WNA, WN1);
          __builtin_amdgcn_s_setprio(3);
          STAMP(13);
          bar();
          h1 = __builtin_amdgcn_raw_buffer_load_b64(rws, ha, 0, SC1);
        }
        bf16x8 pf[2][2];
#pragma unroll
        for (int i = 0; i < 4; i++) { const unsigned a = ia[i & 1] + (i >> 1) * 4096 + rt * 2048; pf[i >> 1][i & 1] = lds_tr2(a, a + 1024); }
        if (rt) count_in(sb + L_CNT, 0x100);
        SB;
#pragma unroll
        for (int h = 0; h < 2; h++)
#pragma unroll
          for (int et = 0; et < 2; et++)
#pragma unroll
            for (int ft = 0; ft < 2; ft++) kv[ft][et] = MFMA16(pf[h][ft], vf[h][rt][et], kv[ft][et]);
        SB;
#pragma unroll
        for (int h = 0; h < 2; h++)
#pragma unroll
          for (int ft = 0; ft < 2; ft++) z[ft] = MFMA16(pf[h][ft], BF8(one4), z[ft]);
        SB;
      }
#pragma unroll
      for (int et = 0; et < 2; et++)
        __builtin_amdgcn_raw_buffer_store_b128(seal4(u32x4{pk2(kv[0][et][0], kv[0][et][1]), pk2(kv[0][et][2], kv[0][et][3]),
                                                           pk2(kv[1][et][0], kv[1][et][1]), pk2(kv[1][et][2], kv[1][et][3])}), rws, pa + et * 256, 0, SC1);
      u32x4 zs;
#pragma unroll
      for (int j = 0; j < 4; j++) zs[j] = seal(__float_as_uint((lane & 1) ? z[1][j] : z[0][j]));
      __builtin_amdgcn_raw_buffer_store_b128(zs, rws, za, 0, SC1);
      SB;
    }
    STAMP(6);
    const unsigned da = sb + L_WO + lw * 32768 + r8 * 128 + ((c8 ^ (r8 & 6)) << 4);
    auto wo_image = [&](int i0, int i1) {
#pragma unroll
      for (int i = i0; i < i1; i++) *LP(u32x4, da + ((i + wrot) & 31) * 1024) = wr_[i];
      SB;
    };
    const unsigned t1 = tya + max(latest(h1, rws, ha, tya, T), 0) + D1;
    {
      u32x4 g[2], gz[2];
      const unsigned pa = W_P + (cb * 16 + r8) * 8192 + rb * 512 + lw * 128 + c8 * 16;
      const unsigned za = W_PZ + (cb * 16 + r8) * 512 + lw * 128 + c8 * 16;
      const unsigned ra = (lane & 8) ? OOB : W_R + cb * 8192 + rb * 512 + lw * 128 + c8 * 16 + g4 * 4;
      STAMP(8);
      wait_until(t1);
      STAMP(16);
#pragma unroll
      for (int i = 0; i < 2; i++) g[i] = __builtin_amdgcn_raw_buffer_load_b128(rws, pa + i * 65536, 0, SC1);
#pragma unroll
      for (int i = 0; i < 2; i++) gz[i] = __builtin_amdgcn_raw_buffer_load_b128(rws, za + i * 4096, 0, SC1);
      SB;
      wo_load(WN1, WN2);
      lds_wait(sb + L_CNT, 0x800, 0xff00);
      wo_image(0, WI1);
      RETRY(31) += settle<2>(g, rws, pa, 65536) ? 1 : 0;
      STAMP(7);
      float s[8];
#pragma unroll
      for (int j = 0; j < 4; j++) {
        s[2 * j] = __uint_as_float(g[0][j] << 16) + __uint_as_float(g[1][j] << 16);
        s[2 * j + 1] = __uint_as_float(g[0][j] & 0xffff0000u) + __uint_as_float(g[1][j] & 0xffff0000u);
      }
      const float a0 = fold16(fold32(s[0], s[4]), fold32(s[2], s[6])), a1 = fold16(fold32(s[1], s[5]), fold32(s[3], s[7]));
      __builtin_amdgcn_raw_buffer_store_b32(seal(pk2(fold8(a0), fold8(a1))), rws, ra, 0, SC1);
      unsigned t2 = now32() + D2;
      if ((int)(t1 + D2B - t2) > 0) t2 = t1 + D2B;
      SB;
      STAMP(2);
      settle<2>(gz, rws, za, 4096);
#pragma unroll
      for (int j = 0; j < 4; j++) s[j] = __uint_as_float(gz[0][j]) + __uint_as_float(gz[1][j]);
      float z0 = fold32(s[0], s[2]), z1 = fold32(s[1], s[3]);
      z0 = fold8(fold16(z0, z0)); z1 = fold8(fold16(z1, z1));
      if ((lane & 24) == 0) *LP(unsigned, sb + L_Z + lw * 64 + (c8 & 1) * 32 + ((c8 >> 1) & 1) * 16 + (c8 >> 2) * 8 + hh * 4) = pk2(z0, z1);
      count_in(sb + L_CNT, 0x10000);
      STAMP(3);
      const unsigned ka = W_R + cb * 8192 + lw * 2048 + lane * 16;
      wait_until(t2);
#pragma unroll
      for (int i = 0; i < 2; i++) g[i] = __builtin_amdgcn_raw_buffer_load_b128(rws, ka + i * 1024, 0, SC1);
      SB;
      wo_load(WN2, 32);
      wo_image(WI1, WI2);
      RETRY(31) += settle<2>(g, rws, ka, 1024) ? 10 : 0;
#pragma unroll
      for (int i = 0; i < 2; i++) *LP(u32x4, sb + L_KV + (lw * 2 + i) * 1024 + lane * 16) = g[i];
    }
    STAMP(4);
    __builtin_amdgcn_s_setprio(0);
    bar();
    const unsigned tx = now32();
    __builtin_amdgcn_raw_buffer_store_b64(u32x2{tx, T}, rws, tid == 0 ? W_H2 + (rb * 16 + cb) * 256 : OOB, 0, SC1);
    ha = tid < 16 ? W_H2 + (orb * 16 + lane) * 256 : OOB;
    __builtin_amdgcn_s_sleep(SLW);
    SB;
    wo_image(WI2, WI3);
    h1 = __builtin_amdgcn_raw_buffer_load_b64(rws, ha, 0, SC1);
    SB;
    wo_image(WI3, 32);
    asm volatile("" :: "v"(touch));
    STAMP(5);
    asm volatile("" : "+v"(h1));
    if (w == 0) {
      const unsigned t3 = tx + latest(h1, rws, ha, tx, T) + D3;
      if (b == 0) __builtin_amdgcn_raw_buffer_store_b32(T, rws, lane == 0 ? W_E : OOB, 0, SC1);
      STAMP(14);
      wait_until(t3);
      STAMP(15);
      unsigned fa = lane < 16 ? W_F + (orb * 16 + lane) * 256 : OOB;
#pragma unroll 1
      for (int sp = 0; sp < SPIN_CAP; sp++) {
        const u32x4 f = __builtin_amdgcn_raw_buffer_load_b128(rws, fa, 0, SC1);
        asm volatile("" ::: "memory");
        if (f[0] == T && f[1] == T && f[2] == T && f[3] == T) fa = OOB;
        if (__all(fa == OOB)) break;
        RETRY(31) = 100 + RETRY(31) % 100;
        __builtin_amdgcn_s_sleep(PSL);
      }
    }
  } else {
    const int wr = lw >> 1, wc = lw & 1;
    __builtin_amdgcn_s_waitcnt(0x0f70);
    f32x4 acc[6][4] = {};
    unsigned xa[2], wa[2];
#pragma unroll
    for (int ks = 0; ks < 2; ks++) xa[ks] = sb + (wr * 64 + l15) * 128 + (((4 * ks + g4) ^ (l15 >> 1)) << 4);
#pragma unroll
    for (int nt = 0; nt < 2; nt++) wa[nt] = sb + 16384 + (8 * g4 + q4) * 128 + (((wc * 2 + nt) ^ ((q4 >> 1) | ((g4 & 1) << 1))) << 5) + p4 * 8;
    bf16x8 fx[2][4], fw[2][6];
    unsigned xs[2], wsl[2];
    auto rdw = [&](int buf, int j, int ks) {
      const unsigned a = wsl[j & 1] + (j < 4 ? 8192 + (j >> 1) * 8192 : 0) + ks * 4096;
      fw[buf][j] = lds_tr2(a, a + 512);
    };
    auto rd = [&](int buf, int ks, unsigned so, int j0, int j1) {
      if (ks == 0) {
        xs[0] = xa[0] + so; xs[1] = xa[1] + so; wsl[0] = wa[0] + so; wsl[1] = wa[1] + so;
        asm volatile("" : "+v"(xs[0]), "+v"(xs[1]), "+v"(wsl[0]), "+v"(wsl[1]));
      }
      rdw(buf, j0, ks);
#pragma unroll
      for (int m = 0; m < 4; m++) fx[buf][m] = lds_b128(xs[ks] + m * 2048);
#pragma unroll
      for (int j = j0 + 1; j < j1; j++) rdw(buf, j, ks);
    };
    auto mm = [&](int buf, int j0, int j1) {
#pragma unroll
      for (int j = j0; j < j1; j++)
#pragma unroll
        for (int m0 = 0; m0 < 4; m0++) {
          const int m = (j & 1) ? 3 - m0 : m0;
          acc[j][m] = (j == 2 || j == 3) ? MFMA16(fx[buf][m], fw[buf][j], acc[j][m]) : MFMA16(fw[buf][j], fx[buf][m], acc[j][m]);
        }
    };
#define SCHED(nds, nmf) do { _Pragma("unroll") for (int i_ = 0; i_ < (nds); i_++) { __builtin_amdgcn_sched_group_barrier(0x8, 1, 0); __builtin_amdgcn_sched_group_barrier(0x100, 1, 0); } \
      if ((nmf) > (nds)) __builtin_amdgcn_sched_group_barrier(0x8, (nmf) - (nds), 0); SB; } while (0)
#pragma unroll
    for (int j = 0; j < 6; j++) asm volatile("" : "+v"(acc[j][0]), "+v"(acc[j][1]), "+v"(acc[j][2]), "+v"(acc[j][3]));
    unsigned pia[4];
    {
      const int sw = ((l31 >> 1) & 1) | ((l31 >> 2) & 2) | (l31 & 4);
#pragma unroll
      for (int q = 0; q < 4; q++) pia[q] = sb + (1 + wc) * SLOT + 24576 + (wr * 64 + l31) * 64 + (((2 * q + hh) ^ sw) << 3);
      asm volatile("" : "+v"(pia[0]), "+v"(pia[1]), "+v"(pia[2]), "+v"(pia[3]));
    }
    const unsigned T = epoch_tag(ws);
    bar();
    STAMP(1);
    rd(0, 0, 0, 0, 6);
    SB;
#pragma unroll 1
    for (int t = 0; t < 12; t += UNR) {
#pragma unroll
      for (int u = 0; u < UNR; u++) {
        rd(1, 1, 0, 0, 6); mm(0, 0, 6); SCHED(16, 24);
        bar();
        rd(0, 0, ((u + 1) % 3) * SLOT, 0, 6); mm(1, 0, 6); SCHED(16, 24);
      }
    }
    rd(1, 1, 0, 0, 6); mm(0, 0, 6); SCHED(16, 24);
    bar();
    rd(0, 0, SLOT, 0, 4); mm(1, 0, 6); SCHED(12, 24);
    rd(1, 1, SLOT, 0, 4); mm(0, 0, 4); SCHED(12, 16);
    bar();
    rd(0, 0, 2 * SLOT, 0, 4); mm(1, 0, 4); SCHED(12, 16);
    rd(1, 1, 2 * SLOT, 0, 4); mm(0, 0, 4); SCHED(12, 16);
    bar();
    rd(0, 0, 0, 0, 4); mm(1, 0, 4); SCHED(12, 16);
    rd(1, 1, 0, 0, 4); mm(0, 0, 4); SCHED(12, 16);
    bf16x8 wp[2];
    {
      const unsigned pa = sb + L_PHI + wc * 2048 + (8 * hh + q4) * 64 + (g4 & 1) * 32 + p4 * 8;
      wp[0] = lds_tr2(pa, pa + 256); wp[1] = lds_tr2(pa + 1024, pa + 1280);
    }
    mm(1, 0, 4);
    STAMP(2);
    const f32x16 zero = {};
    f32x16 pk[2];
#pragma unroll
    for (int rt = 0; rt < 2; rt++) {
      pk[rt] = MFMA32(wp[0], pack16to32(acc[0][2 * rt], acc[0][2 * rt + 1]), zero);
      pk[rt] = MFMA32(wp[1], pack16to32(acc[1][2 * rt], acc[1][2 * rt + 1]), pk[rt]);
    }
#pragma unroll
    for (int rt = 0; rt < 2; rt++)
#pragma unroll
      for (int et = 0; et < 2; et++) {
        const f32x4 a = acc[2 + et][2 * rt], c = acc[2 + et][2 * rt + 1];
        *LP(u32x4, sb + L_V + lw * 4096 + (rt * 2 + et) * 1024 + lane * 16) = u32x4{pk2(a[0], a[1]), pk2(a[2], a[3]), pk2(c[0], c[1]), pk2(c[2], c[3])};
      }
#pragma unroll
    for (int rt = 0; rt < 2; rt++) {
      unsigned ph[2][8];
      dual_softmax(pk[rt], ph[0], ph[1]);
#pragma unroll
      for (int q = 0; q < 4; q++) {
        *LP(u32x2, pia[q] + rt * 2048) = u32x2{ph[0][2 * q], ph[0][2 * q + 1]};
        *LP(u32x2, pia[q] + rt * 2048 + 8192) = u32x2{ph[1][2 * q], ph[1][2 * q + 1]};
      }
      if (!rt) {
        bar();
        asm volatile("" : "+v"(pk[1]));
      } else {
        STAMP(3);
        bar();
      }
    }
    STAMP(4);
    SB;
    rd(0, 0, SLOT, 4, 6);
    SB;
    rd(1, 1, SLOT, 4, 6); mm(0, 4, 6); SCHED(8, 8);
    rd(0, 0, 2 * SLOT, 4, 6); mm(1, 4, 6); SCHED(8, 8);
    rd(1, 1, 2 * SLOT, 4, 6); mm(0, 4, 6); SCHED(8, 8);
    rd(0, 0, 0, 4, 6); mm(1, 4, 6); SCHED(8, 8);
    rd(1, 1, 0, 4, 6); mm(0, 4, 6); SCHED(8, 8);
    count_in(sb + L_CNT, 0x100);
    mm(1, 4, 6);
    u32x4 phq[2][4];
#pragma unroll
    for (int rt = 0; rt < 2; rt++) {
      f32x16 p = MFMA32(wp[0], pack16to32(acc[4][2 * rt], acc[4][2 * rt + 1]), zero);
      p = MFMA32(wp[1], pack16to32(acc[5][2 * rt], acc[5][2 * rt + 1]), p);
      unsigned ph[2][8];
      dual_softmax(p, ph[0], ph[1]);
#pragma unroll
      for (int i = 0; i < 4; i++) { const int sg = i >> 1, st = i & 1; phq[rt][i] = u32x4{ph[sg][2 * st], ph[sg][2 * st + 1], ph[sg][4 + 2 * st], ph[sg][5 + 2 * st]}; }
      asm volatile("" : "+v"(phq[rt][0]), "+v"(phq[rt][1]), "+v"(phq[rt][2]), "+v"(phq[rt][3]));
    }
    unsigned ka = sb + L_KV + wc * 4096 + lane * 16, zl = sb + L_Z + wc * 128 + hh * 32;
    unsigned oa = W_O + (((rb * 8 + wr * 4 + (g4 & 1)) * 32 + cb * 2 + wc) * 1024 + hh * 256) * OS + l15 * 16, oa1 = oa + 65536 * OS;
    unsigned fa = lane == 0 ? W_F + (rb * 16 + cb) * 256 + lw * 4 : OOB, Tv = T;
    asm volatile("" : "+v"(ka), "+v"(zl), "+v"(oa), "+v"(oa1), "+v"(fa), "+v"(Tv));
    STAMP(5);
    lds_wait(sb + L_CNT, 0x40000);
    float rden[2];
    {
      u32x4 zf[4];
#pragma unroll
      for (int i = 0; i < 4; i++) zf[i] = *LP(const u32x4, zl + (i >> 1) * 64 + (i & 1) * 16);
      float dn[2][2] = {};
#pragma unroll
      for (int i = 0; i < 4; i++)
#pragma unroll
        for (int j = 0; j < 4; j++)
#pragma unroll
          for (int rt = 0; rt < 2; rt++) dn[rt][j & 1] = dot2(phq[rt][i][j], zf[i][j], dn[rt][j & 1]);
#pragma unroll
      for (int rt = 0; rt < 2; rt++) { const float d = dn[rt][0] + dn[rt][1]; rden[rt] = __builtin_amdgcn_rcpf(fold32(d, d) + 1e-6f); }
      asm volatile("" : "+v"(rden[0]), "+v"(rden[1]));
    }
    STAMP(16);
    __builtin_amdgcn_s_setprio(2);
    bar();
    STAMP(6);
    {
      bf16x8 kf[4];
#pragma unroll
      for (int i = 0; i < 4; i++) kf[i] = lds_b128(ka + i * 1024);
      auto put = [&](const f32x16& n, float r, int st, unsigned a) {
        const f32x2 r2 = {r, r};
        u32x4 v;
#pragma unroll
        for (int j = 0; j < 4; j++) v[j] = pk2v(f32x2{n[8 * st + 2 * j], n[8 * st + 2 * j + 1]} * r2);
        __builtin_amdgcn_raw_buffer_store_b128(v, rws, a + st * 512 * OS, 0, SC1);
      };
      SB;
      f32x16 n0 = zero, n1 = zero;
#pragma unroll
      for (int i = 0; i < 4; i++) { n0 = MFMA32(kf[i], BF8(phq[0][i]), n0); SB; }
#pragma unroll
      for (int i = 0; i < 3; i++) { n1 = MFMA32(kf[i], BF8(phq[1][i]), n1); SB; }
      put(n0, rden[0], 0, oa); SB;
      n1 = MFMA32(kf[3], BF8(phq[1][3]), n1); SB;
      put(n0, rden[0], 1, oa); SB;
      put(n1, rden[1], 0, oa1); SB; put(n1, rden[1], 1, oa1); SB;
    }
    STAMP(7);
    waitv<0>();
    __builtin_amdgcn_raw_buffer_store_b32(Tv, rws, fa, 0, SC1);
    __builtin_amdgcn_s_setprio(0);
    STAMP(8);
  }
  {
    const int rg = lw, kh = w >> 2;
    const int kr = 16 * (g4 >> 1) + 4 * (g4 & 1) + q4;
    unsigned wa[4];
#pragma unroll
    for (int nt = 0; nt < 4; nt++) wa[nt] = sb + L_WO + kh * 65536 + kr * 128 + ((nt ^ ((kr >> 1) & 3)) << 5) + p4 * 8;
    const unsigned ob = W_O + ((orb * 8 + rg * 2) * 32 + kh * 16) * 1024 * OS;
    constexpr int LAG = OLAG, PF = OPF;
    f32x4 c0[4] = {}, c1[4] = {}, px[4];
    u32x4 o0[PF + 1], o1[PF + 1];
    bf16x8 a[LAG + 2][4];
    unsigned v0 = l15 * 16 + (g4 * 256 + kh * 32768) * OS, v1 = l15 * 16 + (g4 * 256 + (kh ^ 1) * 32768) * OS;
    unsigned xw = sb + L_FX + w * 4096 + lane * 16, xr = sb + L_FX + (w ^ 4) * 4096 + lane * 16;
    unsigned oa = (orb * 128 + rg * 32 + kh * 16 + r8) * 2048 + ocb * 128 + c8 * 16;
    unsigned tw = sb + L_WO + w * 2048 + l15 * 128 + (((g4 >> 1) ^ (l15 & 7)) << 4) + (((g4 & 1) ^ (l15 >> 3)) << 3);
    unsigned tr = sb + L_WO + w * 2048 + r8 * 128 + ((c8 ^ r8) << 4);
    asm volatile("" : "+v"(wa[0]), "+v"(wa[1]), "+v"(wa[2]), "+v"(wa[3]), "+v"(v0), "+v"(v1), "+v"(xw), "+v"(xr), "+v"(oa), "+v"(tw), "+v"(tr));
    asm volatile("" : "+v"(c0[0]), "+v"(c0[1]), "+v"(c0[2]), "+v"(c0[3]), "+v"(c1[0]), "+v"(c1[1]), "+v"(c1[2]), "+v"(c1[3]));
    auto ldo = [&](int i) {
      if (i < 16) o1[i % (PF + 1)] = __builtin_amdgcn_raw_buffer_load_b128(rws, v1, ob + i * 1024 * OS, SC1);
      if (i >= LAG && i < 16 + LAG) o0[i % (PF + 1)] = __builtin_amdgcn_raw_buffer_load_b128(rws, v0, ob + (i - LAG) * 1024 * OS, SC1);
    };
    auto ldw = [&](int i) {
#pragma unroll
      for (int nt = 0; nt < 4; nt++) a[i % (LAG + 2)][nt] = lds_tr2(wa[nt] + i * 4096, wa[nt] + i * 4096 + 1024);
    };
    bar();
    STAMP(9);
#pragma unroll
    for (int i = 0; i <= PF; i++) { ldo(i); SB; }
    SB;
    ldw(0);
    SB;
#pragma unroll
    for (int i = 0; i < 16 + LAG; i++) {
      if (i) ldo(i + PF);
      SB;
      if (i < 15) ldw(i + 1);
      if (i < 16) {
#pragma unroll
        for (int nt = 0; nt < 4; nt++) c1[nt] = MFMA16(a[i % (LAG + 2)][nt], BF8(o1[i % (PF + 1)]), c1[nt]);
      }
      if (i >= LAG) {
#pragma unroll
        for (int nt = 0; nt < 4; nt++) c0[nt] = MFMA16(a[(i - LAG) % (LAG + 2)][nt], BF8(o0[i % (PF + 1)]), c0[nt]);
      }
#pragma unroll
      for (int q = 0; q < 8; q++) { __builtin_amdgcn_sched_group_barrier(0x8, 1, 0); __builtin_amdgcn_sched_group_barrier(0x100, 1, 0); }
      SB;
      if (i == 15) {
        STAMP(10);
#pragma unroll
        for (int nt = 0; nt < 4; nt++) *LP(f32x4, xw + nt * 1024) = c1[nt];
      }
      if (i == (LAG ? 14 + LAG : 15)) {
        bar();
#pragma unroll
        for (int nt = 0; nt < 4; nt++) px[nt] = *LP(const f32x4, xr + nt * 1024);
        SB;
      }
    }
#pragma unroll
    for (int nt = 0; nt < 4; nt++) {
      const f32x4 v = px[nt] + c0[nt];
      *LP(u32x2, tw ^ (nt << 5)) = u32x2{pk2(v[0], v[1]), pk2(v[2], v[3])};
    }
    asm volatile("" ::: "memory");
    const u32x4 t0 = *LP(const u32x4, tr), t1 = *LP(const u32x4, tr + 1024);
    __builtin_amdgcn_raw_buffer_store_b128(t0, rout, oa, 0, SC1);
    __builtin_amdgcn_raw_buffer_store_b128(u32x4{t1[2], t1[3], t1[0], t1[1]}, rout, oa + 16384, 0, SC1);
    asm volatile(".Lhedgehog_end:");
    STAMP(11);
    STAMP(30);
    STAMP_DUMP();
  }
}

extern "C" void kernel_launch(void* const* d_in, const int* in_sizes, int n_in, void* d_out, int out_size, void* d_ws, size_t ws_size,
                              hipStream_t stream) {
  const size_t ws_off = WSO < 0 ? (ws_size - (size_t)(-WSO) * (1u << 20)) & ~(size_t)((2u << 20) - 1) : (size_t)WSO << 20;
  (void)hipFuncSetAttribute((const void*)hedgehog_fused, hipFuncAttributeMaxDynamicSharedMemorySize, LDS_BYTES);
  hedgehog_fused<<<256, 512, LDS_BYTES, stream>>>((const u16*)d_in[0], (const u16*)d_in[1], (const u16*)d_in[2], (const u16*)d_in[3],
                                                   (u16*)d_out, (char*)d_ws + ws_off);
}
```

```cpp
#define WNA 14
#define WN1 24
#define WN2 24
#include <hip/hip_runtime.h>
#include <hip/hip_bf16.h>

typedef unsigned short u16;
typedef unsigned long long u64;
typedef __attribute__((ext_vector_type(8))) __bf16 bf16x8;
typedef __attribute__((ext_vector_type(4))) __bf16 bf16x4;
typedef __attribute__((ext_vector_type(2))) __bf16 bf16x2;
typedef __attribute__((ext_vector_type(16))) float f32x16;
typedef __attribute__((ext_vector_type(4))) float f32x4;
typedef __attribute__((ext_vector_type(2))) float f32x2;
typedef __attribute__((ext_vector_type(4))) unsigned u32x4;
typedef __attribute__((ext_vector_type(2))) unsigned u32x2;
typedef __amdgpu_buffer_rsrc_t rsrc_t;
#define LAS __attribute__((address_space(3)))
#define LP(T, a) ((LAS T*)(size_t)(a))
#define DI __device__ __forceinline__
#define MFMA32(a, b, c) __builtin_amdgcn_mfma_f32_32x32x16_bf16((a), (b), (c), 0, 0, 0)
#define MFMA16(a, b, c) __builtin_amdgcn_mfma_f32_16x16x32_bf16((a), (b), (c), 0, 0, 0)
#define SB __builtin_amdgcn_sched_barrier(0)
#define BF8(v) __builtin_bit_cast(bf16x8, v)
constexpr int SC1 = 16;
constexpr unsigned OOB = 0x80000000u;

#define VPF 3
#define IPN 2
#define WPT 9
#define D1 125
#define D2 30
#define D3 65
#define PSL 2
#define HSL 8
#define OLAG 2
#define OPF 2
#define WSO -12
#define CL0 1
#define SLW 3
#define WN0 3
#define CLO 0
#define PULL 0
#define WSM 1
#define XFIRST 0
#define OS 2
#define WI3 22
#define HY 1
#define D2B 85
#define IP0 0
#define UNR 3
#define QD 0
#define TLBT 1
#define DXA 45
#define DXB 245
#define D4 110
#define FSTEP 3
#define GSL 6
#define CPR 0
#define HP2 3
#define WSL 0
#define WROT 0
#define WAUX 0
#define WN3 32
#define NB 0
#define NOWO 0
#define WI1 8
#define WI2 12

constexpr int SLOT = 40960;
constexpr int L_WO = 0;
constexpr int L_V = 122880;
constexpr int L_PHI = 147456;
constexpr int L_Z = 151552;
constexpr int L_CNT = 151808;
constexpr int L_YC = 151816;
constexpr int L_KV = 139264;
constexpr int L_DUMMY = 152064;
constexpr int L_FX = 131072;
constexpr int LDS_BYTES = 163840;
constexpr unsigned W_O = 0;
constexpr unsigned W_P = 8u << 20;
constexpr unsigned W_PZ = 10u << 20;
constexpr unsigned W_R = W_PZ + (128u << 10);
constexpr unsigned W_H1 = W_R + (128u << 10);
constexpr unsigned W_H2 = W_H1 + (64u << 10);
constexpr unsigned W_F = W_H2 + (64u << 10);
constexpr unsigned W_E = W_F + (64u << 10);
constexpr unsigned W_TS = 9u << 20;
constexpr unsigned W_BYTES = 12u << 20;
constexpr int SPIN_CAP = 1 << 18;

DI void bar() { asm volatile("s_waitcnt lgkmcnt(0)\n\ts_barrier" ::: "memory"); }
DI void bar0() { asm volatile("s_barrier" ::: "memory"); }
template <int N> DI void waitv() { asm volatile("s_waitcnt vmcnt(%0)" ::"i"(N) : "memory"); }
DI unsigned pk2v(f32x2 v) { return __builtin_bit_cast(unsigned, __builtin_convertvector(v, bf16x2)); }
DI unsigned pk2(float a, float b) { return pk2v(f32x2{a, b}); }
DI bf16x8 pack16to32(const f32x4& c0, const f32x4& c1) {
  auto a = __builtin_amdgcn_permlane16_swap(pk2(c0[0], c0[1]), pk2(c1[0], c1[1]), false, false);
  auto b = __builtin_amdgcn_permlane16_swap(pk2(c0[2], c0[3]), pk2(c1[2], c1[3]), false, false);
  return BF8((u32x4{a[0], b[0], a[1], b[1]}));
}
DI float fold32(float a, float b) { auto r = __builtin_amdgcn_permlane32_swap(__float_as_uint(a), __float_as_uint(b), false, false); return __uint_as_float(r[0]) + __uint_as_float(r[1]); }
DI float fold16(float a, float b) { auto r = __builtin_amdgcn_permlane16_swap(__float_as_uint(a), __float_as_uint(b), false, false); return __uint_as_float(r[0]) + __uint_as_float(r[1]); }
DI float fold8(float a) { return a + __uint_as_float(__builtin_amdgcn_update_dpp(0u, __float_as_uint(a), 0x128, 0xf, 0xf, false)); }
DI float dot2(unsigned a, unsigned b, float c) { return __builtin_amdgcn_fdot2_f32_bf16(__builtin_bit_cast(bf16x2, a), __builtin_bit_cast(bf16x2, b), c, false); }
DI bf16x8 lds_b128(unsigned a) { return *LP(const bf16x8, a); }
DI bf16x8 lds_tr2(unsigned a0, unsigned a1) {
  bf16x4 lo = __builtin_amdgcn_ds_read_tr16_b64_v4bf16(LP(bf16x4, a0));
  bf16x4 hi = __builtin_amdgcn_ds_read_tr16_b64_v4bf16(LP(bf16x4, a1));
  return __builtin_shufflevector(lo, hi, 0, 1, 2, 3, 4, 5, 6, 7);
}
template <class M, class P> DI void dual_softmax(const f32x16& p, unsigned (&pos)[8], unsigned (&neg)[8], M mid, P put) {
#define FMX __builtin_elementwise_maximum
#define FMN __builtin_elementwise_minimum
  float mx = FMX(FMX(p[0], p[1]), p[2]), mn = FMN(FMN(p[0], p[1]), p[2]), mx2 = FMX(FMX(p[3], p[4]), p[5]), mn2 = FMN(FMN(p[3], p[4]), p[5]);
#pragma unroll
  for (int i = 6; i < 14; i += 4) {
    mx = FMX(FMX(mx, p[i]), p[i + 1]); mn = FMN(FMN(mn, p[i]), p[i + 1]);
    mx2 = FMX(FMX(mx2, p[i + 2]), p[i + 3]); mn2 = FMN(FMN(mn2, p[i + 2]), p[i + 3]);
  }
  mx = FMX(FMX(mx, p[14]), p[15]); mn = FMN(FMN(mn, p[14]), p[15]);
  mx = FMX(mx, mx2); mn = FMN(mn, mn2);
  {
    auto r = __builtin_amdgcn_permlane32_swap(__float_as_uint(mx), __float_as_uint(mx), false, false);
    auto s = __builtin_amdgcn_permlane32_swap(__float_as_uint(mn), __float_as_uint(mn), false, false);
    mx = FMX(__uint_as_float(r[0]), __uint_as_float(r[1])); mn = FMN(__uint_as_float(s[0]), __uint_as_float(s[1]));
  }
  mid(mx, mn);
  const float c = 1.44269504088896341f;
  const f32x2 cp = {c, c}, cn = {-c, -c}, bx = {-mx * c, -mx * c}, bn = {mn * c, mn * c};
  f32x2 ep[8], en[8];
#pragma unroll
  for (int i = 0; i < 8; i++) {
    const f32x2 v = {p[2 * i], p[2 * i + 1]};
    const f32x2 a = __builtin_elementwise_fma(v, cp, bx), b = __builtin_elementwise_fma(v, cn, bn);
    ep[i] = f32x2{__builtin_amdgcn_exp2f(a[0]), __builtin_amdgcn_exp2f(a[1])};
    en[i] = f32x2{__builtin_amdgcn_exp2f(b[0]), __builtin_amdgcn_exp2f(b[1])};
  }
  const f32x2 s2p = ((ep[0] + ep[1]) + (ep[2] + ep[3])) + ((ep[4] + ep[5]) + (ep[6] + ep[7]));
  const f32x2 s2n = ((en[0] + en[1]) + (en[2] + en[3])) + ((en[4] + en[5]) + (en[6] + en[7]));
  const float sp = s2p[0] + s2p[1], sn = s2n[0] + s2n[1];
  const float rp = __builtin_amdgcn_rcpf(fold32(sp, sp)), rn = __builtin_amdgcn_rcpf(fold32(sn, sn));
  const f32x2 r2p = {rp, rp}, r2n = {rn, rn};
#pragma unroll
  for (int i = 0; i < 8; i++) { pos[i] = pk2v(ep[i] * r2p); neg[i] = pk2v(en[i] * r2n); if (i & 1) put(i >> 1); }
}

constexpr unsigned EMPTY = 0xffffffffu;
DI unsigned seal(unsigned v) { return min(v, EMPTY - 1u); }
DI u32x4 seal4(u32x4 v) { return u32x4{seal(v[0]), seal(v[1]), seal(v[2]), seal(v[3])}; }
DI unsigned umax4(unsigned m, u32x4 v) { return max(max(max(max(m, v[0]), v[1]), v[2]), v[3]); }
template <int N> DI int settle(u32x4 (&g)[N], rsrc_t r, unsigned a, unsigned stride) {
  auto pending = [&]() { unsigned m = 0;
#pragma unroll
    for (int i = 0; i < N; i++) m = umax4(m, g[i]);
    return __any(m == EMPTY); };
  if (__builtin_expect(!pending(), 1)) return 0;
  int sp = 1;
#pragma unroll 1
  for (; sp < SPIN_CAP; sp++) {
    __builtin_amdgcn_s_sleep(PSL);
    asm volatile("" ::: "memory");
#pragma unroll
    for (int i = 0; i < N; i++) g[i] = __builtin_amdgcn_raw_buffer_load_b128(r, a + i * stride, 0, SC1);
    if (!pending()) break;
  }
  return sp;
}
DI unsigned now32() { u64 t; asm volatile("s_memrealtime %0\n\ts_waitcnt lgkmcnt(0)" : "=s"(t) :: "memory"); return (unsigned)t; }
DI void wait_until(unsigned t) {
  unsigned n = __builtin_amdgcn_readfirstlane(1000);
#pragma unroll 1
  while (t - now32() - 1u < 1000u && --n) { if (WSL) __builtin_amdgcn_s_sleep(WSL); }
}
DI int latest(u32x2 h, rsrc_t r, unsigned a, unsigned t0, unsigned T) {
  int d = -0x10000;
  if (a != OOB && h[1] == T) { d = (int)(h[0] - t0); a = OOB; }
  if (!__all(a == OOB)) {
#pragma unroll 1
    for (int sp = 0; sp < SPIN_CAP; sp++) {
      asm volatile("" ::: "memory");
      h = __builtin_amdgcn_raw_buffer_load_b64(r, a, 0, SC1);
      if (a != OOB && h[1] == T) { d = (int)(h[0] - t0); a = OOB; }
      if (__all(a == OOB)) break;
      __builtin_amdgcn_s_sleep(PSL);
    }
  }
  d = max(d, __builtin_amdgcn_update_dpp(d, d, 0x128, 0xf, 0xf, false));
  d = max(d, __builtin_amdgcn_update_dpp(d, d, 0x124, 0xf, 0xf, false));
  d = max(d, __builtin_amdgcn_update_dpp(d, d, 0x122, 0xf, 0xf, false));
  d = max(d, __builtin_amdgcn_update_dpp(d, d, 0x121, 0xf, 0xf, false));
  asm volatile("" ::: "memory");
  return __builtin_amdgcn_readfirstlane(d);
}
DI void count_in(unsigned lds_addr, unsigned n = 1) {
  asm volatile("s_mov_b64 exec, 1\n\tds_add_u32 %0, %1\n\ts_mov_b64 exec, -1" :: "v"(lds_addr), "v"(n) : "memory");
}
template <int SL = 1> DI void lds_wait(unsigned lds_addr, unsigned n, unsigned mask = ~0u) {
#pragma unroll 1
  for (int sp = 0; sp < SPIN_CAP * 8; sp++) { if ((__builtin_amdgcn_readfirstlane(*LP(const volatile unsigned, lds_addr)) & mask) >= n) break; __builtin_amdgcn_s_sleep(SL); }
  asm volatile("" ::: "memory");
}
DI unsigned epoch_tag(const char* ws) {
  unsigned e;
  asm volatile("s_load_dword %0, %1, 0x0\n\ts_waitcnt lgkmcnt(0)" : "=s"(e) : "s"(ws + W_E) : "memory");
  return e + 1u;
}

#define STAMP(i)
#define STAMPV(i)
#define RETRY(i) retry_
#define STAMP_DUMP()

__global__ __launch_bounds__(512, 2) void hedgehog_fused(const u16* __restrict__ x, const u16* __restrict__ wqkv, const u16* __restrict__ wphi,
                                                         const u16* __restrict__ wo, u16* __restrict__ out, char* __restrict__ ws) {
  extern __shared__ __attribute__((aligned(16))) char smem[];
  const int tid = threadIdx.x, lane = tid & 63;
  const int w = __builtin_amdgcn_readfirstlane(tid >> 6), lw = w & 3;
  const int b = blockIdx.x, xcd = b & 7, idx = b >> 3;
  const int rl = idx & 7, cl = idx >> 3;
  const int rb = (xcd & 1) * 8 + rl, cb = (xcd >> 1) * 4 + cl;
  const int orb = (xcd >> 1) * 4 + (idx & 3), ocb = (xcd & 1) * 8 + (idx >> 2);
  const unsigned sb = (unsigned)(size_t)(LAS char*)smem;
  const int hh = lane >> 5, l31 = lane & 31, l15 = lane & 15, g4 = lane >> 4, q4 = l15 >> 2, p4 = lane & 3, r8 = lane >> 3, c8 = lane & 7;
  unsigned st_r = 0, st_c = 0, retry_ = 0; (void)st_r; (void)st_c; (void)retry_;
  const unsigned pw = lw & 1;
  unsigned sp = (b * 2 + (lw >> 1)) * (8192 - pw * 7424);
  unsigned vp = __umul24(l15, 2048 - pw * 1920) + __umul24(g4, 128 + pw * (64 * 6144 - 128));
  vp = g4 < IPN && g4 >= IP0 && l15 < 4 + 2 * pw ? vp : OOB;
  unsigned vx[4], vw[6];
#pragma unroll
  for (int i = 0; i < 4; i++) vx[i] = (rb * 128 + lw * 32 + r8) * 2048 + ((c8 ^ ((i & 1) * 4 + (r8 >> 1))) << 4) + i * (16384 - 1024);
#pragma unroll
  for (int i = 0; i < 6; i++) vw[i] = cb * 128 + (lw * 16 + r8) * 6144 + ((c8 ^ ((i & 1) * 4 + (r8 & 2))) << 4) + (i >> 1) * 2048 + (i & 1) * (8 * 6144 - 1024);
  asm volatile("" : "+v"(vp), "+v"(vx[0]), "+v"(vx[1]), "+v"(vx[2]), "+v"(vx[3]), "+v"(vw[0]), "+v"(vw[1]), "+v"(vw[2]), "+v"(vw[3]), "+v"(vw[4]), "+v"(vw[5]));
  asm volatile("" : "+s"(sp));
  asm volatile("" :: "s"(x), "s"(wqkv), "s"(wphi), "s"(wo), "s"(out), "s"(ws));
  STAMP(0);
  const rsrc_t rws = __builtin_amdgcn_make_buffer_rsrc(ws, 0, W_BYTES, 0x00020000);
  const rsrc_t rx = __builtin_amdgcn_make_buffer_rsrc((void*)x, 0, 2048 * 1024 * 2, 0x00020000);
  const rsrc_t rw = __builtin_amdgcn_make_buffer_rsrc((void*)wqkv, 0, 1024 * 3072 * 2, 0x00020000);
  const rsrc_t rout = __builtin_amdgcn_make_buffer_rsrc(out, 0, 2048 * 1024 * 2, 0x00020000);

  auto stage = [&](unsigned ox, unsigned ow, unsigned so) {
    LAS char* dx = (LAS char*)smem + so + lw * 4096;
    LAS char* dw = (LAS char*)smem + so + 16384 + lw * 2048;
#define LX(i) __builtin_amdgcn_raw_ptr_buffer_load_lds(rx, dx, 16, vx[i], ox, i * 1024, 0)
#define LW(i) __builtin_amdgcn_raw_ptr_buffer_load_lds(rw, dw + (i >> 1) * 8192, 16, vw[i], ow, (i & 1) * 1024, 0)
    LW(2); LW(3); LX(0); LX(1); LX(2); LX(3); LW(4); LW(5); LW(0); LW(1);
  };
  const rsrc_t ro = __builtin_amdgcn_make_buffer_rsrc((void*)wo, 0, NOWO ? 0 : 1024 * 1024 * 2, 0x00020000);
  u32x4 wr_[32];
  const unsigned vo = r8 * 2048 + c8 * 16, so_ = lw * 256 * 2048 + ocb * 128;
  auto wo_load = [&](int i0, int i1) {
#pragma unroll
    for (int i = i0; i < i1; i++) wr_[i] = __builtin_amdgcn_raw_buffer_load_b128(ro, vo, so_ + i * 16384, 0);
    SB;
  };
  u64 pc; unsigned code_len;
  asm volatile("s_getpc_b64 %0\n.Lhedgehog_begin:\n\ts_mov_b32 %1, .Lhedgehog_end-.Lhedgehog_begin" : "=s"(pc), "=s"(code_len));
  if (w < 4) {
    __builtin_amdgcn_raw_ptr_buffer_load_lds(lw & 1 ? rw : rx, (LAS char*)smem + L_DUMMY, 4, vp, sp, 0, 0);
    stage(0, 0, 0);
    SB;
    STAMP(25);
    const int turn = (rl ^ xcd) & 7, u = cl * 4 + lw, l3 = lane / 3;
    const unsigned px = lane < 16 ? lane * 2048 + ((xcd & 1) * 1024 + ((xcd >> 1) * 16 + u) * 16) * 2048 : OOB;
    unsigned pa_ = lane * 128 + l3 * (2048 - 384) + ((xcd & 1) * 96 + u * 6) * 2048, pb_ = lane * 2048 + (u * 12 - 18) * 2048 + 384;
    asm volatile("" : "+v"(pa_), "+v"(pb_));
    const unsigned pq = (lane < 18 ? pa_ : lane < 30 ? pb_ : OOB) + (xcd >> 1) * 512;
    const int uc = idx * 4 + lw;
    const char* sc = (const char*)(pc & ~127ull) + (uc * 256 < code_len ? uc : 0) * 256;
    unsigned o0 = cb * 16 + rb, o1 = rb * 16 + cb, o2 = W_H1 + lw * 65536;
    asm volatile("" : "+s"(o0), "+s"(o1), "+s"(o2));
    const char* s1 = (lw == 3 ? (const char*)wphi : ws) + (lw == 3 ? b * 256 : o2 + (lw == 0 ? o0 : o1) * 256);
    unsigned o3 = W_P + o0 * 8192, o4 = (lw == 1 ? rb : orb) * 262144 + cb * 2048, o5 = o4 * OS;
    asm volatile("" : "+s"(o3), "+s"(o4), "+s"(o5));
    const char* s2 = (lw == 3 ? (const char*)out : ws) + (lw == 0 ? o3 : lw == 3 ? o4 : o5);
    auto pull = [&](int s) {
      __builtin_amdgcn_raw_ptr_buffer_load_lds(rx, (LAS char*)smem + L_DUMMY, 4, px, s * 128, 0, 0);
      __builtin_amdgcn_raw_ptr_buffer_load_lds(rw, (LAS char*)smem + L_DUMMY, 4, pq, s * 64 * 6144, 0, 0);
    };
#define TOUCH(p) asm volatile("s_load_dwordx2 s[100:101], %0, 0x3c\n\ts_load_dwordx2 s[100:101], %0, 0xbc" :: "s"(p) : "s100", "s101")
    TOUCH(sc); TOUCH(s1); TOUCH(s2);
    sc = (const char*)wo + (b * 4 + lw) * 2048 - 512;
    unsigned turns = (0x101u << turn) & (0xffffu << IPN), ox = 128, ow = 64 * 6144, so = SLOT;
    asm volatile("" : "+s"(turns));
    turns >>= VPF - 1;
    unsigned tch = 0x3fc;
#pragma unroll 1
    for (int n = 1; n < 16; n++) {
      if (__builtin_expect(turns & 1, 0)) pull(n + VPF - 2);
      stage(ox, ow, so);
      ox += 128; ow += 64 * 6144; so = so == 2 * SLOT ? 0 : so + SLOT;
      asm volatile("s_bitcmp0_b32 %2, 0\n\ts_cbranch_scc1 1f\n\ts_load_dwordx2 s[100:101], %1, 0x3c\n\ts_load_dwordx2 s[100:101], %1, 0xbc\n1:\n\t"
                   "s_bitcmp1_b32 %0, 0\n\ts_waitcnt vmcnt(12)\n\ts_cbranch_scc1 2f\n\ts_waitcnt vmcnt(10)\n2:\n\ts_barrier"
                   :: "s"(turns), "s"(sc), "s"(tch), "s"(ox), "s"(ow), "s"(so) : "s100", "s101", "scc", "memory");
      turns >>= 1; tch >>= 1; sc += 256;
    }
    const rsrc_t rp = __builtin_amdgcn_make_buffer_rsrc((void*)wphi, 0, 32 * 1024 * 2, 0x00020000);
    __builtin_amdgcn_raw_ptr_buffer_load_lds(rp, (LAS char*)smem + L_PHI + lw * 1024, 16, lane * 16, cb * 4096 + lw * 1024, 0, 0);
    asm volatile("" ::: "memory"); SB;
    const u32x4 e4 = {EMPTY, EMPTY, EMPTY, EMPTY};
#pragma unroll
    for (int i = 0; i < 2; i++) __builtin_amdgcn_raw_buffer_store_b128(e4, rws, W_P + (cb * 16 + rb) * 8192 + lw * 2048 + i * 1024 + lane * 16, 0, SC1);
    __builtin_amdgcn_raw_buffer_store_b128(e4, rws, (cb * 16 + rb) * 512 + lw * 128 + (lane < 8 ? W_PZ + c8 * 16 : lane < 16 ? W_R + c8 * 16 : OOB), 0, SC1);
#pragma unroll
    for (int i = 0; i < 2; i++)
      __builtin_amdgcn_raw_buffer_store_b128(e4, rws, W_O + (((rb * 8 + (cb & 2) * 2 + lw) * 32 + cb * 2 + i) * 1024 + g4 * 256) * OS + l15 * 16, 0, SC1);
    SB;
    wo_load(0, WN0);
    waitv<5 + WN0>(); bar0();
  }
  unsigned T, t3 = 0, fa = OOB;
  const int rg = lw, kh = w >> 2;
  unsigned ga[4], v0, v1, xw, xr, og, tw, tr, fr;
  auto phase2_setup = [&]() {
    const int kr = 16 * (g4 >> 1) + 4 * (g4 & 1) + q4;
#pragma unroll
    for (int nt = 0; nt < 4; nt++) ga[nt] = sb + L_WO + kh * 65536 + kr * 128 + ((nt ^ ((kr >> 1) & 3)) << 5) + p4 * 8;
    v0 = l15 * 16 + (g4 * 256 + kh * 32768) * OS; v1 = l15 * 16 + (g4 * 256 + (kh ^ 1) * 32768) * OS;
    xw = sb + L_FX + w * 4096 + lane * 16; xr = sb + L_FX + (w ^ 4) * 4096 + lane * 16;
    og = (orb * 128 + rg * 32 + kh * 16 + r8) * 2048 + ocb * 128 + c8 * 16;
    tw = sb + L_WO + w * 2048 + l15 * 128 + (((g4 >> 1) ^ (l15 & 7)) << 4) + (((g4 & 1) ^ (l15 >> 3)) << 3);
    tr = sb + L_WO + w * 2048 + r8 * 128 + ((c8 ^ r8) << 4);
    fr = lane < 8 ? W_F + (orb * 16 + kh * 8 + lane) * 256 + (rg >> 1) * 8 : OOB;
    asm volatile("" : "+v"(ga[0]), "+v"(ga[1]), "+v"(ga[2]), "+v"(ga[3]), "+v"(v0), "+v"(v1), "+v"(xw), "+v"(xr), "+v"(og), "+v"(tw), "+v"(tr), "+v"(fr));
  };
  int role = w; asm volatile("" : "+s"(role));

  if (role < 4) {
    T = epoch_tag(ws);
    if (tid == 0) *LP(u32x2, sb + L_CNT) = u32x2{0, T};
    unsigned tya, tx;
    STAMP(12);
    wo_load(WN0, WNA);
    unsigned touch = __builtin_amdgcn_raw_buffer_load_b32(lw & 1 ? rout : rws, lane == 0 ? 0 : OOB, (orb * 262144 + ocb * 128) * (lw & 1 ? 1 : OS), SC1);
    unsigned ha = lane < 16 && lane != rb ? W_H1 + (cb * 16 + lane) * 256 : OOB;
    u32x2 h1;
    {
      const int srow = 4 * g4 + q4;
      const int sw = ((srow >> 1) & 1) | ((srow >> 2) & 2) | (srow & 4);
      unsigned ia[2], va = sb + L_V + (lw >> 1) * 4096 + lane * 16;
#pragma unroll
      for (int ft = 0; ft < 2; ft++) ia[ft] = sb + (1 + (lw >> 1)) * SLOT + 24576 + (lw & 1) * 8192 + srow * 64 + (((4 * ft + p4) ^ sw) << 3);
      unsigned pa = W_P + (cb * 16 + rb) * 8192 + lw * 2048 + g4 * 512 + l15 * 16;
      unsigned za = l15 < 2 ? W_PZ + (cb * 16 + rb) * 512 + lw * 128 + (l15 * 4 + g4) * 16 : OOB;
      u32x4 one4 = {0x3f803f80u, 0x3f803f80u, 0x3f803f80u, 0x3f803f80u};
      f32x4 kv[2][2] = {}, z[2] = {};
      asm volatile("" : "+v"(ia[0]), "+v"(ia[1]), "+v"(va), "+v"(pa), "+v"(za), "+v"(one4), "+v"(ha));
      static_assert(WNA - WN0 + 1 < 16, "");
      __builtin_amdgcn_s_waitcnt(0x0f70 | (WNA - WN0 + 1));
      STAMP(1);
      bar();
      tya = now32();
      __builtin_amdgcn_raw_buffer_store_b64(u32x2{tya, T}, rws, tid == 0 ? W_H1 + (cb * 16 + rb) * 256 : OOB, 0, SC1);
      bf16x8 vf[2][2][2];
#pragma unroll
      for (int i = 0; i < 8; i++) vf[i >> 2][(i >> 1) & 1][i & 1] = lds_b128(va + (i >> 2) * 8192 + (i & 3) * 1024);
#pragma unroll
      for (int rt = 0; rt < 2; rt++) {
        if (rt) {
          asm volatile("" : "+v"(kv[0][0]), "+v"(kv[0][1]), "+v"(kv[1][0]), "+v"(kv[1][1]), "+v"(z[0]), "+v"(z[1]));
          wo_load(WNA, WN1);
          __builtin_amdgcn_s_setprio(3);
          STAMP(13);
          bar();
          h1 = __builtin_amdgcn_raw_buffer_load_b64(rws, ha, 0, SC1);
        }
        bf16x8 pf[2][2];
#pragma unroll
        for (int i = 0; i < 4; i++) { const unsigned a = ia[i & 1] + (i >> 1) * 4096 + rt * 2048; pf[i >> 1][i & 1] = lds_tr2(a, a + 1024); }
        if (rt) count_in(sb + L_CNT, 0x100);
        SB;
#pragma unroll
        for (int h = 0; h < 2; h++)
#pragma unroll
          for (int et = 0; et < 2; et++)
#pragma unroll
            for (int ft = 0; ft < 2; ft++) kv[ft][et] = MFMA16(pf[h][ft], vf[h][rt][et], kv[ft][et]);
        SB;
#pragma unroll
        for (int h = 0; h < 2; h++)
#pragma unroll
          for (int ft = 0; ft < 2; ft++) z[ft] = MFMA16(pf[h][ft], BF8(one4), z[ft]);
        SB;
      }
#pragma unroll
      for (int et = 0; et < 2; et++)
        __builtin_amdgcn_raw_buffer_store_b128(seal4(u32x4{pk2(kv[0][et][0], kv[0][et][1]), pk2(kv[0][et][2], kv[0][et][3]),
                                                           pk2(kv[1][et][0], kv[1][et][1]), pk2(kv[1][et][2], kv[1][et][3])}), rws, pa + et * 256, 0, SC1);
      u32x4 zs;
#pragma unroll
      for (int j = 0; j < 4; j++) zs[j] = seal(__float_as_uint((lane & 1) ? z[1][j] : z[0][j]));
      __builtin_amdgcn_raw_buffer_store_b128(zs, rws, za, 0, SC1);
      SB;
    }
    STAMP(6);
    __builtin_amdgcn_s_setprio(HP2);
    const unsigned da = sb + L_WO + lw * 32768 + r8 * 128 + ((c8 ^ (r8 & 6)) << 4);
    auto wo_image = [&](int i0, int i1) {
#pragma unroll
      for (int i = i0; i < i1; i++) *LP(u32x4, da + i * 1024) = wr_[i];
      SB;
    };
    const unsigned t1 = tya + max(latest(h1, rws, ha, tya, T), 0) + D1;
    {
      u32x4 g[2], gz[2];
      const unsigned pa = W_P + (cb * 16 + r8) * 8192 + rb * 512 + lw * 128 + c8 * 16;
      const unsigned za = W_PZ + (cb * 16 + r8) * 512 + lw * 128 + c8 * 16;
      const unsigned ra = (lane & 8) ? OOB : W_R + cb * 8192 + rb * 512 + lw * 128 + c8 * 16 + g4 * 4;
      wait_until(t1);
      STAMP(16);
#pragma unroll
      for (int i = 0; i < 2; i++) g[i] = __builtin_amdgcn_raw_buffer_load_b128(rws, pa + i * 65536, 0, SC1);
#pragma unroll
      for (int i = 0; i < 2; i++) gz[i] = __builtin_amdgcn_raw_buffer_load_b128(rws, za + i * 4096, 0, SC1);
      SB;
      wo_load(WN1, WN2);
      lds_wait(sb + L_CNT, 0x800, 0xff00);
      wo_image(0, WI1);
      RETRY(31) += settle<2>(g, rws, pa, 65536) ? 1 : 0;
      STAMP(7);
      float s[8];
#pragma unroll
      for (int j = 0; j < 4; j++) {
        s[2 * j] = __uint_as_float(g[0][j] << 16) + __uint_as_float(g[1][j] << 16);
        s[2 * j + 1] = __uint_as_float(g[0][j] & 0xffff0000u) + __uint_as_float(g[1][j] & 0xffff0000u);
      }
      const float a0 = fold16(fold32(s[0], s[4]), fold32(s[2], s[6])), a1 = fold16(fold32(s[1], s[5]), fold32(s[3], s[7]));
      __builtin_amdgcn_raw_buffer_store_b32(seal(pk2(fold8(a0), fold8(a1))), rws, ra, 0, SC1);
      unsigned t2 = now32() + D2;
      if ((int)(t1 + D2B - t2) > 0) t2 = t1 + D2B;
      SB;
      STAMP(2);
      settle<2>(gz, rws, za, 4096);
#pragma unroll
      for (int j = 0; j < 4; j++) s[j] = __uint_as_float(gz[0][j]) + __uint_as_float(gz[1][j]);
      float z0 = fold32(s[0], s[2]), z1 = fold32(s[1], s[3]);
      z0 = fold8(fold16(z0, z0)); z1 = fold8(fold16(z1, z1));
      if ((lane & 24) == 0) *LP(unsigned, sb + L_Z + lw * 64 + (c8 & 1) * 32 + ((c8 >> 1) & 1) * 16 + (c8 >> 2) * 8 + hh * 4) = pk2(z0, z1);
      count_in(sb + L_CNT, 0x10000);
      STAMP(3);
      const unsigned ka = W_R + cb * 8192 + lw * 2048 + lane * 16;
      wait_until(t2);
#pragma unroll
      for (int i = 0; i < 2; i++) g[i] = __builtin_amdgcn_raw_buffer_load_b128(rws, ka + i * 1024, 0, SC1);
      SB;
      wo_load(WN2, 32);
      wo_image(WI1, WI2);
      RETRY(31) += settle<2>(g, rws, ka, 1024) ? 10 : 0;
#pragma unroll
      for (int i = 0; i < 2; i++) *LP(u32x4, sb + L_KV + (lw * 2 + i) * 1024 + lane * 16) = g[i];
    }
    unsigned hs = tid == 0 ? W_H2 + (rb * 16 + cb) * 256 : OOB;
    ha = tid < 16 ? W_H2 + (orb * 16 + lane) * 256 : OOB;
    asm volatile("" : "+v"(hs), "+v"(ha));
    STAMP(4);
    bar();
    tx = now32();
    __builtin_amdgcn_raw_buffer_store_b64(u32x2{tx, T}, rws, hs, 0, SC1);
    __builtin_amdgcn_s_setprio(0);
    phase2_setup();
    SB;
    wo_image(WI2, WI3);
    h1 = __builtin_amdgcn_raw_buffer_load_b64(rws, ha, 0, SC1);
    SB;
    wo_image(WI3, 32);
    asm volatile("" :: "v"(touch));
    STAMP(5);
    asm volatile("" : "+v"(h1));
    if (w == 0) {
      t3 = tx + latest(h1, rws, ha, tx, T) + D3;
      if (lane == 0) *LP(unsigned, sb + L_CNT) = t3;
      if (b == 0) __builtin_amdgcn_raw_buffer_store_b32(T, rws, lane == 0 ? W_E : OOB, 0, SC1);
      STAMP(14);
    }
  } else {
    const int wr = lw >> 1, wc = lw & 1;
    __builtin_amdgcn_s_waitcnt(0x0f70);
    f32x4 acc[6][4] = {};
    unsigned xa[2], wa[2];
#pragma unroll
    for (int ks = 0; ks < 2; ks++) xa[ks] = sb + (wr * 64 + l15) * 128 + (((4 * ks + g4) ^ (l15 >> 1)) << 4);
#pragma unroll
    for (int nt = 0; nt < 2; nt++) wa[nt] = sb + 16384 + (8 * g4 + q4) * 128 + (((wc * 2 + nt) ^ ((q4 >> 1) | ((g4 & 1) << 1))) << 5) + p4 * 8;
    bf16x8 fx[4][4], fw[4][6];
    unsigned xs[2], wsl[2];
    auto rdw = [&](int buf, int j, int ks) {
      const unsigned a = wsl[j & 1] + (j < 4 ? 8192 + (j >> 1) * 8192 : 0) + ks * 4096;
      fw[buf][j] = lds_tr2(a, a + 512);
    };
    auto rd = [&](int buf, int ks, unsigned so, int j0, int j1) {
      if (ks == 0) {
        xs[0] = xa[0] + so; xs[1] = xa[1] + so; wsl[0] = wa[0] + so; wsl[1] = wa[1] + so;
        asm volatile("" : "+v"(xs[0]), "+v"(xs[1]), "+v"(wsl[0]), "+v"(wsl[1]));
      }
      rdw(buf, j0, ks);
#pragma unroll
      for (int m = 0; m < 4; m++) fx[buf][m] = lds_b128(xs[ks] + m * 2048);
#pragma unroll
      for (int j = j0 + 1; j < j1; j++) rdw(buf, j, ks);
    };
    auto mm = [&](int buf, int j0, int j1) {
#pragma unroll
      for (int j = j0; j < j1; j++)
#pragma unroll
        for (int m0 = 0; m0 < 4; m0++) {
          const int m = (j & 1) ? 3 - m0 : m0;
          acc[j][m] = (j == 2 || j == 3) ? MFMA16(fx[buf][m], fw[buf][j], acc[j][m]) : MFMA16(fw[buf][j], fx[buf][m], acc[j][m]);
        }
    };
#define SCHED(nds, nmf) do { _Pragma("unroll") for (int i_ = 0; i_ < (nds); i_++) { __builtin_amdgcn_sched_group_barrier(0x8, 1, 0); __builtin_amdgcn_sched_group_barrier(0x100, 1, 0); } \
      if ((nmf) > (nds)) __builtin_amdgcn_sched_group_barrier(0x8, (nmf) - (nds), 0); SB; } while (0)
#pragma unroll
    for (int j = 0; j < 6; j++) asm volatile("" : "+v"(acc[j][0]), "+v"(acc[j][1]), "+v"(acc[j][2]), "+v"(acc[j][3]));
    unsigned wpa = sb + L_PHI + wc * 2048 + (8 * hh + q4) * 64 + (g4 & 1) * 32 + p4 * 8, vwa = sb + L_V + lw * 4096 + lane * 16;
    unsigned pia[4];
    {
      const int sw = ((l31 >> 1) & 1) | ((l31 >> 2) & 2) | (l31 & 4);
#pragma unroll
      for (int q = 0; q < 4; q++) pia[q] = sb + (1 + wc) * SLOT + 24576 + (wr * 64 + l31) * 64 + (((2 * q + hh) ^ sw) << 3);
      asm volatile("" : "+v"(pia[0]), "+v"(pia[1]), "+v"(pia[2]), "+v"(pia[3]), "+v"(wpa), "+v"(vwa));
    }
    bar();
    STAMP(1);
    rd(0, 0, 0, 0, 6);
    SB;
#pragma unroll 1
    for (int t = 0; t < 12; t += UNR) {
#pragma unroll
      for (int u = 0; u < UNR; u++) {
        rd(1, 1, 0, 0, 6); mm(0, 0, 6); SCHED(16, 24);
        bar();
        rd(0, 0, ((u + 1) % 3) * SLOT, 0, 6); mm(1, 0, 6); SCHED(16, 24);
      }
    }
    rd(1, 1, 0, 0, 6); mm(0, 0, 6); SCHED(16, 24);
    bar();
    rd(0, 0, SLOT, 0, 4); mm(1, 0, 6); SCHED(12, 24);
    rd(1, 1, SLOT, 0, 4); mm(0, 0, 4); SCHED(12, 16);
    bar();
    rd(0, 0, 2 * SLOT, 0, 4); mm(1, 0, 4); SCHED(12, 16);
    rd(1, 1, 2 * SLOT, 0, 4); mm(0, 0, 4); SCHED(12, 16);
    bar();
    __builtin_amdgcn_s_setprio(CPR);
    rd(0, 0, 0, 0, 4); mm(1, 0, 4); SCHED(12, 16);
    bf16x8 wp[2];
    rd(1, 1, 0, 0, 4);
    wp[0] = lds_tr2(wpa, wpa + 256); wp[1] = lds_tr2(wpa + 1024, wpa + 1280);
    mm(0, 0, 4); SCHED(16, 16);
    STAMP(2);
    const f32x16 zero = {};
    auto project = [&](int j0, f32x16 (&p)[2], auto fill0, auto fill1) {
#pragma unroll
      for (int i = 0; i < 8; i++) { const int j = j0 + ((i >> 1) & 1), m = (i >> 2) * 2 + (i & 1); acc[j][m] = MFMA16(fw[1][j], fx[1][m], acc[j][m]); SB; }
      fill0();
      const bf16x8 a0 = pack16to32(acc[j0][0], acc[j0][1]), a1 = pack16to32(acc[j0 + 1][0], acc[j0 + 1][1]);
#pragma unroll
      for (int i = 0; i < 8; i++) { __builtin_amdgcn_sched_group_barrier(0x8, 1, 0); __builtin_amdgcn_sched_group_barrier(0x2, 2, 0); }
      SB;
      p[0] = MFMA32(wp[0], a0, zero); SB;
      const bf16x8 b0 = pack16to32(acc[j0][2], acc[j0][3]); SB;
      p[0] = MFMA32(wp[1], a1, p[0]); SB;
      const bf16x8 b1 = pack16to32(acc[j0 + 1][2], acc[j0 + 1][3]); SB;
      p[1] = MFMA32(wp[0], b0, zero); SB;
      fill1(0); SB;
      p[1] = MFMA32(wp[1], b1, p[1]); SB;
      fill1(1); SB;
    };
    f32x16 pk[2];
    project(0, pk, [&]() { mm(1, 2, 4); }, [&](int rt) {
#pragma unroll
      for (int et = 0; et < 2; et++) {
        const f32x4 a = acc[2 + et][2 * rt], c = acc[2 + et][2 * rt + 1];
        *LP(u32x4, vwa + (rt * 2 + et) * 1024) = u32x4{pk2(a[0], a[1]), pk2(a[2], a[3]), pk2(c[0], c[1]), pk2(c[2], c[3])};
      }
    });
    STAMP(20);
#pragma unroll
    for (int rt = 0; rt < 2; rt++) {
      unsigned ph[2][8];
      dual_softmax(pk[rt], ph[0], ph[1],
        [&](float& mx, float& mn) { if (rt) { asm volatile("" : "+v"(mx), "+v"(mn)); STAMP(21); bar(); STAMP(22);
          rdw(0, 4, 0); rdw(0, 5, 0); rdw(1, 4, 1); rdw(1, 5, 1); rd(2, 0, SLOT, 4, 6); SB;
          asm volatile("" : "+v"(mx), "+v"(mn)); } },
        [&](int q) {
          *LP(u32x2, pia[q] + rt * 2048) = u32x2{ph[0][2 * q], ph[0][2 * q + 1]};
          *LP(u32x2, pia[q] + rt * 2048 + 8192) = u32x2{ph[1][2 * q], ph[1][2 * q + 1]};
          SB;
        });
    }
    STAMP(3);
    bar();
    STAMP(4);
    SB;
    rd(3, 1, SLOT, 4, 6); mm(0, 4, 6); SCHED(8, 8);
    rd(0, 0, 2 * SLOT, 4, 6); mm(1, 4, 6); SCHED(8, 8);
    rd(1, 1, 2 * SLOT, 4, 6); mm(2, 4, 6); SCHED(8, 8);
    count_in(sb + L_CNT, 0x100);
    mm(3, 4, 6); SB;
    mm(0, 4, 6); SB;
    STAMP(23);
    project(4, pk, []() {}, [](int) {});
    STAMP(24);
    u32x4 phq[2][4];
#pragma unroll
    for (int rt = 0; rt < 2; rt++) {
      unsigned ph[2][8];
      dual_softmax(pk[rt], ph[0], ph[1], [](float&, float&) {}, [](int) {});
#pragma unroll
      for (int i = 0; i < 4; i++) { const int sg = i >> 1, st = i & 1; phq[rt][i] = u32x4{ph[sg][2 * st], ph[sg][2 * st + 1], ph[sg][4 + 2 * st], ph[sg][5 + 2 * st]}; }
      asm volatile("" : "+v"(phq[rt][0]), "+v"(phq[rt][1]), "+v"(phq[rt][2]), "+v"(phq[rt][3]));
    }
    unsigned ka = sb + L_KV + wc * 4096 + lane * 16, zl = sb + L_Z + wc * 128 + hh * 32;
    unsigned oa = W_O + (((rb * 8 + wr * 4 + (g4 & 1)) * 32 + cb * 2 + wc) * 1024 + hh * 256) * OS + l15 * 16, oa1 = oa + 65536 * OS;
    fa = lane == 0 ? W_F + (rb * 16 + cb) * 256 + lw * 4 : OOB;
    asm volatile("" : "+v"(ka), "+v"(zl), "+v"(oa), "+v"(oa1), "+v"(fa));
    STAMP(5);
    lds_wait(sb + L_CNT, 0x40000);
    T = __builtin_amdgcn_readfirstlane(*LP(const unsigned, sb + L_CNT + 4));
    float rden[2];
    {
      u32x4 zf[4];
#pragma unroll
      for (int i = 0; i < 4; i++) zf[i] = *LP(const u32x4, zl + (i >> 1) * 64 + (i & 1) * 16);
      float dn[2][2] = {};
#pragma unroll
      for (int i = 0; i < 4; i++)
#pragma unroll
        for (int j = 0; j < 4; j++)
#pragma unroll
          for (int rt = 0; rt < 2; rt++) dn[rt][j & 1] = dot2(phq[rt][i][j], zf[i][j], dn[rt][j & 1]);
#pragma unroll
      for (int rt = 0; rt < 2; rt++) { const float d = dn[rt][0] + dn[rt][1]; rden[rt] = __builtin_amdgcn_rcpf(fold32(d, d) + 1e-6f); }
      asm volatile("" : "+v"(rden[0]), "+v"(rden[1]));
    }
    STAMP(16);
    __builtin_amdgcn_s_setprio(2);
    bar();
    STAMP(6);
    {
      bf16x8 kf[4];
#pragma unroll
      for (int i = 0; i < 4; i++) kf[i] = lds_b128(ka + i * 1024);
      auto put = [&](const f32x16& n, float r, int st, unsigned a) {
        const f32x2 r2 = {r, r};
        u32x4 v;
#pragma unroll
        for (int j = 0; j < 4; j++) v[j] = seal(pk2v(f32x2{n[8 * st + 2 * j], n[8 * st + 2 * j + 1]} * r2));
        __builtin_amdgcn_raw_buffer_store_b128(v, rws, a + st * 512 * OS, 0, SC1);
      };
      SB;
      f32x16 n0 = zero, n1 = zero;
#pragma unroll
      for (int i = 0; i < 4; i++) { n0 = MFMA32(kf[i], BF8(phq[0][i]), n0); SB; }
#pragma unroll
      for (int i = 0; i < 3; i++) { n1 = MFMA32(kf[i], BF8(phq[1][i]), n1); SB; }
      put(n0, rden[0], 0, oa); SB;
      n1 = MFMA32(kf[3], BF8(phq[1][3]), n1); SB;
      put(n0, rden[0], 1, oa); SB;
      put(n1, rden[1], 0, oa1); SB; put(n1, rden[1], 1, oa1); SB;
    }
    STAMP(7);
    phase2_setup();
    if (!NB) { waitv<0>(); __builtin_amdgcn_raw_buffer_store_b32(T, rws, fa, 0, SC1); }
    __builtin_amdgcn_s_setprio(0);
  }
  {
    const unsigned ob = W_O + ((orb * 8 + rg * 2) * 32 + kh * 16) * 1024 * OS;
    constexpr int LAG = OLAG, PF = OPF;
    f32x4 c0[4] = {}, c1[4] = {}, px[4];
    u32x4 o0[PF + 1], o1[PF + 1];
    bf16x8 a[LAG + 2][4];
    auto head = [&](int i) { return 2 * ((i >> 2) ^ rg) + 8 * ((i >> 1) & 1) + (i & 1); };
    auto ldo = [&](int i) {
      if (i < 16) o1[i % (PF + 1)] = __builtin_amdgcn_raw_buffer_load_b128(rws, v1, ob + head(i) * 1024 * OS, SC1);
      if (i >= LAG && i < 16 + LAG) o0[i % (PF + 1)] = __builtin_amdgcn_raw_buffer_load_b128(rws, v0, ob + head(i - LAG) * 1024 * OS, SC1);
    };
    unsigned wb[4];
    auto ldw = [&](int i) {
      if (!(i & 3)) {
#pragma unroll
        for (int nt = 0; nt < 4; nt++) wb[nt] = ga[nt] + head(i) * 4096;
      }
#pragma unroll
      for (int nt = 0; nt < 4; nt++) { const unsigned p = wb[nt] + ((i & 2) * 4 + (i & 1)) * 4096; a[i % (LAG + 2)][nt] = lds_tr2(p, p + 1024); }
    };
    u32x2 fl;
    if (w == 0) wait_until(t3);
    bar();
    STAMP(9);
#pragma unroll
    for (int i = 0; i <= PF; i++) { ldo(i); SB; }
    if (NB) { waitv<2 * PF + 2 - LAG>(); __builtin_amdgcn_raw_buffer_store_b32(T, rws, fa, 0, SC1); }
    const unsigned tf = __builtin_amdgcn_readfirstlane(*LP(const unsigned, sb + L_CNT)) + (D4 - D3);
    SB;
    ldw(0);
    SB;
#pragma unroll
    for (int i = 0; i < 16 + LAG; i++) {
      if (i == FSTEP) { wait_until(tf); fl = __builtin_amdgcn_raw_buffer_load_b64(rws, fr, 0, SC1); SB; }
      if (i == 8 - PF) {
        STAMP(15);
        if (__builtin_expect(!__all(fr == OOB || (fl[0] == T && fl[1] == T)), 0)) {
          RETRY(31) = 100 + RETRY(31) % 100;
#pragma unroll 1
          for (int sp = 0; sp < SPIN_CAP; sp++) {
            if (fl[0] == T && fl[1] == T) fr = OOB;
            if (__all(fr == OOB)) break;
            __builtin_amdgcn_s_sleep(GSL);
            asm volatile("" ::: "memory");
            fl = __builtin_amdgcn_raw_buffer_load_b64(rws, fr, 0, SC1);
          }
        }
        asm volatile("" ::: "memory");
        STAMP(8);
      }
      if (i) ldo(i + PF);
      SB;
      if (i < 8) {
        auto pending = [&]() { unsigned m = umax4(0, o1[i % (PF + 1)]); if (i >= LAG) m = umax4(m, o0[i % (PF + 1)]); return __any(m == EMPTY); };
        if (__builtin_expect(pending(), 0)) {
          if (w >= 4) RETRY(31) |= 1;
#pragma unroll 1
          for (int sp = 0; sp < SPIN_CAP; sp++) {
            __builtin_amdgcn_s_sleep(PSL);
            asm volatile("" ::: "memory");
            ldo(i);
            if (!pending()) break;
          }
        }
      }
      if (i < 15) ldw(i + 1);
      if (i < 16) {
#pragma unroll
        for (int nt = 0; nt < 4; nt++) c1[nt] = MFMA16(a[i % (LAG + 2)][nt], BF8(o1[i % (PF + 1)]), c1[nt]);
      }
      if (i >= LAG) {
#pragma unroll
        for (int nt = 0; nt < 4; nt++) c0[nt] = MFMA16(a[(i - LAG) % (LAG + 2)][nt], BF8(o0[i % (PF + 1)]), c0[nt]);
      }
#pragma unroll
      for (int q = 0; q < 8; q++) { __builtin_amdgcn_sched_group_barrier(0x8, 1, 0); __builtin_amdgcn_sched_group_barrier(0x100, 1, 0); }
      SB;
      if (i == 15) {
        STAMP(10);
#pragma unroll
        for (int nt = 0; nt < 4; nt++) *LP(f32x4, xw + nt * 1024) = c1[nt];
      }
      if (i == (LAG ? 14 + LAG : 15)) {
        bar();
#pragma unroll
        for (int nt = 0; nt < 4; nt++) px[nt] = *LP(const f32x4, xr + nt * 1024);
        SB;
      }
    }
#pragma unroll
    for (int nt = 0; nt < 4; nt++) {
      const f32x4 v = px[nt] + c0[nt];
      *LP(u32x2, tw ^ (nt << 5)) = u32x2{pk2(v[0], v[1]), pk2(v[2], v[3])};
    }
    asm volatile("" ::: "memory");
    const u32x4 t0 = *LP(const u32x4, tr), t1 = *LP(const u32x4, tr + 1024);
    __builtin_amdgcn_raw_buffer_store_b128(t0, rout, og, 0, SC1);
    __builtin_amdgcn_raw_buffer_store_b128(u32x4{t1[2], t1[3], t1[0], t1[1]}, rout, og + 16384, 0, SC1);
    asm volatile(".Lhedgehog_end:");
    STAMP(11);
    STAMP(30);
    STAMP_DUMP();
  }
}

extern "C" void kernel_launch(void* const* d_in, const int* in_sizes, int n_in, void* d_out, int out_size, void* d_ws, size_t ws_size,
                              hipStream_t stream) {
  const size_t ws_off = WSO < 0 ? (ws_size - (size_t)(-WSO) * (1u << 20)) & ~(size_t)((2u << 20) - 1) : (size_t)WSO << 20;
  (void)hipFuncSetAttribute((const void*)hedgehog_fused, hipFuncAttributeMaxDynamicSharedMemorySize, LDS_BYTES);
  hedgehog_fused<<<256, 512, LDS_BYTES, stream>>>((const u16*)d_in[0], (const u16*)d_in[1], (const u16*)d_in[2], (const u16*)d_in[3],
                                                   (u16*)d_out, (char*)d_ws + ws_off);
}
```

```cpp
#define D1 114
#define D2 28
#define D2B 80
#define WI1 6
#define WI2 10
#define WI3 20
#include <hip/hip_runtime.h>
#include <hip/hip_bf16.h>

typedef unsigned short u16;
typedef unsigned long long u64;
typedef __attribute__((ext_vector_type(8))) __bf16 bf16x8;
typedef __attribute__((ext_vector_type(4))) __bf16 bf16x4;
typedef __attribute__((ext_vector_type(2))) __bf16 bf16x2;
typedef __attribute__((ext_vector_type(16))) float f32x16;
typedef __attribute__((ext_vector_type(4))) float f32x4;
typedef __attribute__((ext_vector_type(2))) float f32x2;
typedef __attribute__((ext_vector_type(4))) unsigned u32x4;
typedef __attribute__((ext_vector_type(2))) unsigned u32x2;
typedef __amdgpu_buffer_rsrc_t rsrc_t;
#define LAS __attribute__((address_space(3)))
#define LP(T, a) ((LAS T*)(size_t)(a))
#define DI __device__ __forceinline__
#define MFMA32(a, b, c) __builtin_amdgcn_mfma_f32_32x32x16_bf16((a), (b), (c), 0, 0, 0)
#define MFMA16(a, b, c) __builtin_amdgcn_mfma_f32_16x16x32_bf16((a), (b), (c), 0, 0, 0)
#define SB __builtin_amdgcn_sched_barrier(0)
#define BF8(v) __builtin_bit_cast(bf16x8, v)
constexpr int SC1 = 16;
constexpr unsigned OOB = 0x80000000u;

#define VPF 3
#define IPN 2
#define WPT 9
#define D3 65
#define PSL 2
#define HSL 8
#define OLAG 2
#define OPF 2
#define WSO -12
#define CL0 1
#define SLW 3
#define WN0 3
#define WN1 23
#define WN2 27
#define CLO 0
#define PULL 0
#define WSM 1
#define XFIRST 0
#define OS 2
#define WNA 17
#define HY 1
#define IP0 0
#define UNR 3
#define QD 0
#define TLBT 1
#define DXA 45
#define DXB 245
#define D4 110
#define FSTEP 3
#define GSL 6
#define CPR 0
#define HP2 3
#define WSL 0
#define WROT 0
#define WAUX 0
#define WN3 32
#define NB 0
#define NOWO 0
#define WE 1
#define FC 0
#define D5 25
#define HP 1
#define HPOS(h) (HP ? ((h) & 16) + ((h) & 1) * 8 + (((h) >> 1) & 7) : (h))
#define OPF2 OPF
#define GP 8
#define PM 0
#define TB 1
#define C3 0
#define OAUX 16
#define WL 1
#define BC 0

constexpr int SLOT = 40960;
constexpr int L_WO = 0;
constexpr int L_V = 122880;
constexpr int L_PHI = 147456;
constexpr int L_Z = 151552;
constexpr int L_CNT = 151808;
constexpr int L_YC = 151816;
constexpr int L_KV = 139264;
constexpr int L_DUMMY = 152064;
constexpr int L_FX = 131072;
constexpr int LDS_BYTES = 163840;
constexpr unsigned W_O = 0;
constexpr unsigned W_P = 8u << 20;
constexpr unsigned W_PZ = 10u << 20;
constexpr unsigned W_R = W_PZ + (128u << 10);
constexpr unsigned W_H1 = W_R + (128u << 10);
constexpr unsigned W_H2 = W_H1 + (64u << 10);
constexpr unsigned W_F = W_H2 + (64u << 10);
constexpr unsigned W_H3 = W_H2 + 128;
constexpr unsigned W_E = W_F + (64u << 10);
constexpr unsigned W_TS = 9u << 20;
constexpr unsigned W_BYTES = 12u << 20;
constexpr int SPIN_CAP = 1 << 18;

DI void waitl() { __builtin_amdgcn_s_waitcnt(0xc07f); }
DI void bar() { asm volatile("" ::: "memory"); waitl(); asm volatile("s_barrier" ::: "memory"); }
DI void bar0() { asm volatile("s_barrier" ::: "memory"); }
template <int N> DI void waitv() { asm volatile("s_waitcnt vmcnt(%0)" ::"i"(N) : "memory"); }
DI unsigned pk2v(f32x2 v) { return __builtin_bit_cast(unsigned, __builtin_convertvector(v, bf16x2)); }
DI unsigned pk2(float a, float b) { return pk2v(f32x2{a, b}); }
DI bf16x8 pack16to32(const f32x4& c0, const f32x4& c1) {
  auto a = __builtin_amdgcn_permlane16_swap(pk2(c0[0], c0[1]), pk2(c1[0], c1[1]), false, false);
  auto b = __builtin_amdgcn_permlane16_swap(pk2(c0[2], c0[3]), pk2(c1[2], c1[3]), false, false);
  return BF8((u32x4{a[0], b[0], a[1], b[1]}));
}
DI float fold32(float a, float b) { auto r = __builtin_amdgcn_permlane32_swap(__float_as_uint(a), __float_as_uint(b), false, false); return __uint_as_float(r[0]) + __uint_as_float(r[1]); }
DI float fold16(float a, float b) { auto r = __builtin_amdgcn_permlane16_swap(__float_as_uint(a), __float_as_uint(b), false, false); return __uint_as_float(r[0]) + __uint_as_float(r[1]); }
DI float fold8(float a) { return a + __uint_as_float(__builtin_amdgcn_update_dpp(0u, __float_as_uint(a), 0x128, 0xf, 0xf, false)); }
DI float dot2(unsigned a, unsigned b, float c) { return __builtin_amdgcn_fdot2_f32_bf16(__builtin_bit_cast(bf16x2, a), __builtin_bit_cast(bf16x2, b), c, false); }
DI bf16x8 lds_b128(unsigned a) { return *LP(const bf16x8, a); }
DI bf16x8 lds_tr2(unsigned a0, unsigned a1) {
  bf16x4 lo = __builtin_amdgcn_ds_read_tr16_b64_v4bf16(LP(bf16x4, a0));
  bf16x4 hi = __builtin_amdgcn_ds_read_tr16_b64_v4bf16(LP(bf16x4, a1));
  return __builtin_shufflevector(lo, hi, 0, 1, 2, 3, 4, 5, 6, 7);
}
template <bool SCALED, class M, class P> DI float dual_softmax(const f32x16& p, unsigned (&pos)[8], unsigned (&neg)[8], M mid, P put) {
#define FMX __builtin_elementwise_maximum
#define FMN __builtin_elementwise_minimum
  float mx = FMX(FMX(p[0], p[1]), p[2]), mn = FMN(FMN(p[0], p[1]), p[2]), mx2 = FMX(FMX(p[3], p[4]), p[5]), mn2 = FMN(FMN(p[3], p[4]), p[5]);
#pragma unroll
  for (int i = 6; i < 14; i += 4) {
    mx = FMX(FMX(mx, p[i]), p[i + 1]); mn = FMN(FMN(mn, p[i]), p[i + 1]);
    mx2 = FMX(FMX(mx2, p[i + 2]), p[i + 3]); mn2 = FMN(FMN(mn2, p[i + 2]), p[i + 3]);
  }
  mx = FMX(FMX(mx, p[14]), p[15]); mn = FMN(FMN(mn, p[14]), p[15]);
  mx = FMX(mx, mx2); mn = FMN(mn, mn2);
  {
    auto r = __builtin_amdgcn_permlane32_swap(__float_as_uint(mx), __float_as_uint(mx), false, false);
    auto s = __builtin_amdgcn_permlane32_swap(__float_as_uint(mn), __float_as_uint(mn), false, false);
    mx = FMX(__uint_as_float(r[0]), __uint_as_float(r[1])); mn = FMN(__uint_as_float(s[0]), __uint_as_float(s[1]));
  }
  mid(mx, mn);
  const float c = 1.44269504088896341f;
  const f32x2 cp = {c, c}, cn = {-c, -c}, bx = {-mx * c, -mx * c}, bn = {mn * c, mn * c};
  f32x2 ep[8], en[8];
#pragma unroll
  for (int i = 0; i < 8; i++) {
    const f32x2 v = {p[2 * i], p[2 * i + 1]};
    const f32x2 a = __builtin_elementwise_fma(v, cp, bx), b = __builtin_elementwise_fma(v, cn, bn);
    ep[i] = f32x2{__builtin_amdgcn_exp2f(a[0]), __builtin_amdgcn_exp2f(a[1])};
    en[i] = f32x2{__builtin_amdgcn_exp2f(b[0]), __builtin_amdgcn_exp2f(b[1])};
  }
  const f32x2 s2p = ((ep[0] + ep[1]) + (ep[2] + ep[3])) + ((ep[4] + ep[5]) + (ep[6] + ep[7]));
  const f32x2 s2n = ((en[0] + en[1]) + (en[2] + en[3])) + ((en[4] + en[5]) + (en[6] + en[7]));
  const float sp = s2p[0] + s2p[1], sn = s2n[0] + s2n[1];
  const float tp = fold32(sp, sp);
  const float rp = SCALED ? 1.f : __builtin_amdgcn_rcpf(tp), rn = __builtin_amdgcn_rcpf(fold32(sn, sn)) * (SCALED ? tp : 1.f);
  const f32x2 r2p = {rp, rp}, r2n = {rn, rn};
#pragma unroll
  for (int i = 0; i < 8; i++) { pos[i] = pk2v(SCALED ? ep[i] : ep[i] * r2p); neg[i] = pk2v(en[i] * r2n); if (i & 1) put(i >> 1); }
  return tp;
}

constexpr unsigned EMPTY = 0xffffffffu;
DI unsigned seal(unsigned v) { return min(v, EMPTY - 1u); }
DI u32x4 seal4(u32x4 v) { return u32x4{seal(v[0]), seal(v[1]), seal(v[2]), seal(v[3])}; }
DI unsigned umax4(unsigned m, u32x4 v) { return max(max(max(max(m, v[0]), v[1]), v[2]), v[3]); }
template <int N> DI int settle(u32x4 (&g)[N], rsrc_t r, unsigned a, unsigned stride) {
  auto pending = [&]() { unsigned m = 0;
#pragma unroll
    for (int i = 0; i < N; i++) m = umax4(m, g[i]);
    return __any(m == EMPTY); };
  if (__builtin_expect(!pending(), 1)) return 0;
  int sp = 1;
#pragma unroll 1
  for (; sp < SPIN_CAP; sp++) {
    __builtin_amdgcn_s_sleep(PSL);
    asm volatile("" ::: "memory");
#pragma unroll
    for (int i = 0; i < N; i++) g[i] = __builtin_amdgcn_raw_buffer_load_b128(r, a + i * stride, 0, SC1);
    if (!pending()) break;
  }
  return sp;
}
DI unsigned now32() { u64 t; asm volatile("s_memrealtime %0\n\ts_waitcnt lgkmcnt(0)" : "=s"(t) :: "memory"); return (unsigned)t; }
DI void wait_until(unsigned t) {
  unsigned n = __builtin_amdgcn_readfirstlane(1000);
#pragma unroll 1
  while (t - now32() - 1u < 1000u && --n) { if (WSL) __builtin_amdgcn_s_sleep(WSL); }
}
DI int latest(u32x2 h, rsrc_t r, unsigned a, unsigned t0, unsigned T) {
  int d = -0x10000;
  if (a != OOB && h[1] == T) { d = (int)(h[0] - t0); a = OOB; }
  if (!__all(a == OOB)) {
#pragma unroll 1
    for (int sp = 0; sp < SPIN_CAP; sp++) {
      asm volatile("" ::: "memory");
      h = __builtin_amdgcn_raw_buffer_load_b64(r, a, 0, SC1);
      if (a != OOB && h[1] == T) { d = (int)(h[0] - t0); a = OOB; }
      if (__all(a == OOB)) break;
      __builtin_amdgcn_s_sleep(PSL);
    }
  }
  d = max(d, __builtin_amdgcn_update_dpp(d, d, 0x128, 0xf, 0xf, false));
  d = max(d, __builtin_amdgcn_update_dpp(d, d, 0x124, 0xf, 0xf, false));
  d = max(d, __builtin_amdgcn_update_dpp(d, d, 0x122, 0xf, 0xf, false));
  d = max(d, __builtin_amdgcn_update_dpp(d, d, 0x121, 0xf, 0xf, false));
  asm volatile("" ::: "memory");
  return __builtin_amdgcn_readfirstlane(d);
}
DI void count_in(unsigned lds_addr, unsigned n = 1) {
  asm volatile("s_mov_b64 exec, 1\n\tds_add_u32 %0, %1\n\ts_mov_b64 exec, -1" :: "v"(lds_addr), "v"(n) : "memory");
}
template <int SL = 1> DI void lds_wait(unsigned lds_addr, unsigned n, unsigned mask = ~0u) {
#pragma unroll 1
  for (int sp = 0; sp < SPIN_CAP * 8; sp++) { if ((__builtin_amdgcn_readfirstlane(*LP(const volatile unsigned, lds_addr)) & mask) >= n) break; __builtin_amdgcn_s_sleep(SL); }
  asm volatile("" ::: "memory");
}
DI void epoch_ask(const char* ws) { asm volatile("s_load_dword s99, %0, 0x0" :: "s"(ws + W_E) : "s99"); }
DI unsigned epoch_tag() { unsigned e; asm volatile("s_waitcnt lgkmcnt(0)\n\ts_add_u32 %0, s99, 1" : "=s"(e) :: "scc", "memory"); return e; }

#define STAMP(i)
#define STAMPV(i)
#define RETRY(i) retry_
#define STAMP_DUMP()

__global__ __launch_bounds__(512, 2) void hedgehog_fused(const u16* __restrict__ x, const u16* __restrict__ wqkv, const u16* __restrict__ wphi,
                                                         const u16* __restrict__ wo, u16* __restrict__ out, char* __restrict__ ws) {
  extern __shared__ __attribute__((aligned(16))) char smem[];
  const int tid = threadIdx.x, lane = tid & 63;
  const int w = __builtin_amdgcn_readfirstlane(tid >> 6), lw = w & 3;
  const int b = blockIdx.x, xcd = b & 7, idx = b >> 3;
  const int rl = idx & 7, cl = idx >> 3;
  const int rb = (xcd & 1) * 8 + rl, cb = (xcd >> 1) * 4 + cl;
  const int orb = (xcd >> 1) * 4 + (idx & 3), ocb = (xcd & 1) * 8 + (idx >> 2);
  const unsigned sb = (unsigned)(size_t)(LAS char*)smem;
  const int hh = lane >> 5, l31 = lane & 31, l15 = lane & 15, g4 = lane >> 4, q4 = l15 >> 2, p4 = lane & 3, r8 = lane >> 3, c8 = lane & 7;
  unsigned st_r = 0, st_c = 0, retry_ = 0; (void)st_r; (void)st_c; (void)retry_;
  const unsigned pw = lw & 1;
  unsigned sp = (b * 2 + (lw >> 1)) * (8192 - pw * 7424);
  unsigned vp = __umul24(l15, 2048 - pw * 1920) + __umul24(g4, 128 + pw * (64 * 6144 - 128));
  vp = g4 < IPN && g4 >= IP0 && l15 < 4 + 2 * pw ? vp : OOB;
  if (C3) vp = pw && g4 == 3 && l15 < 6 ? ((idx * 2 + (lw >> 1)) * 6 + l15) * 2048 + (xcd >> 1) * 512 + 384 : vp + sp;
  if (C3) sp = 0;
  unsigned vx[4], vw[6];
#pragma unroll
  for (int i = 0; i < 4; i++) vx[i] = (rb * 128 + lw * 32 + r8) * 2048 + ((c8 ^ ((i & 1) * 4 + (r8 >> 1))) << 4) + i * (16384 - 1024);
#pragma unroll
  for (int i = 0; i < 6; i++) vw[i] = cb * 128 + (lw * 16 + r8) * 6144 + ((c8 ^ ((i & 1) * 4 + (r8 & 2))) << 4) + (i >> 1) * 2048 + (i & 1) * (8 * 6144 - 1024);
  asm volatile("" : "+v"(vp), "+v"(vx[0]), "+v"(vx[1]), "+v"(vx[2]), "+v"(vx[3]), "+v"(vw[0]), "+v"(vw[1]), "+v"(vw[2]), "+v"(vw[3]), "+v"(vw[4]), "+v"(vw[5]));
  asm volatile("" : "+s"(sp));
  asm volatile("" :: "s"(x), "s"(wqkv), "s"(wphi), "s"(wo), "s"(out), "s"(ws));
  const rsrc_t rws = __builtin_amdgcn_make_buffer_rsrc(ws, 0, W_BYTES, 0x00020000);
  const rsrc_t rx = __builtin_amdgcn_make_buffer_rsrc((void*)x, 0, 2048 * 1024 * 2, 0x00020000);
  const rsrc_t rw = __builtin_amdgcn_make_buffer_rsrc((void*)wqkv, 0, 1024 * 3072 * 2, 0x00020000);
  const rsrc_t rout = __builtin_amdgcn_make_buffer_rsrc(out, 0, 2048 * 1024 * 2, 0x00020000);

  auto stage = [&](unsigned ox, unsigned ow, unsigned so) {
    LAS char* dx = (LAS char*)smem + so + lw * 4096;
    LAS char* dw = (LAS char*)smem + so + 16384 + lw * 2048;
#define LX(i) __builtin_amdgcn_raw_ptr_buffer_load_lds(rx, dx, 16, vx[i], ox, i * 1024, 0)
#define LW(i) __builtin_amdgcn_raw_ptr_buffer_load_lds(rw, dw + (i >> 1) * 8192, 16, vw[i], ow, (i & 1) * 1024, 0)
    LW(2); LW(3); LX(0); LX(1); LX(2); LX(3); LW(4); LW(5); LW(0); LW(1);
  };
  const rsrc_t ro = __builtin_amdgcn_make_buffer_rsrc((void*)wo, 0, NOWO ? 0 : 1024 * 1024 * 2, 0x00020000);
  u32x4 wr_[32];
  const unsigned vo = r8 * 2048 + c8 * 16, so_ = lw * 256 * 2048 + ocb * 128;
  auto wo_load = [&](int i0, int i1) {
#pragma unroll
    for (int i = i0; i < i1; i++) wr_[i] = __builtin_amdgcn_raw_buffer_load_b128(ro, vo, so_ + i * 16384, 0);
    SB;
  };
  u64 pc; unsigned code_len;
  asm volatile("s_getpc_b64 %0\n.Lhedgehog_begin:\n\ts_mov_b32 %1, .Lhedgehog_end-.Lhedgehog_begin" : "=s"(pc), "=s"(code_len));
  if (w < 4) {
    __builtin_amdgcn_raw_ptr_buffer_load_lds(lw & 1 ? rw : rx, (LAS char*)smem + L_DUMMY, 4, vp, sp, 0, 0);
    stage(0, 0, 0);
    SB;
    STAMP(25);
    const int m6 = (idx * 43) >> 8;
    const int turn = TB ? (cl == 3 ? rl >> 2 : 2 + (idx - 6 * m6 + xcd) % 6) : (rl ^ xcd) & 7, u = (TB ? (cl == 3 ? rl & 3 : m6) : cl) * 4 + lw, l3 = lane / 3;
    const unsigned px = lane < 16 ? lane * 2048 + ((xcd & 1) * 1024 + ((xcd >> 1) * 16 + u) * 16) * 2048 : OOB;
    unsigned pa_ = lane * 128 + l3 * (2048 - 384) + ((xcd & 1) * 96 + u * 6) * 2048, pb_ = lane * 2048 + (u * 12 - 18) * 2048 + 384;
    asm volatile("" : "+v"(pa_), "+v"(pb_));
    const unsigned pq = (lane < 18 ? pa_ : lane < 30 ? pb_ : OOB) + (xcd >> 1) * 512;
    const int uc = idx * 4 + lw;
    const char* sc = (const char*)(pc & ~127ull) + (uc * 256 < code_len ? uc : 0) * 256;
    unsigned o0 = cb * 16 + rb, o1 = rb * 16 + cb, o2 = W_H1 + lw * 65536;
    asm volatile("" : "+s"(o0), "+s"(o1), "+s"(o2));
    const char* s1 = (lw == 3 ? (const char*)wphi : ws) + (lw == 3 ? b * 256 : o2 + (lw == 0 ? o0 : o1) * 256);
    unsigned o3 = W_P + o0 * 8192, o4 = (lw == 1 ? rb : orb) * 262144 + cb * 2048, o5 = o4 * OS;
    asm volatile("" : "+s"(o3), "+s"(o4), "+s"(o5));
    const char* s2 = (lw == 3 ? (const char*)out : ws) + (lw == 0 ? o3 : lw == 3 ? o4 : o5);
    auto pull = [&](int s) {
      __builtin_amdgcn_raw_ptr_buffer_load_lds(rx, (LAS char*)smem + L_DUMMY, 4, px, s * 128, 0, 0);
      __builtin_amdgcn_raw_ptr_buffer_load_lds(rw, (LAS char*)smem + L_DUMMY, 4, pq, s * 64 * 6144, 0, 0);
    };
#define TOUCH(p) asm volatile("s_load_dwordx2 s[100:101], %0, 0x3c\n\ts_load_dwordx2 s[100:101], %0, 0xbc" :: "s"(p) : "s100", "s101")
    TOUCH(sc); TOUCH(s1); TOUCH(s2); epoch_ask(ws);
    sc = (const char*)wo + (b * 4 + lw) * 2048 - 512;
    unsigned turns = PM ? 1u << (4 + 3 * (turn >> 1) + (turn & 1)) : (0x101u << turn) & (0xffffu << IPN), ox = 128, ow = 64 * 6144, so = SLOT;
    asm volatile("" : "+s"(turns));
    turns >>= VPF - 1;
    unsigned tch = BC ? 0x3ffc : 0x3fc, d2 = 0xbc, dn = 256;
#pragma unroll 1
    for (int n = 1; n < 16; n++) {
      if (__builtin_expect(turns & 1, 0)) pull(n + VPF - 2);
      stage(ox, ow, so);
      ox += 128; ow += 64 * 6144; so = so == 2 * SLOT ? 0 : so + SLOT;
      asm volatile("s_bitcmp0_b32 %2, 0\n\ts_cbranch_scc1 1f\n\ts_load_dwordx2 s[100:101], %1, 0x3c\n\ts_load_dwordx2 s[100:101], %1, %6\n1:\n\t"
                   "s_bitcmp1_b32 %0, 0\n\ts_waitcnt vmcnt(12)\n\ts_cbranch_scc1 2f\n\ts_waitcnt vmcnt(10)\n2:\n\ts_barrier"
                   :: "s"(turns), "s"(sc), "s"(tch), "s"(ox), "s"(ow), "s"(so), "s"(d2) : "s100", "s101", "scc", "memory");
      turns >>= 1; tch >>= 1; sc += dn;
      if (BC && n == 10) { sc = (const char*)wo + uc * 16384 + (xcd & 1) * 1024 + 384; d2 = 0x83c; dn = 4096; }
    }
    const rsrc_t rp = __builtin_amdgcn_make_buffer_rsrc((void*)wphi, 0, 32 * 1024 * 2, 0x00020000);
    __builtin_amdgcn_raw_ptr_buffer_load_lds(rp, (LAS char*)smem + L_PHI + lw * 1024, 16, lane * 16, cb * 4096 + lw * 1024, 0, 0);
    asm volatile("" ::: "memory"); SB;
    const u32x4 e4 = {EMPTY, EMPTY, EMPTY, EMPTY};
#pragma unroll
    for (int i = 0; i < 2; i++) __builtin_amdgcn_raw_buffer_store_b128(e4, rws, W_P + (cb * 16 + rb) * 8192 + lw * 2048 + i * 1024 + lane * 16, 0, SC1);
    __builtin_amdgcn_raw_buffer_store_b128(e4, rws, (cb * 16 + rb) * 512 + lw * 128 + (lane < 8 ? W_PZ + c8 * 16 : lane < 16 ? W_R + c8 * 16 : OOB), 0, SC1);
#pragma unroll
    for (int i = 0; i < 2; i++)
      __builtin_amdgcn_raw_buffer_store_b128(e4, rws, W_O + (((rb * 8 + lw * 2 + i) * 32 + HPOS(cb * 2)) * 1024 + g4 * 256) * OS + l15 * 16, 0, SC1);
    SB;
    wo_load(0, WN0);
    waitv<5 + WN0>(); bar0();
  }
  unsigned T, t3 = 0, fa = OOB;
  const int rg = lw, kh = w >> 2;
  unsigned ga[4], v0, v1, xw, xr, og, tw, tr, fr;
  auto phase2_setup = [&]() {
    const int kr = 16 * (g4 >> 1) + 4 * (g4 & 1) + q4;
#pragma unroll
    for (int nt = 0; nt < 4; nt++) ga[nt] = sb + L_WO + kh * 65536 + kr * 128 + ((nt ^ ((kr >> 1) & 3)) << 5) + p4 * 8;
    v0 = l15 * 16 + (g4 * 256 + kh * 32768) * OS; v1 = l15 * 16 + (g4 * 256 + (kh ^ 1) * 32768) * OS;
    xw = sb + L_FX + w * 4096 + lane * 16; xr = sb + L_FX + (w ^ 4) * 4096 + lane * 16;
    og = (orb * 128 + rg * 32 + kh * 16 + r8) * 2048 + ocb * 128 + c8 * 16;
    tw = sb + L_WO + w * 2048 + l15 * 128 + (((g4 >> 1) ^ (l15 & 7)) << 4) + (((g4 & 1) ^ (l15 >> 3)) << 3);
    tr = sb + L_WO + w * 2048 + r8 * 128 + ((c8 ^ r8) << 4);
    fr = lane < 8 ? W_F + (orb * 16 + kh * 8 + lane) * 256 + (rg >> 1) * 8 : OOB;
    asm volatile("" : "+v"(ga[0]), "+v"(ga[1]), "+v"(ga[2]), "+v"(ga[3]), "+v"(v0), "+v"(v1), "+v"(xw), "+v"(xr), "+v"(og), "+v"(tw), "+v"(tr), "+v"(fr));
  };
  int role = w; asm volatile("" : "+s"(role));

  if (role < 4) {
    T = epoch_tag();
    if (tid == 0) *LP(u32x2, sb + L_CNT) = u32x2{0, T};
    unsigned tya, tx;
    STAMP(12);
    wo_load(WN0, WNA);
    unsigned touch = __builtin_amdgcn_raw_buffer_load_b32(lw & 1 ? rout : rws, lane == 0 ? 0 : OOB, (orb * 262144 + ocb * 128) * (lw & 1 ? 1 : OS), SC1);
    unsigned ha = lane < 16 && lane != rb ? W_H1 + (cb * 16 + lane) * 256 : OOB;
    u32x2 h1;
    {
      const int srow = 4 * g4 + q4;
      const int sw = ((srow >> 1) & 1) | ((srow >> 2) & 2) | (srow & 4);
      unsigned ia[2], va = sb + L_V + (lw >> 1) * 4096 + lane * 16;
#pragma unroll
      for (int ft = 0; ft < 2; ft++) ia[ft] = sb + (1 + (lw >> 1)) * SLOT + 24576 + (lw & 1) * 8192 + srow * 64 + (((4 * ft + p4) ^ sw) << 3);
      unsigned pa = W_P + (cb * 16 + rb) * 8192 + lw * 2048 + g4 * 512 + l15 * 16;
      unsigned za = l15 < 2 ? W_PZ + (cb * 16 + rb) * 512 + lw * 128 + (l15 * 4 + g4) * 16 : OOB;
      u32x4 one4 = {0x3f803f80u, 0x3f803f80u, 0x3f803f80u, 0x3f803f80u};
      f32x4 kv[2][2] = {}, z[2] = {};
      asm volatile("" : "+v"(ia[0]), "+v"(ia[1]), "+v"(va), "+v"(pa), "+v"(za), "+v"(one4), "+v"(ha));
      __builtin_amdgcn_s_waitcnt(0x0f70 | ((WNA - WN0 + 1) & 15) | (((WNA - WN0 + 1) >> 4) << 14));
      STAMP(1);
      bar();
      tya = now32();
      __builtin_amdgcn_raw_buffer_store_b64(u32x2{tya, T}, rws, tid == 0 ? W_H1 + (cb * 16 + rb) * 256 : OOB, 0, SC1);
      SB;
      if (WE) wo_load(WNA, WN1);
      bf16x8 vf[2][2][2];
#pragma unroll
      for (int i = 0; i < 8; i++) vf[i >> 2][(i >> 1) & 1][i & 1] = lds_b128(va + (i >> 2) * 8192 + (i & 3) * 1024);
#pragma unroll
      for (int rt = 0; rt < 2; rt++) {
        if (rt) {
          asm volatile("" : "+v"(kv[0][0]), "+v"(kv[0][1]), "+v"(kv[1][0]), "+v"(kv[1][1]), "+v"(z[0]), "+v"(z[1]));
          if (!WE) wo_load(WNA, WN1);
          __builtin_amdgcn_s_setprio(3);
          STAMP(13);
          bar();
          h1 = __builtin_amdgcn_raw_buffer_load_b64(rws, ha, 0, SC1);
        }
        bf16x8 pf[2][2];
#pragma unroll
        for (int i = 0; i < 4; i++) { const unsigned a = ia[i & 1] + (i >> 1) * 4096 + rt * 2048; pf[i >> 1][i & 1] = lds_tr2(a, a + 1024); }
        if (rt) count_in(sb + L_CNT, 0x100);
        SB;
#pragma unroll
        for (int h = 0; h < 2; h++)
#pragma unroll
          for (int et = 0; et < 2; et++)
#pragma unroll
            for (int ft = 0; ft < 2; ft++) kv[ft][et] = MFMA16(pf[h][ft], vf[h][rt][et], kv[ft][et]);
        SB;
#pragma unroll
        for (int h = 0; h < 2; h++)
#pragma unroll
          for (int ft = 0; ft < 2; ft++) z[ft] = MFMA16(pf[h][ft], BF8(one4), z[ft]);
        SB;
      }
#pragma unroll
      for (int et = 0; et < 2; et++)
        __builtin_amdgcn_raw_buffer_store_b128(seal4(u32x4{pk2(kv[0][et][0], kv[0][et][1]), pk2(kv[0][et][2], kv[0][et][3]),
                                                           pk2(kv[1][et][0], kv[1][et][1]), pk2(kv[1][et][2], kv[1][et][3])}), rws, pa + et * 256, 0, SC1);
      u32x4 zs;
#pragma unroll
      for (int j = 0; j < 4; j++) zs[j] = seal(__float_as_uint((lane & 1) ? z[1][j] : z[0][j]));
      __builtin_amdgcn_raw_buffer_store_b128(zs, rws, za, 0, SC1);
      SB;
    }
    STAMP(6);
    __builtin_amdgcn_s_setprio(HP2);
    const unsigned da = sb + L_WO + lw * 32768 + r8 * 128 + ((c8 ^ (r8 & 6)) << 4);
    auto wo_image = [&](int i0, int i1) {
#pragma unroll
      for (int i = i0; i < i1; i++) *LP(u32x4, da + i * 1024) = wr_[i];
      SB;
    };
    const unsigned t1 = tya + max(latest(h1, rws, ha, tya, T), 0) + D1;
    {
      u32x4 g[2], gz[2];
      const unsigned pa = W_P + (cb * 16 + r8) * 8192 + rb * 512 + lw * 128 + c8 * 16;
      const unsigned za = W_PZ + (cb * 16 + r8) * 512 + lw * 128 + c8 * 16;
      const unsigned ra = (lane & 8) ? OOB : W_R + cb * 8192 + rb * 512 + lw * 128 + c8 * 16 + g4 * 4;
      wait_until(t1);
      STAMP(16);
#pragma unroll
      for (int i = 0; i < 2; i++) g[i] = __builtin_amdgcn_raw_buffer_load_b128(rws, pa + i * 65536, 0, SC1);
#pragma unroll
      for (int i = 0; i < 2; i++) gz[i] = __builtin_amdgcn_raw_buffer_load_b128(rws, za + i * 4096, 0, SC1);
      SB;
      wo_load(WN1, WN2);
      lds_wait(sb + L_CNT, 0x800, 0xff00);
      wo_image(0, WI1);
      RETRY(31) += settle<2>(g, rws, pa, 65536) ? 1 : 0;
      STAMP(7);
      float s[8];
#pragma unroll
      for (int j = 0; j < 4; j++) {
        s[2 * j] = __uint_as_float(g[0][j] << 16) + __uint_as_float(g[1][j] << 16);
        s[2 * j + 1] = __uint_as_float(g[0][j] & 0xffff0000u) + __uint_as_float(g[1][j] & 0xffff0000u);
      }
      const float a0 = fold16(fold32(s[0], s[4]), fold32(s[2], s[6])), a1 = fold16(fold32(s[1], s[5]), fold32(s[3], s[7]));
      __builtin_amdgcn_raw_buffer_store_b32(seal(pk2(fold8(a0), fold8(a1))), rws, ra, 0, SC1);
      unsigned t2 = now32() + D2;
      if ((int)(t1 + D2B - t2) > 0) t2 = t1 + D2B;
      SB;
      STAMP(2);
      settle<2>(gz, rws, za, 4096);
#pragma unroll
      for (int j = 0; j < 4; j++) s[j] = __uint_as_float(gz[0][j]) + __uint_as_float(gz[1][j]);
      float z0 = fold32(s[0], s[2]), z1 = fold32(s[1], s[3]);
      z0 = fold8(fold16(z0, z0)); z1 = fold8(fold16(z1, z1));
      if ((lane & 24) == 0) *LP(unsigned, sb + L_Z + lw * 64 + (c8 & 1) * 32 + ((c8 >> 1) & 1) * 16 + (c8 >> 2) * 8 + hh * 4) = pk2(z0, z1);
      count_in(sb + L_CNT, 0x10000);
      STAMP(3);
      const unsigned ka = W_R + cb * 8192 + lw * 2048 + lane * 16;
      wait_until(t2);
#pragma unroll
      for (int i = 0; i < 2; i++) g[i] = __builtin_amdgcn_raw_buffer_load_b128(rws, ka + i * 1024, 0, SC1);
      SB;
      wo_load(WN2, 32);
      wo_image(WI1, WI2);
      RETRY(31) += settle<2>(g, rws, ka, 1024) ? 10 : 0;
#pragma unroll
      for (int i = 0; i < 2; i++) *LP(u32x4, sb + L_KV + (lw * 2 + i) * 1024 + lane * 16) = g[i];
    }
    unsigned hs = tid == 0 ? W_H2 + (rb * 16 + cb) * 256 : OOB;
    ha = tid < 16 ? W_H2 + (orb * 16 + lane) * 256 : OOB;
    asm volatile("" : "+v"(hs), "+v"(ha));
    STAMP(4);
    bar();
    tx = now32();
    __builtin_amdgcn_raw_buffer_store_b64(u32x2{tx, T}, rws, hs, 0, SC1);
    __builtin_amdgcn_s_setprio(0);
    phase2_setup();
    SB;
    wo_image(WI2, WI3);
    h1 = __builtin_amdgcn_raw_buffer_load_b64(rws, ha, 0, SC1);
    SB;
    wo_image(WI3, 32);
    asm volatile("" :: "v"(touch));
    STAMP(5);
    asm volatile("" : "+v"(h1));
    if (w == 0) {
      t3 = tx + latest(h1, rws, ha, tx, T) + D3;
      if (b == 0) __builtin_amdgcn_raw_buffer_store_b32(T, rws, lane == 0 ? W_E : OOB, 0, SC1);
      STAMP(14);
    }
  } else {
    const int wr = lw >> 1, wc = lw & 1;
    __builtin_amdgcn_s_waitcnt(0x0f70);
    f32x4 acc[6][4] = {};
    unsigned xa[2], wa[2];
#pragma unroll
    for (int ks = 0; ks < 2; ks++) xa[ks] = sb + (wr * 64 + l15) * 128 + (((4 * ks + g4) ^ (l15 >> 1)) << 4);
#pragma unroll
    for (int nt = 0; nt < 2; nt++) wa[nt] = sb + 16384 + (8 * g4 + q4) * 128 + (((wc * 2 + nt) ^ ((q4 >> 1) | ((g4 & 1) << 1))) << 5) + p4 * 8;
    bf16x8 fx[4][4], fw[4][6];
    unsigned xs[3][2], wsl[3][2];
#pragma unroll
    for (int sl = 0; sl < 3; sl++) {
      xs[sl][0] = xa[0] + sl * SLOT; xs[sl][1] = xa[1] + sl * SLOT; wsl[sl][0] = wa[0] + sl * SLOT; wsl[sl][1] = wa[1] + sl * SLOT;
      asm volatile("" : "+v"(xs[sl][0]), "+v"(xs[sl][1]), "+v"(wsl[sl][0]), "+v"(wsl[sl][1]));
    }
    auto rdw = [&](int buf, int j, int ks, int sl) {
      const unsigned a = wsl[sl][j & 1] + (j < 4 ? 8192 + (j >> 1) * 8192 : 0) + ks * 4096;
      fw[buf][j] = lds_tr2(a, a + 512);
    };
    auto rd = [&](int buf, int ks, int sl, int j0, int j1) {
      rdw(buf, j0, ks, sl);
#pragma unroll
      for (int m = 0; m < 4; m++) fx[buf][m] = lds_b128(xs[sl][ks] + m * 2048);
#pragma unroll
      for (int j = j0 + 1; j < j1; j++) rdw(buf, j, ks, sl);
    };
    auto mm = [&](int buf, int j0, int j1) {
#pragma unroll
      for (int j = j0; j < j1; j++)
#pragma unroll
        for (int m0 = 0; m0 < 4; m0++) {
          const int m = (j & 1) ? 3 - m0 : m0;
          acc[j][m] = (j == 2 || j == 3) ? MFMA16(fx[buf][m], fw[buf][j], acc[j][m]) : MFMA16(fw[buf][j], fx[buf][m], acc[j][m]);
        }
    };
#define SCHED(nds, nmf) do { _Pragma("unroll") for (int i_ = 0; i_ < (nds); i_++) { __builtin_amdgcn_sched_group_barrier(0x8, 1, 0); __builtin_amdgcn_sched_group_barrier(0x100, 1, 0); } \
      if ((nmf) > (nds)) __builtin_amdgcn_sched_group_barrier(0x8, (nmf) - (nds), 0); SB; } while (0)
#pragma unroll
    for (int j = 0; j < 6; j++) asm volatile("" : "+v"(acc[j][0]), "+v"(acc[j][1]), "+v"(acc[j][2]), "+v"(acc[j][3]));
    unsigned wpa = sb + L_PHI + wc * 2048 + (8 * hh + q4) * 64 + (g4 & 1) * 32 + p4 * 8, vwa = sb + L_V + lw * 4096 + lane * 16;
    unsigned pia[4];
    {
      const int sw = ((l31 >> 1) & 1) | ((l31 >> 2) & 2) | (l31 & 4);
#pragma unroll
      for (int q = 0; q < 4; q++) pia[q] = sb + (1 + wc) * SLOT + 24576 + (wr * 64 + l31) * 64 + (((2 * q + hh) ^ sw) << 3);
      asm volatile("" : "+v"(pia[0]), "+v"(pia[1]), "+v"(pia[2]), "+v"(pia[3]), "+v"(wpa), "+v"(vwa));
    }
    bar();
    STAMP(1);
    rd(0, 0, 0, 0, 6);
    SB;
#pragma unroll 1
    for (int t = 0; t < 12; t += 3) {
#pragma unroll
      for (int u = 0; u < 3; u++) {
        if (WL) { waitl(); SB; }
        rd(1, 1, u, 0, 6); mm(0, 0, 6); SCHED(16, 24);
        bar();
        rd(0, 0, (u + 1) % 3, 0, 6); mm(1, 0, 6); SCHED(16, 24);
      }
    }
    rd(1, 1, 0, 0, 6); mm(0, 0, 6); SCHED(16, 24);
    bar();
    rd(0, 0, 1, 0, 4); mm(1, 0, 6); SCHED(12, 24);
    rd(1, 1, 1, 0, 4); mm(0, 0, 4); SCHED(12, 16);
    bar();
    rd(0, 0, 2, 0, 4); mm(1, 0, 4); SCHED(12, 16);
    rd(1, 1, 2, 0, 4); mm(0, 0, 4); SCHED(12, 16);
    bar();
    __builtin_amdgcn_s_setprio(CPR);
    rd(0, 0, 0, 0, 4); mm(1, 0, 4); SCHED(12, 16);
    bf16x8 wp[2];
    rd(1, 1, 0, 0, 4);
    wp[0] = lds_tr2(wpa, wpa + 256); wp[1] = lds_tr2(wpa + 1024, wpa + 1280);
    mm(0, 0, 4); SCHED(16, 16);
    STAMP(2);
    const f32x16 zero = {};
    auto project = [&](int j0, f32x16 (&p)[2], auto fill0, auto fill1) {
#pragma unroll
      for (int i = 0; i < 8; i++) { const int j = j0 + ((i >> 1) & 1), m = (i >> 2) * 2 + (i & 1); acc[j][m] = MFMA16(fw[1][j], fx[1][m], acc[j][m]); SB; }
      fill0();
      const bf16x8 a0 = pack16to32(acc[j0][0], acc[j0][1]), a1 = pack16to32(acc[j0 + 1][0], acc[j0 + 1][1]);
#pragma unroll
      for (int i = 0; i < 8; i++) { __builtin_amdgcn_sched_group_barrier(0x8, 1, 0); __builtin_amdgcn_sched_group_barrier(0x2, 2, 0); }
      SB;
      p[0] = MFMA32(wp[0], a0, zero); SB;
      const bf16x8 b0 = pack16to32(acc[j0][2], acc[j0][3]); SB;
      p[0] = MFMA32(wp[1], a1, p[0]); SB;
      const bf16x8 b1 = pack16to32(acc[j0 + 1][2], acc[j0 + 1][3]); SB;
      p[1] = MFMA32(wp[0], b0, zero); SB;
      fill1(0); SB;
      p[1] = MFMA32(wp[1], b1, p[1]); SB;
      fill1(1); SB;
    };
    f32x16 pk[2];
    project(0, pk, [&]() { mm(1, 2, 4); }, [&](int rt) {
#pragma unroll
      for (int et = 0; et < 2; et++) {
        const f32x4 a = acc[2 + et][2 * rt], c = acc[2 + et][2 * rt + 1];
        *LP(u32x4, vwa + (rt * 2 + et) * 1024) = u32x4{pk2(a[0], a[1]), pk2(a[2], a[3]), pk2(c[0], c[1]), pk2(c[2], c[3])};
      }
    });
    STAMP(20);
#pragma unroll
    for (int rt = 0; rt < 2; rt++) {
      unsigned ph[2][8];
      dual_softmax<false>(pk[rt], ph[0], ph[1],
        [&](float& mx, float& mn) { if (rt) { asm volatile("" : "+v"(mx), "+v"(mn)); STAMP(21); bar(); STAMP(22);
          rdw(0, 4, 0, 0); rdw(0, 5, 0, 0); rdw(1, 4, 1, 0); rdw(1, 5, 1, 0); rd(2, 0, 1, 4, 6); SB;
          asm volatile("" : "+v"(mx), "+v"(mn)); } },
        [&](int q) {
          *LP(u32x2, pia[q] + rt * 2048) = u32x2{ph[0][2 * q], ph[0][2 * q + 1]};
          *LP(u32x2, pia[q] + rt * 2048 + 8192) = u32x2{ph[1][2 * q], ph[1][2 * q + 1]};
          SB;
        });
    }
    STAMP(3);
    bar();
    STAMP(4);
    SB;
    rd(3, 1, 1, 4, 6); mm(0, 4, 6); SCHED(8, 8);
    rd(0, 0, 2, 4, 6); mm(1, 4, 6); SCHED(8, 8);
    rd(1, 1, 2, 4, 6); mm(2, 4, 6); SCHED(8, 8);
    count_in(sb + L_CNT, 0x100);
    mm(3, 4, 6); SB;
    mm(0, 4, 6); SB;
    STAMP(23);
    project(4, pk, []() {}, [](int) {});
    STAMP(24);
    u32x4 phq[2][4], zf[4];
    u32x2 zc;
    float eps[2];
    unsigned ka = sb + L_KV + wc * 4096 + lane * 16, zl = sb + L_Z + wc * 128 + hh * 32;
    auto zread = [&]() {
      zc = *LP(const volatile u32x2, sb + L_CNT);
#pragma unroll
      for (int i = 0; i < 4; i++) zf[i] = *LP(const volatile u32x4, zl + (i >> 1) * 64 + (i & 1) * 16);
    };
#pragma unroll
    for (int rt = 0; rt < 2; rt++) {
      unsigned ph[2][8];
      eps[rt] = 1e-6f * dual_softmax<true>(pk[rt], ph[0], ph[1], [](float&, float&) {}, [&](int q) { if (rt && q == 0) { SB; zread(); SB; } });
#pragma unroll
      for (int i = 0; i < 4; i++) { const int sg = i >> 1, st = i & 1; phq[rt][i] = u32x4{ph[sg][2 * st], ph[sg][2 * st + 1], ph[sg][4 + 2 * st], ph[sg][5 + 2 * st]}; }
      asm volatile("" : "+v"(phq[rt][0]), "+v"(phq[rt][1]), "+v"(phq[rt][2]), "+v"(phq[rt][3]));
    }
    unsigned oa = W_O + (((rb * 8 + wr * 4 + (g4 & 1)) * 32 + HPOS(cb * 2 + wc)) * 1024 + hh * 256) * OS + l15 * 16, oa1 = oa + 65536 * OS;
    fa = lane == 0 ? W_F + (rb * 16 + cb) * 256 + lw * 4 : OOB;
    asm volatile("" : "+v"(ka), "+v"(oa), "+v"(oa1), "+v"(fa));
    STAMP(5);
    if (__builtin_amdgcn_readfirstlane(zc[0]) < 0x40000) { lds_wait(sb + L_CNT, 0x40000); zread(); }
    T = __builtin_amdgcn_readfirstlane(zc[1]);
    float rden[2];
    {
      float dn[2][2] = {};
#pragma unroll
      for (int i = 0; i < 4; i++)
#pragma unroll
        for (int j = 0; j < 4; j++)
#pragma unroll
          for (int rt = 0; rt < 2; rt++) dn[rt][j & 1] = dot2(phq[rt][i][j], zf[i][j], dn[rt][j & 1]);
#pragma unroll
      for (int rt = 0; rt < 2; rt++) { const float d = dn[rt][0] + dn[rt][1]; rden[rt] = __builtin_amdgcn_rcpf(fold32(d, d) + eps[rt]); }
      asm volatile("" : "+v"(rden[0]), "+v"(rden[1]));
    }
    STAMP(16);
    __builtin_amdgcn_s_setprio(2);
    bar();
    STAMP(6);
    {
      bf16x8 kf[4];
#pragma unroll
      for (int i = 0; i < 4; i++) kf[i] = lds_b128(ka + i * 1024);
      auto put = [&](const f32x16& n, float r, int st, unsigned a) {
        const f32x2 r2 = {r, r};
        u32x4 v;
#pragma unroll
        for (int j = 0; j < 4; j++) v[j] = pk2v(f32x2{n[8 * st + 2 * j], n[8 * st + 2 * j + 1]} * r2);
        __builtin_amdgcn_raw_buffer_store_b128(v, rws, a + st * 512 * OS, 0, SC1);
      };
      SB;
      f32x16 n0 = zero, n1 = zero;
#pragma unroll
      for (int i = 0; i < 4; i++) { n0 = MFMA32(kf[i], BF8(phq[0][i]), n0); SB; }
#pragma unroll
      for (int i = 0; i < 3; i++) { n1 = MFMA32(kf[i], BF8(phq[1][i]), n1); SB; }
      put(n0, rden[0], 0, oa); SB;
      n1 = MFMA32(kf[3], BF8(phq[1][3]), n1); SB;
      put(n0, rden[0], 1, oa); SB;
      put(n1, rden[1], 0, oa1); SB; put(n1, rden[1], 1, oa1); SB;
    }
    STAMP(7);
    phase2_setup();
    waitv<0>();
    __builtin_amdgcn_raw_buffer_store_b32(T, rws, fa, 0, SC1);
    __builtin_amdgcn_s_setprio(GP ? 1 : 0);
  }
  {
    const unsigned ob = W_O + ((orb * 8 + rg * 2) * 32 + kh * 16) * 1024 * OS;
    constexpr int LAG = OLAG, PF = OPF;
    f32x4 c0[4], c1[4], px[4];
    const f32x4 z4 = {};
    constexpr int RN = OPF2 + 1;
    u32x4 o0[RN], o1[RN];
    bf16x8 a[LAG + 2][4];
    auto head = [&](int i) { return 2 * (i & 7) + (i >> 3); };
    auto ldo = [&](int i) {
      if (i < 16) o1[i % RN] = __builtin_amdgcn_raw_buffer_load_b128(rws, v1, ob + HPOS(head(i)) * 1024 * OS, SC1);
      if (i >= LAG && i < 16 + LAG) o0[i % RN] = __builtin_amdgcn_raw_buffer_load_b128(rws, v0, ob + HPOS(head(i - LAG)) * 1024 * OS, SC1);
    };
    auto ldw = [&](int i) {
#pragma unroll
      for (int nt = 0; nt < 4; nt++) { const unsigned p = ga[nt] + head(i) * 4096; a[i % (LAG + 2)][nt] = lds_tr2(p, p + 1024); }
    };
    auto step = [&](int i) {
      constexpr int E = OPF2 - OPF;
      const int e0 = i <= 8 - PF ? 0 : i - (8 - PF) < E ? i - (8 - PF) : E, e1 = i < 8 - PF ? 0 : i + 1 - (8 - PF) < E ? i + 1 - (8 - PF) : E;
      if (i) ldo(i + PF + e0);
      if (e1 > e0) ldo(i + PF + e1);
      if (GP && i == GP) __builtin_amdgcn_s_setprio(0);
      SB;
      if (i < 15) ldw(i + 1);
      if (i < 16) {
#pragma unroll
        for (int nt = 0; nt < 4; nt++) c1[nt] = MFMA16(a[i % (LAG + 2)][nt], BF8(o1[i % RN]), i ? c1[nt] : z4);
      }
      if (i >= LAG) {
#pragma unroll
        for (int nt = 0; nt < 4; nt++) c0[nt] = MFMA16(a[(i - LAG) % (LAG + 2)][nt], BF8(o0[i % RN]), i > LAG ? c0[nt] : z4);
      }
#pragma unroll
      for (int q = 0; q < 8; q++) { __builtin_amdgcn_sched_group_barrier(0x8, 1, 0); __builtin_amdgcn_sched_group_barrier(0x100, 1, 0); }
      SB;
    };
    if (w == 0) wait_until(t3);
    bar();
    STAMP(9);
#pragma unroll 1
    for (int pass = 0;; pass++) {
#pragma unroll
      for (int i = 0; i <= PF; i++) { ldo(i); SB; }
      ldw(0);
      SB;
      u32x2 fl;
#pragma unroll
      for (int i = 0; i < 9; i++) {
        if (i == FSTEP) { fl = __builtin_amdgcn_raw_buffer_load_b64(rws, fr, 0, SC1); SB; }
        if (i == 8 - PF) {
          STAMP(15);
          if (__builtin_expect(!__all(fr == OOB || (fl[0] == T && fl[1] == T)), 0)) {
            RETRY(31) = 100 + RETRY(31) % 100;
#pragma unroll 1
            for (int sp = 0; sp < SPIN_CAP; sp++) {
              if (fl[0] == T && fl[1] == T) fr = OOB;
              if (__all(fr == OOB)) break;
              __builtin_amdgcn_s_sleep(GSL);
              asm volatile("" ::: "memory");
              fl = __builtin_amdgcn_raw_buffer_load_b64(rws, fr, 0, SC1);
            }
          }
          asm volatile("" ::: "memory");
        }
        step(i);
      }
      if (__builtin_expect(pass || !__any(c0[0][0] != c0[0][0] || c1[0][0] != c1[0][0]), 1)) break;
      if (w >= 4) RETRY(31) |= 1;
      asm volatile("" ::: "memory");
    }
#pragma unroll
    for (int i = 9; i < 16 + LAG; i++) {
      step(i);
      if (i == 15) {
        STAMP(10);
#pragma unroll
        for (int nt = 0; nt < 4; nt++) *LP(f32x4, xw + nt * 1024) = c1[nt];
      }
      if (i == (LAG ? 14 + LAG : 15)) {
        bar();
#pragma unroll
        for (int nt = 0; nt < 4; nt++) px[nt] = *LP(const f32x4, xr + nt * 1024);
        SB;
      }
    }
#pragma unroll
    for (int nt = 0; nt < 4; nt++) {
      const f32x4 v = px[nt] + c0[nt];
      *LP(u32x2, tw ^ (nt << 5)) = u32x2{pk2(v[0], v[1]), pk2(v[2], v[3])};
    }
    asm volatile("" ::: "memory");
    const u32x4 t0 = *LP(const u32x4, tr), t1 = *LP(const u32x4, tr + 1024);
    __builtin_amdgcn_raw_buffer_store_b128(t0, rout, og, 0, OAUX);
    __builtin_amdgcn_raw_buffer_store_b128(u32x4{t1[2], t1[3], t1[0], t1[1]}, rout, og + 16384, 0, OAUX);
    asm volatile(".Lhedgehog_end:");
    STAMP(11);
    STAMP(30);
    STAMP_DUMP();
  }
}

extern "C" void kernel_launch(void* const* d_in, const int* in_sizes, int n_in, void* d_out, int out_size, void* d_ws, size_t ws_size,
                              hipStream_t stream) {
  const size_t ws_off = WSO < 0 ? (ws_size - (size_t)(-WSO) * (1u << 20)) & ~(size_t)((2u << 20) - 1) : (size_t)WSO << 20;
  (void)hipFuncSetAttribute((const void*)hedgehog_fused, hipFuncAttributeMaxDynamicSharedMemorySize, LDS_BYTES);
  hedgehog_fused<<<256, 512, LDS_BYTES, stream>>>((const u16*)d_in[0], (const u16*)d_in[1], (const u16*)d_in[2], (const u16*)d_in[3],
                                                   (u16*)d_out, (char*)d_ws + ws_off);
}
```

```cpp
#define WN2 27
#define WI1 4
#define WI2 6
#include <hip/hip_runtime.h>
#include <hip/hip_bf16.h>

typedef unsigned short u16;
typedef unsigned long long u64;
typedef __attribute__((ext_vector_type(8))) __bf16 bf16x8;
typedef __attribute__((ext_vector_type(4))) __bf16 bf16x4;
typedef __attribute__((ext_vector_type(2))) __bf16 bf16x2;
typedef __attribute__((ext_vector_type(16))) float f32x16;
typedef __attribute__((ext_vector_type(4))) float f32x4;
typedef __attribute__((ext_vector_type(2))) float f32x2;
typedef __attribute__((ext_vector_type(4))) unsigned u32x4;
typedef __attribute__((ext_vector_type(2))) unsigned u32x2;
typedef __amdgpu_buffer_rsrc_t rsrc_t;
#define LAS __attribute__((address_space(3)))
#define LP(T, a) ((LAS T*)(size_t)(a))
#define DI __device__ __forceinline__
#define MFMA32(a, b, c) __builtin_amdgcn_mfma_f32_32x32x16_bf16((a), (b), (c), 0, 0, 0)
#define MFMA16(a, b, c) __builtin_amdgcn_mfma_f32_16x16x32_bf16((a), (b), (c), 0, 0, 0)
#define SB __builtin_amdgcn_sched_barrier(0)
#define BF8(v) __builtin_bit_cast(bf16x8, v)
constexpr int SC1 = 16;
constexpr unsigned OOB = 0x80000000u;

#define D1 118
#define D2 30
#define D2B 85
#define D3 65
#define WN0 3
#define WNA 17
#define WN1 23
#define WI3 18
#define WSO -12
#define STRESS 0
constexpr int OS = 2;
DI int hpos(int h) { return (h & 16) + (h & 1) * 8 + ((h >> 1) & 7); }

constexpr int SLOT = 40960;
constexpr int L_WO = 0;
constexpr int L_V = 122880;
constexpr int L_PHI = 147456;
constexpr int L_Z = 151552;
constexpr int L_CNT = 151808;
constexpr int L_KV = 139264;
constexpr int L_DUMMY = 152064;
constexpr int L_FX = 131072;
constexpr int LDS_BYTES = 163840;
constexpr unsigned W_O = 0;
constexpr unsigned W_P = 8u << 20;
constexpr unsigned W_PZ = 10u << 20;
constexpr unsigned W_R = W_PZ + (128u << 10);
constexpr unsigned W_H1 = W_R + (128u << 10);
constexpr unsigned W_H2 = W_H1 + (64u << 10);
constexpr unsigned W_F = W_H2 + (64u << 10);
constexpr unsigned W_E = W_F + (64u << 10);
constexpr unsigned W_TS = 9u << 20;
constexpr unsigned W_BYTES = 12u << 20;
constexpr int SPIN_CAP = 1 << 18;

DI void waitl() { __builtin_amdgcn_s_waitcnt(0xc07f); }
DI void bar() { asm volatile("" ::: "memory"); waitl(); asm volatile("s_barrier" ::: "memory"); }
DI void bar0() { asm volatile("s_barrier" ::: "memory"); }
template <int N> DI void waitv() { asm volatile("s_waitcnt vmcnt(%0)" ::"i"(N) : "memory"); }
DI unsigned pk2v(f32x2 v) { return __builtin_bit_cast(unsigned, __builtin_convertvector(v, bf16x2)); }
DI unsigned pk2(float a, float b) { return pk2v(f32x2{a, b}); }
DI bf16x8 pack16to32(const f32x4& c0, const f32x4& c1) {
  auto a = __builtin_amdgcn_permlane16_swap(pk2(c0[0], c0[1]), pk2(c1[0], c1[1]), false, false);
  auto b = __builtin_amdgcn_permlane16_swap(pk2(c0[2], c0[3]), pk2(c1[2], c1[3]), false, false);
  return BF8((u32x4{a[0], b[0], a[1], b[1]}));
}
DI float fold32(float a, float b) { auto r = __builtin_amdgcn_permlane32_swap(__float_as_uint(a), __float_as_uint(b), false, false); return __uint_as_float(r[0]) + __uint_as_float(r[1]); }
DI float fold16(float a, float b) { auto r = __builtin_amdgcn_permlane16_swap(__float_as_uint(a), __float_as_uint(b), false, false); return __uint_as_float(r[0]) + __uint_as_float(r[1]); }
DI float fold8(float a) { return a + __uint_as_float(__builtin_amdgcn_update_dpp(0u, __float_as_uint(a), 0x128, 0xf, 0xf, false)); }
DI float dot2(unsigned a, unsigned b, float c) { return __builtin_amdgcn_fdot2_f32_bf16(__builtin_bit_cast(bf16x2, a), __builtin_bit_cast(bf16x2, b), c, false); }
DI bf16x8 lds_b128(unsigned a) { return *LP(const bf16x8, a); }
DI bf16x8 lds_tr2(unsigned a0, unsigned a1) {
  bf16x4 lo = __builtin_amdgcn_ds_read_tr16_b64_v4bf16(LP(bf16x4, a0));
  bf16x4 hi = __builtin_amdgcn_ds_read_tr16_b64_v4bf16(LP(bf16x4, a1));
  return __builtin_shufflevector(lo, hi, 0, 1, 2, 3, 4, 5, 6, 7);
}
template <bool SCALED, class M, class P> DI float dual_softmax(const f32x16& p, unsigned (&pos)[8], unsigned (&neg)[8], M mid, P put) {
#define FMX __builtin_elementwise_maximum
#define FMN __builtin_elementwise_minimum
  float mx = FMX(FMX(p[0], p[1]), p[2]), mn = FMN(FMN(p[0], p[1]), p[2]), mx2 = FMX(FMX(p[3], p[4]), p[5]), mn2 = FMN(FMN(p[3], p[4]), p[5]);
#pragma unroll
  for (int i = 6; i < 14; i += 4) {
    mx = FMX(FMX(mx, p[i]), p[i + 1]); mn = FMN(FMN(mn, p[i]), p[i + 1]);
    mx2 = FMX(FMX(mx2, p[i + 2]), p[i + 3]); mn2 = FMN(FMN(mn2, p[i + 2]), p[i + 3]);
  }
  mx = FMX(FMX(mx, p[14]), p[15]); mn = FMN(FMN(mn, p[14]), p[15]);
  mx = FMX(mx, mx2); mn = FMN(mn, mn2);
  {
    auto r = __builtin_amdgcn_permlane32_swap(__float_as_uint(mx), __float_as_uint(mx), false, false);
    auto s = __builtin_amdgcn_permlane32_swap(__float_as_uint(mn), __float_as_uint(mn), false, false);
    mx = FMX(__uint_as_float(r[0]), __uint_as_float(r[1])); mn = FMN(__uint_as_float(s[0]), __uint_as_float(s[1]));
  }
  mid(mx, mn);
  const float c = 1.44269504088896341f;
  const f32x2 cp = {c, c}, cn = {-c, -c}, bx = {-mx * c, -mx * c}, bn = {mn * c, mn * c};
  f32x2 ep[8], en[8];
#pragma unroll
  for (int i = 0; i < 8; i++) {
    const f32x2 v = {p[2 * i], p[2 * i + 1]};
    const f32x2 a = __builtin_elementwise_fma(v, cp, bx), b = __builtin_elementwise_fma(v, cn, bn);
    ep[i] = f32x2{__builtin_amdgcn_exp2f(a[0]), __builtin_amdgcn_exp2f(a[1])};
    en[i] = f32x2{__builtin_amdgcn_exp2f(b[0]), __builtin_amdgcn_exp2f(b[1])};
  }
  const f32x2 s2p = ((ep[0] + ep[1]) + (ep[2] + ep[3])) + ((ep[4] + ep[5]) + (ep[6] + ep[7]));
  const f32x2 s2n = ((en[0] + en[1]) + (en[2] + en[3])) + ((en[4] + en[5]) + (en[6] + en[7]));
  const float sp = s2p[0] + s2p[1], sn = s2n[0] + s2n[1];
  const float tp = fold32(sp, sp);
  const float rp = SCALED ? 1.f : __builtin_amdgcn_rcpf(tp), rn = __builtin_amdgcn_rcpf(fold32(sn, sn)) * (SCALED ? tp : 1.f);
  const f32x2 r2p = {rp, rp}, r2n = {rn, rn};
#pragma unroll
  for (int i = 0; i < 8; i++) { pos[i] = pk2v(SCALED ? ep[i] : ep[i] * r2p); neg[i] = pk2v(en[i] * r2n); if (i & 1) put(i >> 1); }
  return tp;
}

constexpr unsigned EMPTY = 0xffffffffu;
DI unsigned seal(unsigned v) { return min(v, EMPTY - 1u); }
DI u32x4 seal4(u32x4 v) { return u32x4{seal(v[0]), seal(v[1]), seal(v[2]), seal(v[3])}; }
DI unsigned umax4(unsigned m, u32x4 v) { return max(max(max(max(m, v[0]), v[1]), v[2]), v[3]); }
template <int N> DI int settle(u32x4 (&g)[N], rsrc_t r, unsigned a, unsigned stride) {
  auto pending = [&]() { unsigned m = 0;
#pragma unroll
    for (int i = 0; i < N; i++) m = umax4(m, g[i]);
    return __any(m == EMPTY); };
  if (__builtin_expect(!pending(), 1)) return 0;
  int sp = 1;
#pragma unroll 1
  for (; sp < SPIN_CAP; sp++) {
    __builtin_amdgcn_s_sleep(2);
    asm volatile("" ::: "memory");
#pragma unroll
    for (int i = 0; i < N; i++) g[i] = __builtin_amdgcn_raw_buffer_load_b128(r, a + i * stride, 0, SC1);
    if (!pending()) break;
  }
  return sp;
}
DI unsigned now32() { u64 t; asm volatile("s_memrealtime %0\n\ts_waitcnt lgkmcnt(0)" : "=s"(t) :: "memory"); return (unsigned)t; }
DI void wait_until(unsigned t) {
  unsigned n = __builtin_amdgcn_readfirstlane(1000);
#pragma unroll 1
  while (t - now32() - 1u < 1000u && --n) {}
}
DI int latest(u32x2 h, rsrc_t r, unsigned a, unsigned t0, unsigned T) {
  int d = -0x10000;
  if (a != OOB && h[1] == T) { d = (int)(h[0] - t0); a = OOB; }
  if (!__all(a == OOB)) {
#pragma unroll 1
    for (int sp = 0; sp < SPIN_CAP; sp++) {
      asm volatile("" ::: "memory");
      h = __builtin_amdgcn_raw_buffer_load_b64(r, a, 0, SC1);
      if (a != OOB && h[1] == T) { d = (int)(h[0] - t0); a = OOB; }
      if (__all(a == OOB)) break;
      __builtin_amdgcn_s_sleep(2);
    }
  }
  d = max(d, __builtin_amdgcn_update_dpp(d, d, 0x128, 0xf, 0xf, false));
  d = max(d, __builtin_amdgcn_update_dpp(d, d, 0x124, 0xf, 0xf, false));
  d = max(d, __builtin_amdgcn_update_dpp(d, d, 0x122, 0xf, 0xf, false));
  d = max(d, __builtin_amdgcn_update_dpp(d, d, 0x121, 0xf, 0xf, false));
  asm volatile("" ::: "memory");
  return __builtin_amdgcn_readfirstlane(d);
}
DI void count_in(unsigned lds_addr, unsigned n = 1) {
  asm volatile("s_mov_b64 exec, 1\n\tds_add_u32 %0, %1\n\ts_mov_b64 exec, -1" :: "v"(lds_addr), "v"(n) : "memory");
}
template <int SL = 1> DI void lds_wait(unsigned lds_addr, unsigned n, unsigned mask = ~0u) {
#pragma unroll 1
  for (int sp = 0; sp < SPIN_CAP * 8; sp++) { if ((__builtin_amdgcn_readfirstlane(*LP(const volatile unsigned, lds_addr)) & mask) >= n) break; __builtin_amdgcn_s_sleep(SL); }
  asm volatile("" ::: "memory");
}
DI void epoch_ask(const char* ws) { asm volatile("s_load_dword s99, %0, 0x0" :: "s"(ws + W_E) : "s99"); }
DI unsigned epoch_tag() { unsigned e; asm volatile("s_waitcnt lgkmcnt(0)\n\ts_mov_b32 %0, s99" : "=s"(e) :: "memory"); return e * 1664525u + 1013904223u; }

#define JOLT(k)
#define STAMP(i)
#define STAMPV(i)
#define RETRY(i) retry_
#define STAMP_DUMP()

__global__ __launch_bounds__(512, 2) void hedgehog_fused(const u16* __restrict__ x, const u16* __restrict__ wqkv, const u16* __restrict__ wphi,
                                                         const u16* __restrict__ wo, u16* __restrict__ out, char* __restrict__ ws) {
  extern __shared__ __attribute__((aligned(16))) char smem[];
  const int tid = threadIdx.x, lane = tid & 63;
  const int w = __builtin_amdgcn_readfirstlane(tid >> 6), lw = w & 3;
  const int b = blockIdx.x, xcd = b & 7, idx = b >> 3;
  const int rl = idx & 7, cl = idx >> 3;
  const int rb = (xcd & 1) * 8 + rl, cb = (xcd >> 1) * 4 + cl;
  const int orb = (xcd >> 1) * 4 + (idx & 3), ocb = (xcd & 1) * 8 + (idx >> 2);
  const unsigned sb = (unsigned)(size_t)(LAS char*)smem;
  const int hh = lane >> 5, l31 = lane & 31, l15 = lane & 15, g4 = lane >> 4, q4 = l15 >> 2, p4 = lane & 3, r8 = lane >> 3, c8 = lane & 7;
  unsigned st_r = 0, st_c = 0, retry_ = 0; (void)st_r; (void)st_c; (void)retry_;
  const unsigned pw = lw & 1;
  unsigned sp = (b * 2 + (lw >> 1)) * (8192 - pw * 7424);
  unsigned vp = __umul24(l15, 2048 - pw * 1920) + __umul24(g4, 128 + pw * (64 * 6144 - 128));
  vp = g4 < 2 && l15 < 4 + 2 * pw ? vp : OOB;
  unsigned vx[4], vw[6];
#pragma unroll
  for (int i = 0; i < 4; i++) vx[i] = (rb * 128 + lw * 32 + r8) * 2048 + ((c8 ^ ((i & 1) * 4 + (r8 >> 1))) << 4) + i * (16384 - 1024);
#pragma unroll
  for (int i = 0; i < 6; i++) vw[i] = cb * 128 + (lw * 16 + r8) * 6144 + ((c8 ^ ((i & 1) * 4 + (r8 & 2))) << 4) + (i >> 1) * 2048 + (i & 1) * (8 * 6144 - 1024);
  asm volatile("" : "+v"(vp), "+v"(vx[0]), "+v"(vx[1]), "+v"(vx[2]), "+v"(vx[3]), "+v"(vw[0]), "+v"(vw[1]), "+v"(vw[2]), "+v"(vw[3]), "+v"(vw[4]), "+v"(vw[5]));
  asm volatile("" : "+s"(sp));
  asm volatile("" :: "s"(x), "s"(wqkv), "s"(wphi), "s"(wo), "s"(out), "s"(ws));
  const rsrc_t rws = __builtin_amdgcn_make_buffer_rsrc(ws, 0, W_BYTES, 0x00020000);
  const rsrc_t rx = __builtin_amdgcn_make_buffer_rsrc((void*)x, 0, 2048 * 1024 * 2, 0x00020000);
  const rsrc_t rw = __builtin_amdgcn_make_buffer_rsrc((void*)wqkv, 0, 1024 * 3072 * 2, 0x00020000);
  const rsrc_t rout = __builtin_amdgcn_make_buffer_rsrc(out, 0, 2048 * 1024 * 2, 0x00020000);

  auto stage = [&](unsigned ox, unsigned ow, unsigned so) {
    LAS char* dx = (LAS char*)smem + so + lw * 4096;
    LAS char* dw = (LAS char*)smem + so + 16384 + lw * 2048;
#define LX(i) __builtin_amdgcn_raw_ptr_buffer_load_lds(rx, dx, 16, vx[i], ox, i * 1024, 0)
#define LW(i) __builtin_amdgcn_raw_ptr_buffer_load_lds(rw, dw + (i >> 1) * 8192, 16, vw[i], ow, (i & 1) * 1024, 0)
    LW(2); LW(3); LX(0); LX(1); LX(2); LX(3); LW(4); LW(5); LW(0); LW(1);
  };
  const rsrc_t ro = __builtin_amdgcn_make_buffer_rsrc((void*)wo, 0, 1024 * 1024 * 2, 0x00020000);
  u32x4 wr_[32];
  const unsigned vo = r8 * 2048 + c8 * 16, so_ = lw * 256 * 2048 + ocb * 128;
  auto wo_load = [&](int i0, int i1) {
#pragma unroll
    for (int i = i0; i < i1; i++) wr_[i] = __builtin_amdgcn_raw_buffer_load_b128(ro, vo, so_ + i * 16384, 0);
    SB;
  };
  auto clear_o = [&]() {
#pragma unroll
    for (int i = 0; i < 2; i++)
      __builtin_amdgcn_raw_buffer_store_b128(u32x4{EMPTY, EMPTY, EMPTY, EMPTY}, rws, W_O + (((rb * 8 + lw * 2 + i) * 32 + hpos(cb * 2)) * 1024 + g4 * 256) * OS + l15 * 16, 0, SC1);
  };
  u64 pc; unsigned code_len;
  asm volatile("s_getpc_b64 %0\n.Lhedgehog_begin:\n\ts_mov_b32 %1, .Lhedgehog_end-.Lhedgehog_begin" : "=s"(pc), "=s"(code_len));
  if (w < 4) {
    __builtin_amdgcn_raw_ptr_buffer_load_lds(lw & 1 ? rw : rx, (LAS char*)smem + L_DUMMY, 4, vp, sp, 0, 0);
    stage(0, 0, 0);
    SB;
    STAMP(25);
    const int m6 = (idx * 43) >> 8;
    const int turn = cl == 3 ? rl >> 2 : 2 + (idx - 6 * m6 + xcd) % 6, u = (cl == 3 ? rl & 3 : m6) * 4 + lw, l3 = lane / 3;
    const unsigned px = lane < 16 ? lane * 2048 + ((xcd & 1) * 1024 + ((xcd >> 1) * 16 + u) * 16) * 2048 : OOB;
    unsigned pa_ = lane * 128 + l3 * (2048 - 384) + ((xcd & 1) * 96 + u * 6) * 2048, pb_ = lane * 2048 + (u * 12 - 18) * 2048 + 384;
    asm volatile("" : "+v"(pa_), "+v"(pb_));
    const unsigned pq = (lane < 18 ? pa_ : lane < 30 ? pb_ : OOB) + (xcd >> 1) * 512;
    const int uc = idx * 4 + lw;
    const char* sc = (const char*)(pc & ~127ull) + (uc * 256 < code_len ? uc : 0) * 256;
    unsigned o0 = cb * 16 + rb, o1 = rb * 16 + cb, o2 = W_H1 + lw * 65536;
    asm volatile("" : "+s"(o0), "+s"(o1), "+s"(o2));
    const char* s1 = (lw == 3 ? (const char*)wphi : ws) + (lw == 3 ? b * 256 : o2 + (lw == 0 ? o0 : o1) * 256);
    unsigned o3 = W_P + o0 * 8192, o4 = (lw == 1 ? rb : orb) * 262144 + cb * 2048, o5 = o4 * OS + 256;
    asm volatile("" : "+s"(o3), "+s"(o4), "+s"(o5));
    const char* s2 = (lw == 3 ? (const char*)out : ws) + (lw == 0 ? o3 : lw == 3 ? o4 : o5);
    auto pull = [&](int s) {
      __builtin_amdgcn_raw_ptr_buffer_load_lds(rx, (LAS char*)smem + L_DUMMY, 4, px, s * 128, 0, 0);
      __builtin_amdgcn_raw_ptr_buffer_load_lds(rw, (LAS char*)smem + L_DUMMY, 4, pq, s * 64 * 6144, 0, 0);
    };
#define TOUCH(p) asm volatile("s_load_dwordx2 s[100:101], %0, 0x3c\n\ts_load_dwordx2 s[100:101], %0, 0xbc" :: "s"(p) : "s100", "s101")
    TOUCH(sc); TOUCH(s1); TOUCH(s2); epoch_ask(ws);
    sc = (const char*)wo + (b * 4 + lw) * 2048 - 512;
    unsigned turns = (0x101u << turn) >> 2, tch = 0x3fc, ox = 128, ow = 64 * 6144, so = SLOT;
    asm volatile("" : "+s"(turns));
#pragma unroll 1
    for (int n = 1; n < 16; n++) {
      if (__builtin_expect(turns & 1, 0)) pull(n + 1);
      stage(ox, ow, so);
      ox += 128; ow += 64 * 6144; so = so == 2 * SLOT ? 0 : so + SLOT;
      asm volatile("s_bitcmp0_b32 %2, 0\n\ts_cbranch_scc1 1f\n\ts_load_dwordx2 s[100:101], %1, 0x3c\n\ts_load_dwordx2 s[100:101], %1, 0xbc\n1:\n\t"
                   "s_bitcmp1_b32 %0, 0\n\ts_waitcnt vmcnt(12)\n\ts_cbranch_scc1 2f\n\ts_waitcnt vmcnt(10)\n2:\n\ts_barrier"
                   :: "s"(turns), "s"(sc), "s"(tch), "s"(ox), "s"(ow), "s"(so) : "s100", "s101", "scc", "memory");
      turns >>= 1; tch >>= 1; sc += 256;
    }
    const rsrc_t rp = __builtin_amdgcn_make_buffer_rsrc((void*)wphi, 0, 32 * 1024 * 2, 0x00020000);
    __builtin_amdgcn_raw_ptr_buffer_load_lds(rp, (LAS char*)smem + L_PHI + lw * 1024, 16, lane * 16, cb * 4096 + lw * 1024, 0, 0);
    asm volatile("" ::: "memory"); SB;
    const u32x4 e4 = {EMPTY, EMPTY, EMPTY, EMPTY};
    JOLT(40);
#pragma unroll
    for (int i = 0; i < 2; i++) __builtin_amdgcn_raw_buffer_store_b128(e4, rws, W_P + (cb * 16 + rb) * 8192 + lw * 2048 + i * 1024 + lane * 16, 0, SC1);
    __builtin_amdgcn_raw_buffer_store_b128(e4, rws, (cb * 16 + rb) * 512 + lw * 128 + (lane < 8 ? W_PZ + c8 * 16 : lane < 16 ? W_R + c8 * 16 : OOB), 0, SC1);
    clear_o();
    SB;
    wo_load(0, WN0);
    waitv<5 + WN0>(); bar0();
  }
  unsigned T, t3 = 0, fa = OOB;
  const int rg = lw, kh = w >> 2;
  unsigned ga[4], v0, v1, xw, xr, og, tw, tr, fr;
  auto phase2_setup = [&]() {
    const int kr = 16 * (g4 >> 1) + 4 * (g4 & 1) + q4;
#pragma unroll
    for (int nt = 0; nt < 4; nt++) ga[nt] = sb + L_WO + kh * 65536 + kr * 128 + ((nt ^ ((kr >> 1) & 3)) << 5) + p4 * 8;
    v0 = l15 * 16 + (g4 * 256 + kh * 32768) * OS; v1 = l15 * 16 + (g4 * 256 + (kh ^ 1) * 32768) * OS;
    xw = sb + L_FX + w * 4096 + lane * 16; xr = sb + L_FX + (w ^ 4) * 4096 + lane * 16;
    og = (orb * 128 + rg * 32 + kh * 16 + r8) * 2048 + ocb * 128 + c8 * 16;
    tw = sb + L_WO + w * 2048 + l15 * 128 + (((g4 >> 1) ^ (l15 & 7)) << 4) + (((g4 & 1) ^ (l15 >> 3)) << 3);
    tr = sb + L_WO + w * 2048 + r8 * 128 + ((c8 ^ r8) << 4);
    fr = lane < 8 ? W_F + (orb * 16 + kh * 8 + lane) * 256 + (rg >> 1) * 8 : OOB;
    asm volatile("" : "+v"(ga[0]), "+v"(ga[1]), "+v"(ga[2]), "+v"(ga[3]), "+v"(v0), "+v"(v1), "+v"(xw), "+v"(xr), "+v"(og), "+v"(tw), "+v"(tr), "+v"(fr));
  };
  int role = w; asm volatile("" : "+s"(role));

  if (role < 4) {
    T = epoch_tag();
    if (tid == 0) *LP(u32x2, sb + L_CNT) = u32x2{0, T};
    unsigned tya, tx;
    STAMP(12); JOLT(12);
    wo_load(WN0, WNA);
    unsigned touch = __builtin_amdgcn_raw_buffer_load_b32(lw & 1 ? rout : rws, lane == 0 ? 0 : OOB, lw & 1 ? orb * 262144 + ocb * 128 : orb * 262144 * OS + 256, SC1);
    unsigned ha = lane < 16 && lane != rb ? W_H1 + (cb * 16 + lane) * 256 : OOB;
    u32x2 h1;
    {
      const int srow = 4 * g4 + q4;
      const int sw = ((srow >> 1) & 1) | ((srow >> 2) & 2) | (srow & 4);
      unsigned ia[2], va = sb + L_V + (lw >> 1) * 4096 + lane * 16;
#pragma unroll
      for (int ft = 0; ft < 2; ft++) ia[ft] = sb + (1 + (lw >> 1)) * SLOT + 24576 + (lw & 1) * 8192 + srow * 64 + (((4 * ft + p4) ^ sw) << 3);
      unsigned pa = W_P + (cb * 16 + rb) * 8192 + lw * 2048 + g4 * 512 + l15 * 16;
      unsigned za = l15 < 2 ? W_PZ + (cb * 16 + rb) * 512 + lw * 128 + (l15 * 4 + g4) * 16 : OOB;
      u32x4 one4 = {0x3f803f80u, 0x3f803f80u, 0x3f803f80u, 0x3f803f80u};
      f32x4 kv[2][2] = {}, z[2] = {};
      asm volatile("" : "+v"(ia[0]), "+v"(ia[1]), "+v"(va), "+v"(pa), "+v"(za), "+v"(one4), "+v"(ha));
      waitv<WNA - WN0 + 1>();
      __builtin_amdgcn_s_waitcnt(0x0f70 | ((WNA - WN0 + 1) & 15) | (((WNA - WN0 + 1) >> 4) << 14));
      STAMP(1); JOLT(1);
      bar();
      tya = now32();
      __builtin_amdgcn_raw_buffer_store_b64(u32x2{tya, T}, rws, tid == 0 ? W_H1 + (cb * 16 + rb) * 256 : OOB, 0, SC1);
      SB;
      wo_load(WNA, WN1);
      bf16x8 vf[2][2][2];
#pragma unroll
      for (int i = 0; i < 8; i++) vf[i >> 2][(i >> 1) & 1][i & 1] = lds_b128(va + (i >> 2) * 8192 + (i & 3) * 1024);
#pragma unroll
      for (int rt = 0; rt < 2; rt++) {
        if (rt) {
          asm volatile("" : "+v"(kv[0][0]), "+v"(kv[0][1]), "+v"(kv[1][0]), "+v"(kv[1][1]), "+v"(z[0]), "+v"(z[1]));
          __builtin_amdgcn_s_setprio(3);
          STAMP(13); JOLT(13);
          bar();
        }
        bf16x8 pf[2][2];
#pragma unroll
        for (int i = 0; i < 4; i++) { const unsigned a = ia[i & 1] + (i >> 1) * 4096 + rt * 2048; pf[i >> 1][i & 1] = lds_tr2(a, a + 1024); SB; }
        if (rt) {
          h1 = __builtin_amdgcn_raw_buffer_load_b64(rws, ha, 0, SC1);
          SB;
        }
#pragma unroll
        for (int h = 0; h < 2; h++)
#pragma unroll
          for (int et = 0; et < 2; et++)
#pragma unroll
            for (int ft = 0; ft < 2; ft++) { kv[ft][et] = MFMA16(pf[h][ft], vf[h][rt][et], kv[ft][et]); SB; }
#pragma unroll
        for (int h = 0; h < 2; h++) {
#pragma unroll
          for (int ft = 0; ft < 2; ft++) z[ft] = MFMA16(pf[h][ft], BF8(one4), z[ft]);
          SB;
          if (rt) __builtin_amdgcn_raw_buffer_store_b128(seal4(u32x4{pk2(kv[0][h][0], kv[0][h][1]), pk2(kv[0][h][2], kv[0][h][3]),
                                                                     pk2(kv[1][h][0], kv[1][h][1]), pk2(kv[1][h][2], kv[1][h][3])}), rws, pa + h * 256, 0, SC1);
          SB;
        }
      }
      u32x4 zs;
#pragma unroll
      for (int j = 0; j < 4; j++) zs[j] = seal(__float_as_uint((lane & 1) ? z[1][j] : z[0][j]));
      __builtin_amdgcn_raw_buffer_store_b128(zs, rws, za, 0, SC1);
      count_in(sb + L_CNT, 0x100);
      SB;
    }
    STAMP(6); JOLT(6);
    const unsigned da = sb + L_WO + lw * 32768 + r8 * 128 + ((c8 ^ (r8 & 6)) << 4);
    auto wo_image = [&](int i0, int i1) {
#pragma unroll
      for (int i = i0; i < i1; i++) *LP(u32x4, da + i * 1024) = wr_[i];
      SB;
    };
    const unsigned t1 = tya + max(latest(h1, rws, ha, tya, T), 0) + D1;
    {
      u32x4 g[2], gz[2];
      unsigned pa = W_P + (cb * 16 + r8) * 8192 + rb * 512 + lw * 128 + c8 * 16;
      unsigned za = W_PZ + (cb * 16 + r8) * 512 + lw * 128 + c8 * 16;
      unsigned ra = (lane & 8) ? OOB : W_R + cb * 8192 + rb * 512 + lw * 128 + c8 * 16 + g4 * 4;
      unsigned ka = W_R + cb * 8192 + lw * 2048 + lane * 16;
      asm volatile("" : "+v"(pa), "+v"(za), "+v"(ra), "+v"(ka));
      wait_until(t1);
      STAMP(16); JOLT(16);
#pragma unroll
      for (int i = 0; i < 2; i++) g[i] = __builtin_amdgcn_raw_buffer_load_b128(rws, pa, i * 65536, SC1);
#pragma unroll
      for (int i = 0; i < 2; i++) gz[i] = __builtin_amdgcn_raw_buffer_load_b128(rws, za, i * 4096, SC1);
      SB;
      wo_load(WN1, WN2);
      lds_wait(sb + L_CNT, 0x800, 0xff00);
      wo_image(0, WI1);
      RETRY(31) += settle<2>(g, rws, pa, 65536) ? 1 : 0;
      STAMP(7); JOLT(7);
      float s[8];
#pragma unroll
      for (int j = 0; j < 4; j++) {
        s[2 * j] = __uint_as_float(g[0][j] << 16) + __uint_as_float(g[1][j] << 16);
        s[2 * j + 1] = __uint_as_float(g[0][j] & 0xffff0000u) + __uint_as_float(g[1][j] & 0xffff0000u);
      }
      const float a0 = fold16(fold32(s[0], s[4]), fold32(s[2], s[6])), a1 = fold16(fold32(s[1], s[5]), fold32(s[3], s[7]));
      __builtin_amdgcn_raw_buffer_store_b32(seal(pk2(fold8(a0), fold8(a1))), rws, ra, 0, SC1);
      unsigned t2 = now32() + D2;
      if ((int)(t1 + D2B - t2) > 0) t2 = t1 + D2B;
      SB;
      STAMP(2); JOLT(2);
      settle<2>(gz, rws, za, 4096);
#pragma unroll
      for (int j = 0; j < 4; j++) s[j] = __uint_as_float(gz[0][j]) + __uint_as_float(gz[1][j]);
      float z0 = fold32(s[0], s[2]), z1 = fold32(s[1], s[3]);
      z0 = fold8(fold16(z0, z0)); z1 = fold8(fold16(z1, z1));
      if ((lane & 24) == 0) *LP(unsigned, sb + L_Z + lw * 64 + (c8 & 1) * 32 + ((c8 >> 1) & 1) * 16 + (c8 >> 2) * 8 + hh * 4) = pk2(z0, z1);
      count_in(sb + L_CNT, 0x10000);
      STAMP(3); JOLT(3);
      wait_until(t2);
      STAMP(18);
#pragma unroll
      for (int i = 0; i < 2; i++) g[i] = __builtin_amdgcn_raw_buffer_load_b128(rws, ka + i * 1024, 0, SC1);
      SB;
      wo_load(WN2, 32);
      wo_image(WI1, WI2);
      RETRY(31) += settle<2>(g, rws, ka, 1024) ? 10 : 0;
#pragma unroll
      for (int i = 0; i < 2; i++) *LP(u32x4, sb + L_KV + (lw * 2 + i) * 1024 + lane * 16) = g[i];
    }
    unsigned hs = tid == 0 ? W_H2 + (rb * 16 + cb) * 256 : OOB;
    ha = tid < 16 ? W_H2 + (orb * 16 + lane) * 256 : OOB;
    asm volatile("" : "+v"(hs), "+v"(ha));
    STAMP(4); JOLT(4);
    bar();
    tx = now32();
    __builtin_amdgcn_raw_buffer_store_b64(u32x2{tx, T}, rws, hs, 0, SC1);
    __builtin_amdgcn_s_setprio(0);
    phase2_setup();
    SB;
    wo_image(WI2, WI3);
    h1 = __builtin_amdgcn_raw_buffer_load_b64(rws, ha, 0, SC1);
    SB;
    wo_image(WI3, 32);
    asm volatile("" :: "v"(touch));
    STAMP(5); JOLT(5);
    asm volatile("" : "+v"(h1));
    if (w == 0) {
      t3 = tx + latest(h1, rws, ha, tx, T) + D3;
      if (b == 0) __builtin_amdgcn_raw_buffer_store_b32(T, rws, lane == 0 ? W_E : OOB, 0, SC1);
      STAMP(14); JOLT(14);
    }
  } else {
    const int wr = lw >> 1, wc = lw & 1;
    __builtin_amdgcn_s_waitcnt(0x0f70);
    f32x4 acc[6][4] = {};
    unsigned xa[2], wa[2];
#pragma unroll
    for (int ks = 0; ks < 2; ks++) xa[ks] = sb + (wr * 64 + l15) * 128 + (((4 * ks + g4) ^ (l15 >> 1)) << 4);
#pragma unroll
    for (int nt = 0; nt < 2; nt++) wa[nt] = sb + 16384 + (8 * g4 + q4) * 128 + (((wc * 2 + nt) ^ ((q4 >> 1) | ((g4 & 1) << 1))) << 5) + p4 * 8;
    bf16x8 fx[4][4], fw[4][6];
    unsigned xs[3][2], wsl[3][2];
#pragma unroll
    for (int sl = 0; sl < 3; sl++) {
      xs[sl][0] = xa[0] + sl * SLOT; xs[sl][1] = xa[1] + sl * SLOT; wsl[sl][0] = wa[0] + sl * SLOT; wsl[sl][1] = wa[1] + sl * SLOT;
      asm volatile("" : "+v"(xs[sl][0]), "+v"(xs[sl][1]), "+v"(wsl[sl][0]), "+v"(wsl[sl][1]));
    }
    auto rdw = [&](int buf, int j, int ks, int sl) {
      const unsigned a = wsl[sl][j & 1] + (j < 4 ? 8192 + (j >> 1) * 8192 : 0) + ks * 4096;
      fw[buf][j] = lds_tr2(a, a + 512);
    };
    auto rd = [&](int buf, int ks, int sl, int j0, int j1) {
      rdw(buf, j0, ks, sl);
#pragma unroll
      for (int m = 0; m < 4; m++) fx[buf][m] = lds_b128(xs[sl][ks] + m * 2048);
#pragma unroll
      for (int j = j0 + 1; j < j1; j++) rdw(buf, j, ks, sl);
    };
    auto mm = [&](int buf, int j0, int j1) {
#pragma unroll
      for (int j = j0; j < j1; j++)
#pragma unroll
        for (int m0 = 0; m0 < 4; m0++) {
          const int m = (j & 1) ? 3 - m0 : m0;
          acc[j][m] = (j == 2 || j == 3) ? MFMA16(fx[buf][m], fw[buf][j], acc[j][m]) : MFMA16(fw[buf][j], fx[buf][m], acc[j][m]);
        }
    };
#define SCHED(nds, nmf) do { _Pragma("unroll") for (int i_ = 0; i_ < (nds); i_++) { __builtin_amdgcn_sched_group_barrier(0x8, 1, 0); __builtin_amdgcn_sched_group_barrier(0x100, 1, 0); } \
      if ((nmf) > (nds)) __builtin_amdgcn_sched_group_barrier(0x8, (nmf) - (nds), 0); SB; } while (0)
#pragma unroll
    for (int j = 0; j < 6; j++) asm volatile("" : "+v"(acc[j][0]), "+v"(acc[j][1]), "+v"(acc[j][2]), "+v"(acc[j][3]));
    unsigned wpa = sb + L_PHI + wc * 2048 + (8 * hh + q4) * 64 + (g4 & 1) * 32 + p4 * 8, vwa = sb + L_V + lw * 4096 + lane * 16;
    unsigned pia[4];
    {
      const int sw = ((l31 >> 1) & 1) | ((l31 >> 2) & 2) | (l31 & 4);
#pragma unroll
      for (int q = 0; q < 4; q++) pia[q] = sb + (1 + wc) * SLOT + 24576 + (wr * 64 + l31) * 64 + (((2 * q + hh) ^ sw) << 3);
      asm volatile("" : "+v"(pia[0]), "+v"(pia[1]), "+v"(pia[2]), "+v"(pia[3]), "+v"(wpa), "+v"(vwa));
    }
    bar();
    STAMP(1); JOLT(1);
    rd(0, 0, 0, 0, 6);
    SB;
#pragma unroll 1
    for (int t = 0; t < 12; t += 3) {
#pragma unroll
      for (int u = 0; u < 3; u++) {
        waitl(); SB;
        rd(1, 1, u, 0, 6); mm(0, 0, 6); SCHED(16, 24);
        bar();
        rd(0, 0, (u + 1) % 3, 0, 6); mm(1, 0, 6); SCHED(16, 24);
      }
    }
    rd(1, 1, 0, 0, 6); mm(0, 0, 6); SCHED(16, 24);
    bar();
    rd(0, 0, 1, 0, 4); mm(1, 0, 6); SCHED(12, 24);
    rd(1, 1, 1, 0, 4); mm(0, 0, 4); SCHED(12, 16);
    bar();
    rd(0, 0, 2, 0, 4); mm(1, 0, 4); SCHED(12, 16);
    rd(1, 1, 2, 0, 4); mm(0, 0, 4); SCHED(12, 16);
    bar();
    rd(0, 0, 0, 0, 4); mm(1, 0, 4); SCHED(12, 16);
    bf16x8 wp[2];
    rd(1, 1, 0, 0, 4);
    wp[0] = lds_tr2(wpa, wpa + 256); wp[1] = lds_tr2(wpa + 1024, wpa + 1280);
    mm(0, 0, 4); SCHED(16, 16);
    STAMP(2); JOLT(2);
    const f32x16 zero = {};
    auto project = [&](int j0, f32x16 (&p)[2], auto fill0, auto fill1) {
#pragma unroll
      for (int i = 0; i < 8; i++) { const int j = j0 + ((i >> 1) & 1), m = (i >> 2) * 2 + (i & 1); acc[j][m] = MFMA16(fw[1][j], fx[1][m], acc[j][m]); SB; }
      fill0();
      const bf16x8 a0 = pack16to32(acc[j0][0], acc[j0][1]), a1 = pack16to32(acc[j0 + 1][0], acc[j0 + 1][1]);
#pragma unroll
      for (int i = 0; i < 8; i++) { __builtin_amdgcn_sched_group_barrier(0x8, 1, 0); __builtin_amdgcn_sched_group_barrier(0x2, 2, 0); }
      SB;
      p[0] = MFMA32(wp[0], a0, zero); SB;
      const bf16x8 b0 = pack16to32(acc[j0][2], acc[j0][3]); SB;
      p[0] = MFMA32(wp[1], a1, p[0]); SB;
      const bf16x8 b1 = pack16to32(acc[j0 + 1][2], acc[j0 + 1][3]); SB;
      p[1] = MFMA32(wp[0], b0, zero); SB;
      fill1(0); SB;
      p[1] = MFMA32(wp[1], b1, p[1]); SB;
      fill1(1); SB;
    };
    f32x16 pk[2];
    project(0, pk, [&]() { mm(1, 2, 4); }, [&](int rt) {
#pragma unroll
      for (int et = 0; et < 2; et++) {
        const f32x4 a = acc[2 + et][2 * rt], c = acc[2 + et][2 * rt + 1];
        *LP(u32x4, vwa + (rt * 2 + et) * 1024) = u32x4{pk2(a[0], a[1]), pk2(a[2], a[3]), pk2(c[0], c[1]), pk2(c[2], c[3])};
      }
    });
    STAMP(20);
#pragma unroll
    for (int rt = 0; rt < 2; rt++) {
      unsigned ph[2][8];
      dual_softmax<false>(pk[rt], ph[0], ph[1],
        [&](float& mx, float& mn) { if (rt) { asm volatile("" : "+v"(mx), "+v"(mn)); STAMP(21); bar(); STAMP(22);
          rdw(0, 4, 0, 0); rdw(0, 5, 0, 0); rdw(1, 4, 1, 0); rdw(1, 5, 1, 0); rd(2, 0, 1, 4, 6); SB;
          asm volatile("" : "+v"(mx), "+v"(mn)); } },
        [&](int q) {
          *LP(u32x2, pia[q] + rt * 2048) = u32x2{ph[0][2 * q], ph[0][2 * q + 1]};
          *LP(u32x2, pia[q] + rt * 2048 + 8192) = u32x2{ph[1][2 * q], ph[1][2 * q + 1]};
          SB;
        });
    }
    STAMP(3); JOLT(3);
    bar();
    STAMP(4); JOLT(4);
    SB;
    rd(3, 1, 1, 4, 6); mm(0, 4, 6); SCHED(8, 8);
    rd(0, 0, 2, 4, 6); mm(1, 4, 6); SCHED(8, 8);
    rd(1, 1, 2, 4, 6); mm(2, 4, 6); SCHED(8, 8);
    count_in(sb + L_CNT, 0x100);
    mm(3, 4, 6); SB;
    mm(0, 4, 6); SB;
    STAMP(23);
    project(4, pk, []() {}, [](int) {});
    STAMP(24);
    u32x4 phq[2][4], zf[4];
    u32x2 zc;
    float eps[2];
    unsigned ka = sb + L_KV + wc * 4096 + lane * 16, zl = sb + L_Z + wc * 128 + hh * 32;
    auto zread = [&]() {
      zc = *LP(const volatile u32x2, sb + L_CNT);
#pragma unroll
      for (int i = 0; i < 4; i++) zf[i] = *LP(const volatile u32x4, zl + (i >> 1) * 64 + (i & 1) * 16);
    };
#pragma unroll
    for (int rt = 0; rt < 2; rt++) {
      unsigned ph[2][8];
      eps[rt] = 1e-6f * dual_softmax<true>(pk[rt], ph[0], ph[1], [](float&, float&) {}, [&](int q) { if (rt && q == 0) { SB; zread(); SB; } });
#pragma unroll
      for (int i = 0; i < 4; i++) { const int sg = i >> 1, st = i & 1; phq[rt][i] = u32x4{ph[sg][2 * st], ph[sg][2 * st + 1], ph[sg][4 + 2 * st], ph[sg][5 + 2 * st]}; }
      asm volatile("" : "+v"(phq[rt][0]), "+v"(phq[rt][1]), "+v"(phq[rt][2]), "+v"(phq[rt][3]));
    }
    unsigned oa = W_O + (((rb * 8 + wr * 4 + (g4 & 1)) * 32 + hpos(cb * 2 + wc)) * 1024 + hh * 256) * OS + l15 * 16, oa1 = oa + 65536 * OS;
    fa = lane == 0 ? W_F + (rb * 16 + cb) * 256 + lw * 4 : OOB;
    asm volatile("" : "+v"(ka), "+v"(oa), "+v"(oa1), "+v"(fa));
    STAMP(5); JOLT(5);
    if (__builtin_amdgcn_readfirstlane(zc[0]) < 0x40000) { lds_wait(sb + L_CNT, 0x40000); zread(); }
    T = __builtin_amdgcn_readfirstlane(zc[1]);
    float rden[2];
    {
      float dn[2][2] = {};
#pragma unroll
      for (int i = 0; i < 4; i++)
#pragma unroll
        for (int j = 0; j < 4; j++)
#pragma unroll
          for (int rt = 0; rt < 2; rt++) dn[rt][j & 1] = dot2(phq[rt][i][j], zf[i][j], dn[rt][j & 1]);
#pragma unroll
      for (int rt = 0; rt < 2; rt++) { const float d = dn[rt][0] + dn[rt][1]; rden[rt] = __builtin_amdgcn_rcpf(fold32(d, d) + eps[rt]); }
      asm volatile("" : "+v"(rden[0]), "+v"(rden[1]));
    }
    STAMP(16); JOLT(16);
    __builtin_amdgcn_s_setprio(2);
    bar();
    STAMP(6); JOLT(6);
    {
      bf16x8 kf[4];
#pragma unroll
      for (int i = 0; i < 4; i++) kf[i] = lds_b128(ka + i * 1024);
      auto put = [&](const f32x16& n, float r, int st, unsigned a) {
        const f32x2 r2 = {r, r};
        u32x4 v;
#pragma unroll
        for (int j = 0; j < 4; j++) v[j] = pk2v(f32x2{n[8 * st + 2 * j], n[8 * st + 2 * j + 1]} * r2);
        __builtin_amdgcn_raw_buffer_store_b128(v, rws, a + st * 512 * OS, 0, SC1);
      };
      SB;
      f32x16 n0 = zero, n1 = zero;
#pragma unroll
      for (int i = 0; i < 4; i++) { n0 = MFMA32(kf[i], BF8(phq[0][i]), n0); SB; }
#pragma unroll
      for (int i = 0; i < 3; i++) { n1 = MFMA32(kf[i], BF8(phq[1][i]), n1); SB; }
      put(n0, rden[0], 0, oa); SB;
      n1 = MFMA32(kf[3], BF8(phq[1][3]), n1); SB;
      put(n0, rden[0], 1, oa); SB;
      put(n1, rden[1], 0, oa1); SB; put(n1, rden[1], 1, oa1); SB;
    }
    STAMP(7); JOLT(7);
    phase2_setup();
    waitv<0>();
    __builtin_amdgcn_raw_buffer_store_b32(T, rws, fa, 0, SC1);
    __builtin_amdgcn_s_setprio(1);
  }
  {
    const unsigned ob = W_O + ((orb * 8 + rg * 2) * 32 + kh * 16) * 1024 * OS;
    constexpr int LAG = 2, PF = 2, RN = PF + 1;
    f32x4 c0[4], c1[4], px[4];
    const f32x4 z4 = {};
    u32x4 o0[RN], o1[RN];
    bf16x8 a[LAG + 2][4];
    auto head = [&](int i) { return 2 * (i & 7) + (i >> 3); };
    auto ldo = [&](int i) {
      if (i < 16) o1[i % RN] = __builtin_amdgcn_raw_buffer_load_b128(rws, v1, ob + hpos(head(i)) * 1024 * OS, SC1);
      if (i >= LAG && i < 16 + LAG) o0[i % RN] = __builtin_amdgcn_raw_buffer_load_b128(rws, v0, ob + hpos(head(i - LAG)) * 1024 * OS, SC1);
    };
    auto ldw = [&](int i) {
#pragma unroll
      for (int nt = 0; nt < 4; nt++) { const unsigned p = ga[nt] + head(i) * 4096; a[i % (LAG + 2)][nt] = lds_tr2(p, p + 1024); }
    };
    auto step = [&](int i) {
      if (i) ldo(i + PF);
      if (i == 8) __builtin_amdgcn_s_setprio(0);
      SB;
      if (i < 15) ldw(i + 1);
      if (i < 16) {
#pragma unroll
        for (int nt = 0; nt < 4; nt++) c1[nt] = MFMA16(a[i % (LAG + 2)][nt], BF8(o1[i % RN]), i ? c1[nt] : z4);
      }
      if (i >= LAG) {
#pragma unroll
        for (int nt = 0; nt < 4; nt++) c0[nt] = MFMA16(a[(i - LAG) % (LAG + 2)][nt], BF8(o0[i % RN]), i > LAG ? c0[nt] : z4);
      }
#pragma unroll
      for (int q = 0; q < 8; q++) { __builtin_amdgcn_sched_group_barrier(0x8, 1, 0); __builtin_amdgcn_sched_group_barrier(0x100, 1, 0); }
      SB;
    };
    if (w == 0) wait_until(t3);
    bar();
    STAMP(9); JOLT(9);
#pragma unroll 1
    for (int pass = 0;; pass++) {
#pragma unroll
      for (int i = 0; i <= PF; i++) { ldo(i); SB; }
      ldw(0);
      SB;
      u32x2 fl;
#pragma unroll
      for (int i = 0; i < 9; i++) {
        if (i == 3) { fl = __builtin_amdgcn_raw_buffer_load_b64(rws, fr, 0, SC1); SB; }
        if (i == 8 - PF) {
          STAMP(15);
          if (__builtin_expect(!__all(fr == OOB || (fl[0] == T && fl[1] == T)), 0)) {
            RETRY(31) = 100 + RETRY(31) % 100;
#pragma unroll 1
            for (int sp = 0; sp < SPIN_CAP; sp++) {
              if (fl[0] == T && fl[1] == T) fr = OOB;
              if (__all(fr == OOB)) break;
              __builtin_amdgcn_s_sleep(6);
              asm volatile("" ::: "memory");
              fl = __builtin_amdgcn_raw_buffer_load_b64(rws, fr, 0, SC1);
            }
          }
          asm volatile("" ::: "memory");
        }
        step(i);
      }
      if (__builtin_expect(pass || !__any(c0[0][0] != c0[0][0] || c1[0][0] != c1[0][0]), 1)) break;
      if (w >= 4) RETRY(31) |= 1;
      asm volatile("" ::: "memory");
    }
#pragma unroll
    for (int i = 9; i < 16 + LAG; i++) {
      step(i);
      if (i == 15) {
        STAMP(10);
#pragma unroll
        for (int nt = 0; nt < 4; nt++) *LP(f32x4, xw + nt * 1024) = c1[nt];
      }
      if (i == (LAG ? 14 + LAG : 15)) {
        bar();
#pragma unroll
        for (int nt = 0; nt < 4; nt++) px[nt] = *LP(const f32x4, xr + nt * 1024);
        SB;
      }
    }
#pragma unroll
    for (int nt = 0; nt < 4; nt++) {
      const f32x4 v = px[nt] + c0[nt];
      *LP(u32x2, tw ^ (nt << 5)) = u32x2{pk2(v[0], v[1]), pk2(v[2], v[3])};
    }
    asm volatile("" ::: "memory");
    const u32x4 t0 = *LP(const u32x4, tr), t1 = *LP(const u32x4, tr + 1024);
    __builtin_amdgcn_raw_buffer_store_b128(t0, rout, og, 0, SC1);
    __builtin_amdgcn_raw_buffer_store_b128(u32x4{t1[2], t1[3], t1[0], t1[1]}, rout, og + 16384, 0, SC1);
    asm volatile(".Lhedgehog_end:");
    STAMP(11);
    STAMP(30);
    STAMP_DUMP();
  }
}

extern "C" void kernel_launch(void* const* d_in, const int* in_sizes, int n_in, void* d_out, int out_size, void* d_ws, size_t ws_size,
                              hipStream_t stream) {
  const size_t ws_off = WSO < 0 ? (ws_size - (size_t)(-WSO) * (1u << 20)) & ~(size_t)((2u << 20) - 1) : (size_t)WSO << 20;
  (void)hipFuncSetAttribute((const void*)hedgehog_fused, hipFuncAttributeMaxDynamicSharedMemorySize, LDS_BYTES);
  hedgehog_fused<<<256, 512, LDS_BYTES, stream>>>((const u16*)d_in[0], (const u16*)d_in[1], (const u16*)d_in[2], (const u16*)d_in[3],
                                                   (u16*)d_out, (char*)d_ws + ws_off);
}
```

```cpp
#include <hip/hip_runtime.h>
#include <hip/hip_bf16.h>

typedef unsigned short u16;
typedef unsigned long long u64;
typedef __attribute__((ext_vector_type(8))) __bf16 bf16x8;
typedef __attribute__((ext_vector_type(4))) __bf16 bf16x4;
typedef __attribute__((ext_vector_type(2))) __bf16 bf16x2;
typedef __attribute__((ext_vector_type(16))) float f32x16;
typedef __attribute__((ext_vector_type(4))) float f32x4;
typedef __attribute__((ext_vector_type(2))) float f32x2;
typedef __attribute__((ext_vector_type(4))) unsigned u32x4;
typedef __attribute__((ext_vector_type(2))) unsigned u32x2;
typedef __amdgpu_buffer_rsrc_t rsrc_t;
#define LAS __attribute__((address_space(3)))
#define LP(T, a) ((LAS T*)(size_t)(a))
#define DI __device__ __forceinline__
#define MFMA32(a, b, c) __builtin_amdgcn_mfma_f32_32x32x16_bf16((a), (b), (c), 0, 0, 0)
#define MFMA16(a, b, c) __builtin_amdgcn_mfma_f32_16x16x32_bf16((a), (b), (c), 0, 0, 0)
#define SB __builtin_amdgcn_sched_barrier(0)
#define BF8(v) __builtin_bit_cast(bf16x8, v)
constexpr int SC1 = 16;
constexpr unsigned OOB = 0x80000000u;

#define VPF 3
#define IPN 2
#define WPT 9
#define D1 118
#define D2 30
#define D3 65
#define PSL 2
#define HSL 8
#define OLAG 2
#define OPF 2
#define WSO -12
#define CL0 1
#define SLW 3
#define WN0 3
#define WN1 23
#define WN2 27
#define CLO 0
#define PULL 0
#define WSM 1
#define XFIRST 0
#define OS 2
#define WNA 17
#define WI3 18
#define HY 1
#define D2B 85
#define IP0 0
#define UNR 3
#define QD 0
#define TLBT 1
#define DXA 45
#define DXB 245
#define D4 110
#define FSTEP 3
#define GSL 6
#define CPR 0
#define HP2 3
#define WSL 0
#define WROT 0
#define WAUX 0
#define WN3 32
#define NB 0
#define NOWO 0
#define WE 1
#define FC 0
#define D5 25
#define HP 1
#define HPOS(h) (HP ? ((h) & 16) + ((h) & 1) * 8 + (((h) >> 1) & 7) : (h))
#define OPF2 OPF
#define GP 8
#define PM 0
#define TB 1
#define C3 0
#define OAUX 16
#define WL 1
#define BC 0
#define WI1 4
#define WI2 6

constexpr int SLOT = 40960;
constexpr int L_WO = 0;
constexpr int L_V = 122880;
constexpr int L_PHI = 147456;
constexpr int L_Z = 151552;
constexpr int L_CNT = 151808;
constexpr int L_YC = 151816;
constexpr int L_KV = 139264;
constexpr int L_DUMMY = 152064;
constexpr int L_FX = 131072;
constexpr int LDS_BYTES = 163840;
constexpr unsigned W_O = 0;
constexpr unsigned W_P = 8u << 20;
constexpr unsigned W_PZ = 10u << 20;
constexpr unsigned W_R = W_PZ + (128u << 10);
constexpr unsigned W_H1 = W_R + (128u << 10);
constexpr unsigned W_H2 = W_H1 + (64u << 10);
constexpr unsigned W_F = W_H2 + (64u << 10);
constexpr unsigned W_H3 = W_H2 + 128;
constexpr unsigned W_E = W_F + (64u << 10);
constexpr unsigned W_TS = 9u << 20;
constexpr unsigned W_BYTES = 12u << 20;
constexpr int SPIN_CAP = 1 << 18;

DI void waitl() { __builtin_amdgcn_s_waitcnt(0xc07f); }
DI void bar() { asm volatile("" ::: "memory"); waitl(); asm volatile("s_barrier" ::: "memory"); }
DI void bar0() { asm volatile("s_barrier" ::: "memory"); }
template <int N> DI void waitv() { asm volatile("s_waitcnt vmcnt(%0)" ::"i"(N) : "memory"); }
DI unsigned pk2v(f32x2 v) { return __builtin_bit_cast(unsigned, __builtin_convertvector(v, bf16x2)); }
DI unsigned pk2(float a, float b) { return pk2v(f32x2{a, b}); }
DI bf16x8 pack16to32(const f32x4& c0, const f32x4& c1) {
  auto a = __builtin_amdgcn_permlane16_swap(pk2(c0[0], c0[1]), pk2(c1[0], c1[1]), false, false);
  auto b = __builtin_amdgcn_permlane16_swap(pk2(c0[2], c0[3]), pk2(c1[2], c1[3]), false, false);
  return BF8((u32x4{a[0], b[0], a[1], b[1]}));
}
DI float fold32(float a, float b) { auto r = __builtin_amdgcn_permlane32_swap(__float_as_uint(a), __float_as_uint(b), false, false); return __uint_as_float(r[0]) + __uint_as_float(r[1]); }
DI float fold16(float a, float b) { auto r = __builtin_amdgcn_permlane16_swap(__float_as_uint(a), __float_as_uint(b), false, false); return __uint_as_float(r[0]) + __uint_as_float(r[1]); }
DI float fold8(float a) { return a + __uint_as_float(__builtin_amdgcn_update_dpp(0u, __float_as_uint(a), 0x128, 0xf, 0xf, false)); }
DI float dot2(unsigned a, unsigned b, float c) { return __builtin_amdgcn_fdot2_f32_bf16(__builtin_bit_cast(bf16x2, a), __builtin_bit_cast(bf16x2, b), c, false); }
DI bf16x8 lds_b128(unsigned a) { return *LP(const bf16x8, a); }
DI bf16x8 lds_tr2(unsigned a0, unsigned a1) {
  bf16x4 lo = __builtin_amdgcn_ds_read_tr16_b64_v4bf16(LP(bf16x4, a0));
  bf16x4 hi = __builtin_amdgcn_ds_read_tr16_b64_v4bf16(LP(bf16x4, a1));
  return __builtin_shufflevector(lo, hi, 0, 1, 2, 3, 4, 5, 6, 7);
}
template <bool SCALED, class M, class P> DI float dual_softmax(const f32x16& p, unsigned (&pos)[8], unsigned (&neg)[8], M mid, P put) {
#define FMX __builtin_elementwise_maximum
#define FMN __builtin_elementwise_minimum
  float mx = FMX(FMX(p[0], p[1]), p[2]), mn = FMN(FMN(p[0], p[1]), p[2]), mx2 = FMX(FMX(p[3], p[4]), p[5]), mn2 = FMN(FMN(p[3], p[4]), p[5]);
#pragma unroll
  for (int i = 6; i < 14; i += 4) {
    mx = FMX(FMX(mx, p[i]), p[i + 1]); mn = FMN(FMN(mn, p[i]), p[i + 1]);
    mx2 = FMX(FMX(mx2, p[i + 2]), p[i + 3]); mn2 = FMN(FMN(mn2, p[i + 2]), p[i + 3]);
  }
  mx = FMX(FMX(mx, p[14]), p[15]); mn = FMN(FMN(mn, p[14]), p[15]);
  mx = FMX(mx, mx2); mn = FMN(mn, mn2);
  {
    auto r = __builtin_amdgcn_permlane32_swap(__float_as_uint(mx), __float_as_uint(mx), false, false);
    auto s = __builtin_amdgcn_permlane32_swap(__float_as_uint(mn), __float_as_uint(mn), false, false);
    mx = FMX(__uint_as_float(r[0]), __uint_as_float(r[1])); mn = FMN(__uint_as_float(s[0]), __uint_as_float(s[1]));
  }
  mid(mx, mn);
  const float c = 1.44269504088896341f;
  const f32x2 cp = {c, c}, cn = {-c, -c}, bx = {-mx * c, -mx * c}, bn = {mn * c, mn * c};
  f32x2 ep[8], en[8];
#pragma unroll
  for (int i = 0; i < 8; i++) {
    const f32x2 v = {p[2 * i], p[2 * i + 1]};
    const f32x2 a = __builtin_elementwise_fma(v, cp, bx), b = __builtin_elementwise_fma(v, cn, bn);
    ep[i] = f32x2{__builtin_amdgcn_exp2f(a[0]), __builtin_amdgcn_exp2f(a[1])};
    en[i] = f32x2{__builtin_amdgcn_exp2f(b[0]), __builtin_amdgcn_exp2f(b[1])};
  }
  const f32x2 s2p = ((ep[0] + ep[1]) + (ep[2] + ep[3])) + ((ep[4] + ep[5]) + (ep[6] + ep[7]));
  const f32x2 s2n = ((en[0] + en[1]) + (en[2] + en[3])) + ((en[4] + en[5]) + (en[6] + en[7]));
  const float sp = s2p[0] + s2p[1], sn = s2n[0] + s2n[1];
  const float tp = fold32(sp, sp);
  const float rp = SCALED ? 1.f : __builtin_amdgcn_rcpf(tp), rn = __builtin_amdgcn_rcpf(fold32(sn, sn)) * (SCALED ? tp : 1.f);
  const f32x2 r2p = {rp, rp}, r2n = {rn, rn};
#pragma unroll
  for (int i = 0; i < 8; i++) { pos[i] = pk2v(SCALED ? ep[i] : ep[i] * r2p); neg[i] = pk2v(en[i] * r2n); if (i & 1) put(i >> 1); }
  return tp;
}

constexpr unsigned EMPTY = 0xffffffffu;
DI unsigned seal(unsigned v) { return min(v, EMPTY - 1u); }
DI u32x4 seal4(u32x4 v) { return u32x4{seal(v[0]), seal(v[1]), seal(v[2]), seal(v[3])}; }
DI unsigned umax4(unsigned m, u32x4 v) { return max(max(max(max(m, v[0]), v[1]), v[2]), v[3]); }
template <int N> DI int settle(u32x4 (&g)[N], rsrc_t r, unsigned a, unsigned stride) {
  auto pending = [&]() { unsigned m = 0;
#pragma unroll
    for (int i = 0; i < N; i++) m = umax4(m, g[i]);
    return __any(m == EMPTY); };
  if (__builtin_expect(!pending(), 1)) return 0;
  int sp = 1;
#pragma unroll 1
  for (; sp < SPIN_CAP; sp++) {
    __builtin_amdgcn_s_sleep(PSL);
    asm volatile("" ::: "memory");
#pragma unroll
    for (int i = 0; i < N; i++) g[i] = __builtin_amdgcn_raw_buffer_load_b128(r, a + i * stride, 0, SC1);
    if (!pending()) break;
  }
  return sp;
}
DI unsigned now32() { u64 t; asm volatile("s_memrealtime %0\n\ts_waitcnt lgkmcnt(0)" : "=s"(t) :: "memory"); return (unsigned)t; }
DI void wait_until(unsigned t) {
  unsigned n = __builtin_amdgcn_readfirstlane(1000);
#pragma unroll 1
  while (t - now32() - 1u < 1000u && --n) { if (WSL) __builtin_amdgcn_s_sleep(WSL); }
}
DI int latest(u32x2 h, rsrc_t r, unsigned a, unsigned t0, unsigned T) {
  int d = -0x10000;
  if (a != OOB && h[1] == T) { d = (int)(h[0] - t0); a = OOB; }
  if (!__all(a == OOB)) {
#pragma unroll 1
    for (int sp = 0; sp < SPIN_CAP; sp++) {
      asm volatile("" ::: "memory");
      h = __builtin_amdgcn_raw_buffer_load_b64(r, a, 0, SC1);
      if (a != OOB && h[1] == T) { d = (int)(h[0] - t0); a = OOB; }
      if (__all(a == OOB)) break;
      __builtin_amdgcn_s_sleep(PSL);
    }
  }
  d = max(d, __builtin_amdgcn_update_dpp(d, d, 0x128, 0xf, 0xf, false));
  d = max(d, __builtin_amdgcn_update_dpp(d, d, 0x124, 0xf, 0xf, false));
  d = max(d, __builtin_amdgcn_update_dpp(d, d, 0x122, 0xf, 0xf, false));
  d = max(d, __builtin_amdgcn_update_dpp(d, d, 0x121, 0xf, 0xf, false));
  asm volatile("" ::: "memory");
  return __builtin_amdgcn_readfirstlane(d);
}
DI void count_in(unsigned lds_addr, unsigned n = 1) {
  asm volatile("s_mov_b64 exec, 1\n\tds_add_u32 %0, %1\n\ts_mov_b64 exec, -1" :: "v"(lds_addr), "v"(n) : "memory");
}
template <int SL = 1> DI void lds_wait(unsigned lds_addr, unsigned n, unsigned mask = ~0u) {
#pragma unroll 1
  for (int sp = 0; sp < SPIN_CAP * 8; sp++) { if ((__builtin_amdgcn_readfirstlane(*LP(const volatile unsigned, lds_addr)) & mask) >= n) break; __builtin_amdgcn_s_sleep(SL); }
  asm volatile("" ::: "memory");
}
DI void epoch_ask(const char* ws) { asm volatile("s_load_dword s99, %0, 0x0" :: "s"(ws + W_E) : "s99"); }
DI unsigned epoch_tag() { unsigned e; asm volatile("s_waitcnt lgkmcnt(0)\n\ts_add_u32 %0, s99, 1" : "=s"(e) :: "scc", "memory"); return e; }

#define STAMP(i)
#define STAMPV(i)
#define RETRY(i) retry_
#define STAMP_DUMP()

__global__ __launch_bounds__(512, 2) void hedgehog_fused(const u16* __restrict__ x, const u16* __restrict__ wqkv, const u16* __restrict__ wphi,
                                                         const u16* __restrict__ wo, u16* __restrict__ out, char* __restrict__ ws) {
  extern __shared__ __attribute__((aligned(16))) char smem[];
  const int tid = threadIdx.x, lane = tid & 63;
  const int w = __builtin_amdgcn_readfirstlane(tid >> 6), lw = w & 3;
  const int b = blockIdx.x, xcd = b & 7, idx = b >> 3;
  const int rl = idx & 7, cl = idx >> 3;
  const int rb = (xcd & 1) * 8 + rl, cb = (xcd >> 1) * 4 + cl;
  const int orb = (xcd >> 1) * 4 + (idx & 3), ocb = (xcd & 1) * 8 + (idx >> 2);
  const unsigned sb = (unsigned)(size_t)(LAS char*)smem;
  const int hh = lane >> 5, l31 = lane & 31, l15 = lane & 15, g4 = lane >> 4, q4 = l15 >> 2, p4 = lane & 3, r8 = lane >> 3, c8 = lane & 7;
  unsigned st_r = 0, st_c = 0, retry_ = 0; (void)st_r; (void)st_c; (void)retry_;
  const unsigned pw = lw & 1;
  unsigned sp = (b * 2 + (lw >> 1)) * (8192 - pw * 7424);
  unsigned vp = __umul24(l15, 2048 - pw * 1920) + __umul24(g4, 128 + pw * (64 * 6144 - 128));
  vp = g4 < IPN && g4 >= IP0 && l15 < 4 + 2 * pw ? vp : OOB;
  if (C3) vp = pw && g4 == 3 && l15 < 6 ? ((idx * 2 + (lw >> 1)) * 6 + l15) * 2048 + (xcd >> 1) * 512 + 384 : vp + sp;
  if (C3) sp = 0;
  unsigned vx[4], vw[6];
#pragma unroll
  for (int i = 0; i < 4; i++) vx[i] = (rb * 128 + lw * 32 + r8) * 2048 + ((c8 ^ ((i & 1) * 4 + (r8 >> 1))) << 4) + i * (16384 - 1024);
#pragma unroll
  for (int i = 0; i < 6; i++) vw[i] = cb * 128 + (lw * 16 + r8) * 6144 + ((c8 ^ ((i & 1) * 4 + (r8 & 2))) << 4) + (i >> 1) * 2048 + (i & 1) * (8 * 6144 - 1024);
  asm volatile("" : "+v"(vp), "+v"(vx[0]), "+v"(vx[1]), "+v"(vx[2]), "+v"(vx[3]), "+v"(vw[0]), "+v"(vw[1]), "+v"(vw[2]), "+v"(vw[3]), "+v"(vw[4]), "+v"(vw[5]));
  asm volatile("" : "+s"(sp));
  asm volatile("" :: "s"(x), "s"(wqkv), "s"(wphi), "s"(wo), "s"(out), "s"(ws));
  const rsrc_t rws = __builtin_amdgcn_make_buffer_rsrc(ws, 0, W_BYTES, 0x00020000);
  const rsrc_t rx = __builtin_amdgcn_make_buffer_rsrc((void*)x, 0, 2048 * 1024 * 2, 0x00020000);
  const rsrc_t rw = __builtin_amdgcn_make_buffer_rsrc((void*)wqkv, 0, 1024 * 3072 * 2, 0x00020000);
  const rsrc_t rout = __builtin_amdgcn_make_buffer_rsrc(out, 0, 2048 * 1024 * 2, 0x00020000);

  auto stage = [&](unsigned ox, unsigned ow, unsigned so) {
    LAS char* dx = (LAS char*)smem + so + lw * 4096;
    LAS char* dw = (LAS char*)smem + so + 16384 + lw * 2048;
#define LX(i) __builtin_amdgcn_raw_ptr_buffer_load_lds(rx, dx, 16, vx[i], ox, i * 1024, 0)
#define LW(i) __builtin_amdgcn_raw_ptr_buffer_load_lds(rw, dw + (i >> 1) * 8192, 16, vw[i], ow, (i & 1) * 1024, 0)
    LW(2); LW(3); LX(0); LX(1); LX(2); LX(3); LW(4); LW(5); LW(0); LW(1);
  };
  const rsrc_t ro = __builtin_amdgcn_make_buffer_rsrc((void*)wo, 0, NOWO ? 0 : 1024 * 1024 * 2, 0x00020000);
  u32x4 wr_[32];
  const unsigned vo = r8 * 2048 + c8 * 16, so_ = lw * 256 * 2048 + ocb * 128;
  auto wo_load = [&](int i0, int i1) {
#pragma unroll
    for (int i = i0; i < i1; i++) wr_[i] = __builtin_amdgcn_raw_buffer_load_b128(ro, vo, so_ + i * 16384, 0);
    SB;
  };
  u64 pc; unsigned code_len;
  asm volatile("s_getpc_b64 %0\n.Lhedgehog_begin:\n\ts_mov_b32 %1, .Lhedgehog_end-.Lhedgehog_begin" : "=s"(pc), "=s"(code_len));
  if (w < 4) {
    __builtin_amdgcn_raw_ptr_buffer_load_lds(lw & 1 ? rw : rx, (LAS char*)smem + L_DUMMY, 4, vp, sp, 0, 0);
    stage(0, 0, 0);
    SB;
    STAMP(25);
    const int m6 = (idx * 43) >> 8;
    const int turn = TB ? (cl == 3 ? rl >> 2 : 2 + (idx - 6 * m6 + xcd) % 6) : (rl ^ xcd) & 7, u = (TB ? (cl == 3 ? rl & 3 : m6) : cl) * 4 + lw, l3 = lane / 3;
    const unsigned px = lane < 16 ? lane * 2048 + ((xcd & 1) * 1024 + ((xcd >> 1) * 16 + u) * 16) * 2048 : OOB;
    unsigned pa_ = lane * 128 + l3 * (2048 - 384) + ((xcd & 1) * 96 + u * 6) * 2048, pb_ = lane * 2048 + (u * 12 - 18) * 2048 + 384;
    asm volatile("" : "+v"(pa_), "+v"(pb_));
    const unsigned pq = (lane < 18 ? pa_ : lane < 30 ? pb_ : OOB) + (xcd >> 1) * 512;
    const int uc = idx * 4 + lw;
    const char* sc = (const char*)(pc & ~127ull) + (uc * 256 < code_len ? uc : 0) * 256;
    unsigned o0 = cb * 16 + rb, o1 = rb * 16 + cb, o2 = W_H1 + lw * 65536;
    asm volatile("" : "+s"(o0), "+s"(o1), "+s"(o2));
    const char* s1 = (lw == 3 ? (const char*)wphi : ws) + (lw == 3 ? b * 256 : o2 + (lw == 0 ? o0 : o1) * 256);
    unsigned o3 = W_P + o0 * 8192, o4 = (lw == 1 ? rb : orb) * 262144 + cb * 2048, o5 = o4 * OS;
    asm volatile("" : "+s"(o3), "+s"(o4), "+s"(o5));
    const char* s2 = (lw == 3 ? (const char*)out : ws) + (lw == 0 ? o3 : lw == 3 ? o4 : o5);
    auto pull = [&](int s) {
      __builtin_amdgcn_raw_ptr_buffer_load_lds(rx, (LAS char*)smem + L_DUMMY, 4, px, s * 128, 0, 0);
      __builtin_amdgcn_raw_ptr_buffer_load_lds(rw, (LAS char*)smem + L_DUMMY, 4, pq, s * 64 * 6144, 0, 0);
    };
#define TOUCH(p) asm volatile("s_load_dwordx2 s[100:101], %0, 0x3c\n\ts_load_dwordx2 s[100:101], %0, 0xbc" :: "s"(p) : "s100", "s101")
    TOUCH(sc); TOUCH(s1); TOUCH(s2); epoch_ask(ws);
    sc = (const char*)wo + (b * 4 + lw) * 2048 - 512;
    unsigned turns = PM ? 1u << (4 + 3 * (turn >> 1) + (turn & 1)) : (0x101u << turn) & (0xffffu << IPN), ox = 128, ow = 64 * 6144, so = SLOT;
    asm volatile("" : "+s"(turns));
    turns >>= VPF - 1;
    unsigned tch = BC ? 0x3ffc : 0x3fc, d2 = 0xbc, dn = 256;
#pragma unroll 1
    for (int n = 1; n < 16; n++) {
      if (__builtin_expect(turns & 1, 0)) pull(n + VPF - 2);
      stage(ox, ow, so);
      ox += 128; ow += 64 * 6144; so = so == 2 * SLOT ? 0 : so + SLOT;
      asm volatile("s_bitcmp0_b32 %2, 0\n\ts_cbranch_scc1 1f\n\ts_load_dwordx2 s[100:101], %1, 0x3c\n\ts_load_dwordx2 s[100:101], %1, %6\n1:\n\t"
                   "s_bitcmp1_b32 %0, 0\n\ts_waitcnt vmcnt(12)\n\ts_cbranch_scc1 2f\n\ts_waitcnt vmcnt(10)\n2:\n\ts_barrier"
                   :: "s"(turns), "s"(sc), "s"(tch), "s"(ox), "s"(ow), "s"(so), "s"(d2) : "s100", "s101", "scc", "memory");
      turns >>= 1; tch >>= 1; sc += dn;
      if (BC && n == 10) { sc = (const char*)wo + uc * 16384 + (xcd & 1) * 1024 + 384; d2 = 0x83c; dn = 4096; }
    }
    const rsrc_t rp = __builtin_amdgcn_make_buffer_rsrc((void*)wphi, 0, 32 * 1024 * 2, 0x00020000);
    __builtin_amdgcn_raw_ptr_buffer_load_lds(rp, (LAS char*)smem + L_PHI + lw * 1024, 16, lane * 16, cb * 4096 + lw * 1024, 0, 0);
    asm volatile("" ::: "memory"); SB;
    const u32x4 e4 = {EMPTY, EMPTY, EMPTY, EMPTY};
#pragma unroll
    for (int i = 0; i < 2; i++) __builtin_amdgcn_raw_buffer_store_b128(e4, rws, W_P + (cb * 16 + rb) * 8192 + lw * 2048 + i * 1024 + lane * 16, 0, SC1);
    __builtin_amdgcn_raw_buffer_store_b128(e4, rws, (cb * 16 + rb) * 512 + lw * 128 + (lane < 8 ? W_PZ + c8 * 16 : lane < 16 ? W_R + c8 * 16 : OOB), 0, SC1);
#pragma unroll
    for (int i = 0; i < 2; i++)
      __builtin_amdgcn_raw_buffer_store_b128(e4, rws, W_O + (((rb * 8 + lw * 2 + i) * 32 + HPOS(cb * 2)) * 1024 + g4 * 256) * OS + l15 * 16, 0, SC1);
    SB;
    wo_load(0, WN0);
    waitv<5 + WN0>(); bar0();
  }
  unsigned T, t3 = 0, fa = OOB;
  const int rg = lw, kh = w >> 2;
  unsigned ga[4], v0, v1, xw, xr, og, tw, tr, fr;
  auto phase2_setup = [&]() {
    const int kr = 16 * (g4 >> 1) + 4 * (g4 & 1) + q4;
#pragma unroll
    for (int nt = 0; nt < 4; nt++) ga[nt] = sb + L_WO + kh * 65536 + kr * 128 + ((nt ^ ((kr >> 1) & 3)) << 5) + p4 * 8;
    v0 = l15 * 16 + (g4 * 256 + kh * 32768) * OS; v1 = l15 * 16 + (g4 * 256 + (kh ^ 1) * 32768) * OS;
    xw = sb + L_FX + w * 4096 + lane * 16; xr = sb + L_FX + (w ^ 4) * 4096 + lane * 16;
    og = (orb * 128 + rg * 32 + kh * 16 + r8) * 2048 + ocb * 128 + c8 * 16;
    tw = sb + L_WO + w * 2048 + l15 * 128 + (((g4 >> 1) ^ (l15 & 7)) << 4) + (((g4 & 1) ^ (l15 >> 3)) << 3);
    tr = sb + L_WO + w * 2048 + r8 * 128 + ((c8 ^ r8) << 4);
    fr = lane < 8 ? W_F + (orb * 16 + kh * 8 + lane) * 256 + (rg >> 1) * 8 : OOB;
    asm volatile("" : "+v"(ga[0]), "+v"(ga[1]), "+v"(ga[2]), "+v"(ga[3]), "+v"(v0), "+v"(v1), "+v"(xw), "+v"(xr), "+v"(og), "+v"(tw), "+v"(tr), "+v"(fr));
  };
  int role = w; asm volatile("" : "+s"(role));

  if (role < 4) {
    T = epoch_tag();
    if (tid == 0) *LP(u32x2, sb + L_CNT) = u32x2{0, T};
    unsigned tya, tx;
    STAMP(12);
    wo_load(WN0, WNA);
    unsigned touch = __builtin_amdgcn_raw_buffer_load_b32(lw & 1 ? rout : rws, lane == 0 ? 0 : OOB, (orb * 262144 + ocb * 128) * (lw & 1 ? 1 : OS), SC1);
    unsigned ha = lane < 16 && lane != rb ? W_H1 + (cb * 16 + lane) * 256 : OOB;
    u32x2 h1;
    {
      const int srow = 4 * g4 + q4;
      const int sw = ((srow >> 1) & 1) | ((srow >> 2) & 2) | (srow & 4);
      unsigned ia[2], va = sb + L_V + (lw >> 1) * 4096 + lane * 16;
#pragma unroll
      for (int ft = 0; ft < 2; ft++) ia[ft] = sb + (1 + (lw >> 1)) * SLOT + 24576 + (lw & 1) * 8192 + srow * 64 + (((4 * ft + p4) ^ sw) << 3);
      unsigned pa = W_P + (cb * 16 + rb) * 8192 + lw * 2048 + g4 * 512 + l15 * 16;
      unsigned za = l15 < 2 ? W_PZ + (cb * 16 + rb) * 512 + lw * 128 + (l15 * 4 + g4) * 16 : OOB;
      u32x4 one4 = {0x3f803f80u, 0x3f803f80u, 0x3f803f80u, 0x3f803f80u};
      f32x4 kv[2][2] = {}, z[2] = {};
      asm volatile("" : "+v"(ia[0]), "+v"(ia[1]), "+v"(va), "+v"(pa), "+v"(za), "+v"(one4), "+v"(ha));
      __builtin_amdgcn_s_waitcnt(0x0f70 | ((WNA - WN0 + 1) & 15) | (((WNA - WN0 + 1) >> 4) << 14));
      STAMP(1);
      bar();
      tya = now32();
      __builtin_amdgcn_raw_buffer_store_b64(u32x2{tya, T}, rws, tid == 0 ? W_H1 + (cb * 16 + rb) * 256 : OOB, 0, SC1);
      SB;
      if (WE) wo_load(WNA, WN1);
      bf16x8 vf[2][2][2];
#pragma unroll
      for (int i = 0; i < 8; i++) vf[i >> 2][(i >> 1) & 1][i & 1] = lds_b128(va + (i >> 2) * 8192 + (i & 3) * 1024);
#pragma unroll
      for (int rt = 0; rt < 2; rt++) {
        if (rt) {
          asm volatile("" : "+v"(kv[0][0]), "+v"(kv[0][1]), "+v"(kv[1][0]), "+v"(kv[1][1]), "+v"(z[0]), "+v"(z[1]));
          if (!WE) wo_load(WNA, WN1);
          __builtin_amdgcn_s_setprio(3);
          STAMP(13);
          bar();
          h1 = __builtin_amdgcn_raw_buffer_load_b64(rws, ha, 0, SC1);
        }
        bf16x8 pf[2][2];
#pragma unroll
        for (int i = 0; i < 4; i++) { const unsigned a = ia[i & 1] + (i >> 1) * 4096 + rt * 2048; pf[i >> 1][i & 1] = lds_tr2(a, a + 1024); }
        if (rt) count_in(sb + L_CNT, 0x100);
        SB;
#pragma unroll
        for (int h = 0; h < 2; h++)
#pragma unroll
          for (int et = 0; et < 2; et++)
#pragma unroll
            for (int ft = 0; ft < 2; ft++) kv[ft][et] = MFMA16(pf[h][ft], vf[h][rt][et], kv[ft][et]);
        SB;
#pragma unroll
        for (int h = 0; h < 2; h++)
#pragma unroll
          for (int ft = 0; ft < 2; ft++) z[ft] = MFMA16(pf[h][ft], BF8(one4), z[ft]);
        SB;
      }
#pragma unroll
      for (int et = 0; et < 2; et++)
        __builtin_amdgcn_raw_buffer_store_b128(seal4(u32x4{pk2(kv[0][et][0], kv[0][et][1]), pk2(kv[0][et][2], kv[0][et][3]),
                                                           pk2(kv[1][et][0], kv[1][et][1]), pk2(kv[1][et][2], kv[1][et][3])}), rws, pa + et * 256, 0, SC1);
      u32x4 zs;
#pragma unroll
      for (int j = 0; j < 4; j++) zs[j] = seal(__float_as_uint((lane & 1) ? z[1][j] : z[0][j]));
      __builtin_amdgcn_raw_buffer_store_b128(zs, rws, za, 0, SC1);
      SB;
    }
    STAMP(6);
    __builtin_amdgcn_s_setprio(HP2);
    const unsigned da = sb + L_WO + lw * 32768 + r8 * 128 + ((c8 ^ (r8 & 6)) << 4);
    auto wo_image = [&](int i0, int i1) {
#pragma unroll
      for (int i = i0; i < i1; i++) *LP(u32x4, da + i * 1024) = wr_[i];
      SB;
    };
    const unsigned t1 = tya + max(latest(h1, rws, ha, tya, T), 0) + D1;
    {
      u32x4 g[2], gz[2];
      const unsigned pa = W_P + (cb * 16 + r8) * 8192 + rb * 512 + lw * 128 + c8 * 16;
      const unsigned za = W_PZ + (cb * 16 + r8) * 512 + lw * 128 + c8 * 16;
      const unsigned ra = (lane & 8) ? OOB : W_R + cb * 8192 + rb * 512 + lw * 128 + c8 * 16 + g4 * 4;
      wait_until(t1);
      STAMP(16);
#pragma unroll
      for (int i = 0; i < 2; i++) g[i] = __builtin_amdgcn_raw_buffer_load_b128(rws, pa + i * 65536, 0, SC1);
#pragma unroll
      for (int i = 0; i < 2; i++) gz[i] = __builtin_amdgcn_raw_buffer_load_b128(rws, za + i * 4096, 0, SC1);
      SB;
      wo_load(WN1, WN2);
      lds_wait(sb + L_CNT, 0x800, 0xff00);
      wo_image(0, WI1);
      RETRY(31) += settle<2>(g, rws, pa, 65536) ? 1 : 0;
      STAMP(7);
      float s[8];
#pragma unroll
      for (int j = 0; j < 4; j++) {
        s[2 * j] = __uint_as_float(g[0][j] << 16) + __uint_as_float(g[1][j] << 16);
        s[2 * j + 1] = __uint_as_float(g[0][j] & 0xffff0000u) + __uint_as_float(g[1][j] & 0xffff0000u);
      }
      const float a0 = fold16(fold32(s[0], s[4]), fold32(s[2], s[6])), a1 = fold16(fold32(s[1], s[5]), fold32(s[3], s[7]));
      __builtin_amdgcn_raw_buffer_store_b32(seal(pk2(fold8(a0), fold8(a1))), rws, ra, 0, SC1);
      unsigned t2 = now32() + D2;
      if ((int)(t1 + D2B - t2) > 0) t2 = t1 + D2B;
      SB;
      STAMP(2);
      settle<2>(gz, rws, za, 4096);
#pragma unroll
      for (int j = 0; j < 4; j++) s[j] = __uint_as_float(gz[0][j]) + __uint_as_float(gz[1][j]);
      float z0 = fold32(s[0], s[2]), z1 = fold32(s[1], s[3]);
      z0 = fold8(fold16(z0, z0)); z1 = fold8(fold16(z1, z1));
      if ((lane & 24) == 0) *LP(unsigned, sb + L_Z + lw * 64 + (c8 & 1) * 32 + ((c8 >> 1) & 1) * 16 + (c8 >> 2) * 8 + hh * 4) = pk2(z0, z1);
      count_in(sb + L_CNT, 0x10000);
      STAMP(3);
      const unsigned ka = W_R + cb * 8192 + lw * 2048 + lane * 16;
      wait_until(t2);
#pragma unroll
      for (int i = 0; i < 2; i++) g[i] = __builtin_amdgcn_raw_buffer_load_b128(rws, ka + i * 1024, 0, SC1);
      SB;
      wo_load(WN2, 32);
      wo_image(WI1, WI2);
      RETRY(31) += settle<2>(g, rws, ka, 1024) ? 10 : 0;
#pragma unroll
      for (int i = 0; i < 2; i++) *LP(u32x4, sb + L_KV + (lw * 2 + i) * 1024 + lane * 16) = g[i];
    }
    unsigned hs = tid == 0 ? W_H2 + (rb * 16 + cb) * 256 : OOB;
    ha = tid < 16 ? W_H2 + (orb * 16 + lane) * 256 : OOB;
    asm volatile("" : "+v"(hs), "+v"(ha));
    STAMP(4);
    bar();
    tx = now32();
    __builtin_amdgcn_raw_buffer_store_b64(u32x2{tx, T}, rws, hs, 0, SC1);
    __builtin_amdgcn_s_setprio(0);
    phase2_setup();
    SB;
    wo_image(WI2, WI3);
    h1 = __builtin_amdgcn_raw_buffer_load_b64(rws, ha, 0, SC1);
    SB;
    wo_image(WI3, 32);
    asm volatile("" :: "v"(touch));
    STAMP(5);
    asm volatile("" : "+v"(h1));
    if (w == 0) {
      t3 = tx + latest(h1, rws, ha, tx, T) + D3;
      if (b == 0) __builtin_amdgcn_raw_buffer_store_b32(T, rws, lane == 0 ? W_E : OOB, 0, SC1);
      STAMP(14);
    }
  } else {
    const int wr = lw >> 1, wc = lw & 1;
    __builtin_amdgcn_s_waitcnt(0x0f70);
    f32x4 acc[6][4] = {};
    unsigned xa[2], wa[2];
#pragma unroll
    for (int ks = 0; ks < 2; ks++) xa[ks] = sb + (wr * 64 + l15) * 128 + (((4 * ks + g4) ^ (l15 >> 1)) << 4);
#pragma unroll
    for (int nt = 0; nt < 2; nt++) wa[nt] = sb + 16384 + (8 * g4 + q4) * 128 + (((wc * 2 + nt) ^ ((q4 >> 1) | ((g4 & 1) << 1))) << 5) + p4 * 8;
    bf16x8 fx[4][4], fw[4][6];
    unsigned xs[3][2], wsl[3][2];
#pragma unroll
    for (int sl = 0; sl < 3; sl++) {
      xs[sl][0] = xa[0] + sl * SLOT; xs[sl][1] = xa[1] + sl * SLOT; wsl[sl][0] = wa[0] + sl * SLOT; wsl[sl][1] = wa[1] + sl * SLOT;
      asm volatile("" : "+v"(xs[sl][0]), "+v"(xs[sl][1]), "+v"(wsl[sl][0]), "+v"(wsl[sl][1]));
    }
    auto rdw = [&](int buf, int j, int ks, int sl) {
      const unsigned a = wsl[sl][j & 1] + (j < 4 ? 8192 + (j >> 1) * 8192 : 0) + ks * 4096;
      fw[buf][j] = lds_tr2(a, a + 512);
    };
    auto rd = [&](int buf, int ks, int sl, int j0, int j1) {
      rdw(buf, j0, ks, sl);
#pragma unroll
      for (int m = 0; m < 4; m++) fx[buf][m] = lds_b128(xs[sl][ks] + m * 2048);
#pragma unroll
      for (int j = j0 + 1; j < j1; j++) rdw(buf, j, ks, sl);
    };
    auto mm = [&](int buf, int j0, int j1) {
#pragma unroll
      for (int j = j0; j < j1; j++)
#pragma unroll
        for (int m0 = 0; m0 < 4; m0++) {
          const int m = (j & 1) ? 3 - m0 : m0;
          acc[j][m] = (j == 2 || j == 3) ? MFMA16(fx[buf][m], fw[buf][j], acc[j][m]) : MFMA16(fw[buf][j], fx[buf][m], acc[j][m]);
        }
    };
#define SCHED(nds, nmf) do { _Pragma("unroll") for (int i_ = 0; i_ < (nds); i_++) { __builtin_amdgcn_sched_group_barrier(0x8, 1, 0); __builtin_amdgcn_sched_group_barrier(0x100, 1, 0); } \
      if ((nmf) > (nds)) __builtin_amdgcn_sched_group_barrier(0x8, (nmf) - (nds), 0); SB; } while (0)
#pragma unroll
    for (int j = 0; j < 6; j++) asm volatile("" : "+v"(acc[j][0]), "+v"(acc[j][1]), "+v"(acc[j][2]), "+v"(acc[j][3]));
    unsigned wpa = sb + L_PHI + wc * 2048 + (8 * hh + q4) * 64 + (g4 & 1) * 32 + p4 * 8, vwa = sb + L_V + lw * 4096 + lane * 16;
    unsigned pia[4];
    {
      const int sw = ((l31 >> 1) & 1) | ((l31 >> 2) & 2) | (l31 & 4);
#pragma unroll
      for (int q = 0; q < 4; q++) pia[q] = sb + (1 + wc) * SLOT + 24576 + (wr * 64 + l31) * 64 + (((2 * q + hh) ^ sw) << 3);
      asm volatile("" : "+v"(pia[0]), "+v"(pia[1]), "+v"(pia[2]), "+v"(pia[3]), "+v"(wpa), "+v"(vwa));
    }
    bar();
    STAMP(1);
    rd(0, 0, 0, 0, 6);
    SB;
#pragma unroll 1
    for (int t = 0; t < 12; t += 3) {
#pragma unroll
      for (int u = 0; u < 3; u++) {
        if (WL) { waitl(); SB; }
        rd(1, 1, u, 0, 6); mm(0, 0, 6); SCHED(16, 24);
        bar();
        rd(0, 0, (u + 1) % 3, 0, 6); mm(1, 0, 6); SCHED(16, 24);
      }
    }
    rd(1, 1, 0, 0, 6); mm(0, 0, 6); SCHED(16, 24);
    bar();
    rd(0, 0, 1, 0, 4); mm(1, 0, 6); SCHED(12, 24);
    rd(1, 1, 1, 0, 4); mm(0, 0, 4); SCHED(12, 16);
    bar();
    rd(0, 0, 2, 0, 4); mm(1, 0, 4); SCHED(12, 16);
    rd(1, 1, 2, 0, 4); mm(0, 0, 4); SCHED(12, 16);
    bar();
    __builtin_amdgcn_s_setprio(CPR);
    rd(0, 0, 0, 0, 4); mm(1, 0, 4); SCHED(12, 16);
    bf16x8 wp[2];
    rd(1, 1, 0, 0, 4);
    wp[0] = lds_tr2(wpa, wpa + 256); wp[1] = lds_tr2(wpa + 1024, wpa + 1280);
    mm(0, 0, 4); SCHED(16, 16);
    STAMP(2);
    const f32x16 zero = {};
    auto project = [&](int j0, f32x16 (&p)[2], auto fill0, auto fill1) {
#pragma unroll
      for (int i = 0; i < 8; i++) { const int j = j0 + ((i >> 1) & 1), m = (i >> 2) * 2 + (i & 1); acc[j][m] = MFMA16(fw[1][j], fx[1][m], acc[j][m]); SB; }
      fill0();
      const bf16x8 a0 = pack16to32(acc[j0][0], acc[j0][1]), a1 = pack16to32(acc[j0 + 1][0], acc[j0 + 1][1]);
#pragma unroll
      for (int i = 0; i < 8; i++) { __builtin_amdgcn_sched_group_barrier(0x8, 1, 0); __builtin_amdgcn_sched_group_barrier(0x2, 2, 0); }
      SB;
      p[0] = MFMA32(wp[0], a0, zero); SB;
      const bf16x8 b0 = pack16to32(acc[j0][2], acc[j0][3]); SB;
      p[0] = MFMA32(wp[1], a1, p[0]); SB;
      const bf16x8 b1 = pack16to32(acc[j0 + 1][2], acc[j0 + 1][3]); SB;
      p[1] = MFMA32(wp[0], b0, zero); SB;
      fill1(0); SB;
      p[1] = MFMA32(wp[1], b1, p[1]); SB;
      fill1(1); SB;
    };
    f32x16 pk[2];
    project(0, pk, [&]() { mm(1, 2, 4); }, [&](int rt) {
#pragma unroll
      for (int et = 0; et < 2; et++) {
        const f32x4 a = acc[2 + et][2 * rt], c = acc[2 + et][2 * rt + 1];
        *LP(u32x4, vwa + (rt * 2 + et) * 1024) = u32x4{pk2(a[0], a[1]), pk2(a[2], a[3]), pk2(c[0], c[1]), pk2(c[2], c[3])};
      }
    });
    STAMP(20);
#pragma unroll
    for (int rt = 0; rt < 2; rt++) {
      unsigned ph[2][8];
      dual_softmax<false>(pk[rt], ph[0], ph[1],
        [&](float& mx, float& mn) { if (rt) { asm volatile("" : "+v"(mx), "+v"(mn)); STAMP(21); bar(); STAMP(22);
          rdw(0, 4, 0, 0); rdw(0, 5, 0, 0); rdw(1, 4, 1, 0); rdw(1, 5, 1, 0); rd(2, 0, 1, 4, 6); SB;
          asm volatile("" : "+v"(mx), "+v"(mn)); } },
        [&](int q) {
          *LP(u32x2, pia[q] + rt * 2048) = u32x2{ph[0][2 * q], ph[0][2 * q + 1]};
          *LP(u32x2, pia[q] + rt * 2048 + 8192) = u32x2{ph[1][2 * q], ph[1][2 * q + 1]};
          SB;
        });
    }
    STAMP(3);
    bar();
    STAMP(4);
    SB;
    rd(3, 1, 1, 4, 6); mm(0, 4, 6); SCHED(8, 8);
    rd(0, 0, 2, 4, 6); mm(1, 4, 6); SCHED(8, 8);
    rd(1, 1, 2, 4, 6); mm(2, 4, 6); SCHED(8, 8);
    count_in(sb + L_CNT, 0x100);
    mm(3, 4, 6); SB;
    mm(0, 4, 6); SB;
    STAMP(23);
    project(4, pk, []() {}, [](int) {});
    STAMP(24);
    u32x4 phq[2][4], zf[4];
    u32x2 zc;
    float eps[2];
    unsigned ka = sb + L_KV + wc * 4096 + lane * 16, zl = sb + L_Z + wc * 128 + hh * 32;
    auto zread = [&]() {
      zc = *LP(const volatile u32x2, sb + L_CNT);
#pragma unroll
      for (int i = 0; i < 4; i++) zf[i] = *LP(const volatile u32x4, zl + (i >> 1) * 64 + (i & 1) * 16);
    };
#pragma unroll
    for (int rt = 0; rt < 2; rt++) {
      unsigned ph[2][8];
      eps[rt] = 1e-6f * dual_softmax<true>(pk[rt], ph[0], ph[1], [](float&, float&) {}, [&](int q) { if (rt && q == 0) { SB; zread(); SB; } });
#pragma unroll
      for (int i = 0; i < 4; i++) { const int sg = i >> 1, st = i & 1; phq[rt][i] = u32x4{ph[sg][2 * st], ph[sg][2 * st + 1], ph[sg][4 + 2 * st], ph[sg][5 + 2 * st]}; }
      asm volatile("" : "+v"(phq[rt][0]), "+v"(phq[rt][1]), "+v"(phq[rt][2]), "+v"(phq[rt][3]));
    }
    unsigned oa = W_O + (((rb * 8 + wr * 4 + (g4 & 1)) * 32 + HPOS(cb * 2 + wc)) * 1024 + hh * 256) * OS + l15 * 16, oa1 = oa + 65536 * OS;
    fa = lane == 0 ? W_F + (rb * 16 + cb) * 256 + lw * 4 : OOB;
    asm volatile("" : "+v"(ka), "+v"(oa), "+v"(oa1), "+v"(fa));
    STAMP(5);
    if (__builtin_amdgcn_readfirstlane(zc[0]) < 0x40000) { lds_wait(sb + L_CNT, 0x40000); zread(); }
    T = __builtin_amdgcn_readfirstlane(zc[1]);
    float rden[2];
    {
      float dn[2][2] = {};
#pragma unroll
      for (int i = 0; i < 4; i++)
#pragma unroll
        for (int j = 0; j < 4; j++)
#pragma unroll
          for (int rt = 0; rt < 2; rt++) dn[rt][j & 1] = dot2(phq[rt][i][j], zf[i][j], dn[rt][j & 1]);
#pragma unroll
      for (int rt = 0; rt < 2; rt++) { const float d = dn[rt][0] + dn[rt][1]; rden[rt] = __builtin_amdgcn_rcpf(fold32(d, d) + eps[rt]); }
      asm volatile("" : "+v"(rden[0]), "+v"(rden[1]));
    }
    STAMP(16);
    __builtin_amdgcn_s_setprio(2);
    bar();
    STAMP(6);
    {
      bf16x8 kf[4];
#pragma unroll
      for (int i = 0; i < 4; i++) kf[i] = lds_b128(ka + i * 1024);
      auto put = [&](const f32x16& n, float r, int st, unsigned a) {
        const f32x2 r2 = {r, r};
        u32x4 v;
#pragma unroll
        for (int j = 0; j < 4; j++) v[j] = pk2v(f32x2{n[8 * st + 2 * j], n[8 * st + 2 * j + 1]} * r2);
        __builtin_amdgcn_raw_buffer_store_b128(v, rws, a + st * 512 * OS, 0, SC1);
      };
      SB;
      f32x16 n0 = zero, n1 = zero;
#pragma unroll
      for (int i = 0; i < 4; i++) { n0 = MFMA32(kf[i], BF8(phq[0][i]), n0); SB; }
#pragma unroll
      for (int i = 0; i < 3; i++) { n1 = MFMA32(kf[i], BF8(phq[1][i]), n1); SB; }
      put(n0, rden[0], 0, oa); SB;
      n1 = MFMA32(kf[3], BF8(phq[1][3]), n1); SB;
      put(n0, rden[0], 1, oa); SB;
      put(n1, rden[1], 0, oa1); SB; put(n1, rden[1], 1, oa1); SB;
    }
    STAMP(7);
    phase2_setup();
    waitv<0>();
    __builtin_amdgcn_raw_buffer_store_b32(T, rws, fa, 0, SC1);
    __builtin_amdgcn_s_setprio(GP ? 1 : 0);
  }
  {
    const unsigned ob = W_O + ((orb * 8 + rg * 2) * 32 + kh * 16) * 1024 * OS;
    constexpr int LAG = OLAG, PF = OPF;
    f32x4 c0[4], c1[4], px[4];
    const f32x4 z4 = {};
    constexpr int RN = OPF2 + 1;
    u32x4 o0[RN], o1[RN];
    bf16x8 a[LAG + 2][4];
    auto head = [&](int i) { return 2 * (i & 7) + (i >> 3); };
    auto ldo = [&](int i) {
      if (i < 16) o1[i % RN] = __builtin_amdgcn_raw_buffer_load_b128(rws, v1, ob + HPOS(head(i)) * 1024 * OS, SC1);
      if (i >= LAG && i < 16 + LAG) o0[i % RN] = __builtin_amdgcn_raw_buffer_load_b128(rws, v0, ob + HPOS(head(i - LAG)) * 1024 * OS, SC1);
    };
    auto ldw = [&](int i) {
#pragma unroll
      for (int nt = 0; nt < 4; nt++) { const unsigned p = ga[nt] + head(i) * 4096; a[i % (LAG + 2)][nt] = lds_tr2(p, p + 1024); }
    };
    auto step = [&](int i) {
      constexpr int E = OPF2 - OPF;
      const int e0 = i <= 8 - PF ? 0 : i - (8 - PF) < E ? i - (8 - PF) : E, e1 = i < 8 - PF ? 0 : i + 1 - (8 - PF) < E ? i + 1 - (8 - PF) : E;
      if (i) ldo(i + PF + e0);
      if (e1 > e0) ldo(i + PF + e1);
      if (GP && i == GP) __builtin_amdgcn_s_setprio(0);
      SB;
      if (i < 15) ldw(i + 1);
      if (i < 16) {
#pragma unroll
        for (int nt = 0; nt < 4; nt++) c1[nt] = MFMA16(a[i % (LAG + 2)][nt], BF8(o1[i % RN]), i ? c1[nt] : z4);
      }
      if (i >= LAG) {
#pragma unroll
        for (int nt = 0; nt < 4; nt++) c0[nt] = MFMA16(a[(i - LAG) % (LAG + 2)][nt], BF8(o0[i % RN]), i > LAG ? c0[nt] : z4);
      }
#pragma unroll
      for (int q = 0; q < 8; q++) { __builtin_amdgcn_sched_group_barrier(0x8, 1, 0); __builtin_amdgcn_sched_group_barrier(0x100, 1, 0); }
      SB;
    };
    if (w == 0) wait_until(t3);
    bar();
    STAMP(9);
#pragma unroll 1
    for (int pass = 0;; pass++) {
#pragma unroll
      for (int i = 0; i <= PF; i++) { ldo(i); SB; }
      ldw(0);
      SB;
      u32x2 fl;
#pragma unroll
      for (int i = 0; i < 9; i++) {
        if (i == FSTEP) { fl = __builtin_amdgcn_raw_buffer_load_b64(rws, fr, 0, SC1); SB; }
        if (i == 8 - PF) {
          STAMP(15);
          if (__builtin_expect(!__all(fr == OOB || (fl[0] == T && fl[1] == T)), 0)) {
            RETRY(31) = 100 + RETRY(31) % 100;
#pragma unroll 1
            for (int sp = 0; sp < SPIN_CAP; sp++) {
              if (fl[0] == T && fl[1] == T) fr = OOB;
              if (__all(fr == OOB)) break;
              __builtin_amdgcn_s_sleep(GSL);
              asm volatile("" ::: "memory");
              fl = __builtin_amdgcn_raw_buffer_load_b64(rws, fr, 0, SC1);
            }
          }
          asm volatile("" ::: "memory");
        }
        step(i);
      }
      if (__builtin_expect(pass || !__any(c0[0][0] != c0[0][0] || c1[0][0] != c1[0][0]), 1)) break;
      if (w >= 4) RETRY(31) |= 1;
      asm volatile("" ::: "memory");
    }
#pragma unroll
    for (int i = 9; i < 16 + LAG; i++) {
      step(i);
      if (i == 15) {
        STAMP(10);
#pragma unroll
        for (int nt = 0; nt < 4; nt++) *LP(f32x4, xw + nt * 1024) = c1[nt];
      }
      if (i == (LAG ? 14 + LAG : 15)) {
        bar();
#pragma unroll
        for (int nt = 0; nt < 4; nt++) px[nt] = *LP(const f32x4, xr + nt * 1024);
        SB;
      }
    }
#pragma unroll
    for (int nt = 0; nt < 4; nt++) {
      const f32x4 v = px[nt] + c0[nt];
      *LP(u32x2, tw ^ (nt << 5)) = u32x2{pk2(v[0], v[1]), pk2(v[2], v[3])};
    }
    asm volatile("" ::: "memory");
    const u32x4 t0 = *LP(const u32x4, tr), t1 = *LP(const u32x4, tr + 1024);
    __builtin_amdgcn_raw_buffer_store_b128(t0, rout, og, 0, OAUX);
    __builtin_amdgcn_raw_buffer_store_b128(u32x4{t1[2], t1[3], t1[0], t1[1]}, rout, og + 16384, 0, OAUX);
    asm volatile(".Lhedgehog_end:");
    STAMP(11);
    STAMP(30);
    STAMP_DUMP();
  }
}

extern "C" void kernel_launch(void* const* d_in, const int* in_sizes, int n_in, void* d_out, int out_size, void* d_ws, size_t ws_size,
                              hipStream_t stream) {
  const size_t ws_off = WSO < 0 ? (ws_size - (size_t)(-WSO) * (1u << 20)) & ~(size_t)((2u << 20) - 1) : (size_t)WSO << 20;
  (void)hipFuncSetAttribute((const void*)hedgehog_fused, hipFuncAttributeMaxDynamicSharedMemorySize, LDS_BYTES);
  hedgehog_fused<<<256, 512, LDS_BYTES, stream>>>((const u16*)d_in[0], (const u16*)d_in[1], (const u16*)d_in[2], (const u16*)d_in[3],
                                                   (u16*)d_out, (char*)d_ws + ws_off);
}
```
